```python
import torch

def _softmax(x, axis=-1):
    return torch.softmax(x, dim=axis)
def _gelu(x):
    return torch.nn.functional.gelu(x, approximate="tanh")
def _silu(x):
    return torch.nn.functional.silu(x)
def _sigmoid(x):
    return torch.sigmoid(x)
def _layernorm(x, axis=-1, eps=1e-5):
    m = x.mean(dim=axis, keepdim=True)
    v = x.var(dim=axis, keepdim=True, unbiased=False)
    return (x - m) / torch.sqrt(v + eps)
def _scatter_add(tgt, idx, src, axis=0):
    return tgt.clone().index_add_(axis, idx, src)
def _segment_sum(src, seg, n):
    out = torch.zeros((n,) + src.shape[1:], dtype=src.dtype)
    return out.index_add_(0, seg, src)
def _pad(x, pw):
    flat = tuple(v for pair in reversed(pw) for v in pair)
    return torch.nn.functional.pad(x, flat)

kernel_name = 'recurrent_scan__softmax_full_mha__glu_gated_geglu__prompt'

def setup_inputs():

    _w = (lambda a: (lambda t: t.to(torch.bfloat16) if t.is_floating_point() else t)(torch.as_tensor(a)))
    def _A_setup():
        B, S, D = 1, 2048, 512
        return {
            "x": _w((lambda *s: torch.randn(*s, dtype=torch.bfloat16) * 0.05)(B, S, D)),
            "wqkv": _w((lambda *s: torch.randn(*s, dtype=torch.bfloat16) * 0.2)(D, 3 * D)),
            "wo": _w((lambda *s: torch.randn(*s, dtype=torch.bfloat16) * 0.2)(D, D)),
        }
    _A_in = _A_setup()

    def _B_setup():
        B, S, Dm, Df = 1, 2048, 512, 1376
        return {
            "x": _w((lambda *s: torch.randn(*s, dtype=torch.bfloat16) * 0.05)(B, S, Dm)),
            "w_gate": _w((lambda *s: torch.randn(*s, dtype=torch.bfloat16) * 0.1)(Dm, Df) * (Dm ** -0.5)),
            "w_up": _w((lambda *s: torch.randn(*s, dtype=torch.bfloat16) * 0.1)(Dm, Df) * (Dm ** -0.5)),
            "w_down": _w((lambda *s: torch.randn(*s, dtype=torch.bfloat16) * 0.1)(Df, Dm) * (Df ** -0.5)),
        }
    _B_in = _B_setup()

    return {
        "A_x": _A_in["x"],
        "A_wqkv": _A_in["wqkv"],
        "A_wo": _A_in["wo"],
        "B_x": _B_in["x"],
        "B_w_gate": _B_in["w_gate"],
        "B_w_up": _B_in["w_up"],
        "B_w_down": _B_in["w_down"],
    }

def reference(A_x, A_wqkv, A_wo, B_x, B_w_gate, B_w_up, B_w_down):
    def _A_ref(x, wqkv, wo):
        B, S, D = x.shape
        H = 32; Dh = D // H
        qkv = (lambda x, *d: x.permute(*d))((x @ wqkv).reshape(B, S, 3, H, Dh), 2, 0, 3, 1, 4)
        q, k, v = qkv[0], qkv[1], qkv[2]
        scale = Dh ** -0.5
        scores = torch.einsum("bhqd,bhkd->bhqk", q, k) * scale
        p = _softmax(scores, axis=-1)
        o = torch.einsum("bhqk,bhkd->bhqd", p, v)
        return (lambda x, *d: x.permute(*d))(o, 0, 2, 1, 3).reshape(B, S, D) @ wo

    def _B_ref(x, w_gate, w_up, w_down):
        g = torch.einsum("bsd,df->bsf", x, w_gate)
        u = torch.einsum("bsd,df->bsf", x, w_up)
        h = _gelu(g) * u
        return torch.einsum("bsf,fd->bsd", h, w_down)

    x = A_x
    xn = _layernorm(x, axis=-1)
    s = _A_ref(xn, A_wqkv, A_wo)
    g = _silu(_B_ref(xn, B_w_gate, B_w_up, B_w_down))
    return x + s * g
```

```cpp
#include <hip/hip_runtime.h>
#include <stdint.h>

typedef __attribute__((ext_vector_type(8))) short bf16x8;
typedef __attribute__((ext_vector_type(4))) short s16x4;
typedef __attribute__((ext_vector_type(2))) __bf16 bf2;
typedef __attribute__((ext_vector_type(16))) float f32x16;
typedef __attribute__((ext_vector_type(4))) float f32x4;
typedef __attribute__((ext_vector_type(2))) float f32x2;
typedef __attribute__((ext_vector_type(4))) unsigned u32x4;
typedef unsigned long long u64;
typedef __attribute__((address_space(3))) char* lds_t;
typedef __attribute__((address_space(1))) u64 gu64;

template <int N> struct IC { static constexpr int value = N; };
template <bool B> struct BC { static constexpr bool value = B; };
#define DI __device__ __forceinline__
#define MFMA(a, b, c) __builtin_amdgcn_mfma_f32_32x32x16_bf16((a), (b), (c), 0, 0, 0)
#define RLX_AGENT __ATOMIC_RELAXED, __HIP_MEMORY_SCOPE_AGENT

constexpr unsigned WS_EPOCH = 0;
constexpr unsigned WS_F1 = 4096;
constexpr unsigned WS_F2 = 8192;
constexpr unsigned WS_F3 = 16384;
constexpr unsigned WS_QF = 65536;
constexpr unsigned WS_KV = WS_QF + (2u << 20);
constexpr unsigned WS_HF = WS_KV + (4u << 20);
constexpr unsigned WS_OF = WS_HF + 64u * 86u * 1024u;
constexpr unsigned WS_END = WS_OF + (2u << 20);
constexpr unsigned SLOT = 28672, XIMG = 0, WIMG = 16384, GIMG = 0, UIMG = 12288 + 32;
constexpr unsigned L_STAT = 153600;
constexpr unsigned L_RSTD = L_STAT + 1024;
constexpr unsigned L_CSQ = L_RSTD + 512;
constexpr unsigned L_CSG = L_CSQ;
constexpr unsigned L_WSL = 131072;
constexpr unsigned LDS_BYTES = 163840;
constexpr float QSCALE = 0.25f * 1.4426950408889634f;
constexpr float M_OFF = 64.0f;

DI unsigned cvtpk(float lo, float hi) { return __builtin_bit_cast(unsigned, __builtin_convertvector(f32x2{lo, hi}, bf2)); }
DI float dot2(unsigned a, unsigned b, float c) { return __builtin_amdgcn_fdot2_f32_bf16(__builtin_bit_cast(bf2, a), __builtin_bit_cast(bf2, b), c, false); }
DI void glds(unsigned voff, const char* sbase, unsigned ldst) {
    asm volatile("s_mov_b32 m0, %2\n\ts_nop 0\n\tglobal_load_lds_dwordx4 %0, %1" ::"v"(voff), "s"(sbase), "s"(ldst) : "memory"); }
DI void glds_dev(unsigned voff, const char* sbase, unsigned ldst) {
    asm volatile("s_mov_b32 m0, %2\n\ts_nop 0\n\tglobal_load_lds_dwordx4 %0, %1 sc1" ::"v"(voff), "s"(sbase), "s"(ldst) : "memory"); }
#define LOAD_DEV(dst, voff, sbase) asm volatile("global_load_dwordx4 %0, %1, %2 sc1" : "=v"(dst) : "v"(voff), "s"(sbase) : "memory")
#define WAITVM(n) asm volatile("s_waitcnt vmcnt(" #n ")" ::: "memory")
#define BAR() asm volatile("s_waitcnt lgkmcnt(0)\n\ts_barrier" ::: "memory")
template <int N> DI void waitvm() { asm volatile("s_waitcnt vmcnt(%0)" ::"i"(N) : "memory"); }
DI bf16x8 lds128(lds_t p) { return *(const __attribute__((address_space(3))) bf16x8*)p; }
DI f32x4 lds128f(lds_t p) { return *(const __attribute__((address_space(3))) f32x4*)p; }
DI s16x4 ldstr(lds_t p) { return __builtin_amdgcn_ds_read_tr16_b64_v4i16((__attribute__((address_space(3))) s16x4*)p); }
DI bf16x8 trfrag(lds_t p, int step) { s16x4 a = ldstr(p), b = ldstr(p + step); return __builtin_shufflevector(a, b, 0, 1, 2, 3, 4, 5, 6, 7); }
DI float swapadd(float v) { auto r = __builtin_amdgcn_permlane32_swap(__float_as_uint(v), __float_as_uint(v), false, false); return __uint_as_float(r[0]) + __uint_as_float(r[1]); }
DI float swapmax(float v) { auto r = __builtin_amdgcn_permlane32_swap(__float_as_uint(v), __float_as_uint(v), false, false); return fmaxf(__uint_as_float(r[0]), __uint_as_float(r[1])); }
DI float frag_sum(bf16x8 f, float acc) { u32x4 u = __builtin_bit_cast(u32x4, f); acc = dot2(u[0], 0x3f803f80u, acc); acc = dot2(u[1], 0x3f803f80u, acc); acc = dot2(u[2], 0x3f803f80u, acc); return dot2(u[3], 0x3f803f80u, acc); }
DI bf16x8 split_a(float v) { unsigned h = cvtpk(v, v); float lo = v - __uint_as_float(h << 16); u32x4 u = {h, cvtpk(lo, lo), 0u, 0u}; return __builtin_bit_cast(bf16x8, u); }
DI bf16x8 split_b(float v) { unsigned h = cvtpk(v, v); float lo = v - __uint_as_float(h << 16); unsigned p = cvtpk(v, lo); u32x4 u = {p, p, 0u, 0u}; return __builtin_bit_cast(bf16x8, u); }

DI void wait_tags(const char* ws, unsigned off, u64 tag) {
    const u64 t0 = __builtin_amdgcn_s_memrealtime();
    for (;;) {
        u64 v = __hip_atomic_load((gu64*)(ws + off), RLX_AGENT);
        if (__all(v == tag)) break;
        __builtin_amdgcn_s_sleep(1);
        if (__builtin_amdgcn_s_memrealtime() - t0 > 2000000ull) break;
    }
    asm volatile("" ::: "memory");
}

#define EX(S, i) "v_exp_f32 v[" #S "+" #i "], v[" #S "+" #i "]\n\t"
#define CV(S, p, i, j) "v_cvt_pk_bf16_f32 v[248+" #p "], v[" #S "+" #i "], v[" #S "+" #j "]\n\t"
#define TILE(SC, SN, VAc, VBp, VAl, KOFF, VOFF0, VOFF1) \
    "v_mfma_f32_32x32x16_bf16 v[" #SN ":" #SN "+15], v[224:227], v[228:231], v[208:223]\n\t" \
    EX(SC, 0) EX(SC, 1) EX(SC, 2) CV(SC, 0, 0, 1) EX(SC, 3) EX(SC, 4) CV(SC, 1, 2, 3) EX(SC, 5) \
    "v_mfma_f32_32x32x16_bf16 v[192:207], v[" #VBp ":" #VBp "+3], v[252:255], v[192:207]\n\t" \
    "ds_read_b128 v[224:227], %[aK] offset:" #KOFF "\n\t" \
    "s_mov_b64 exec, %[msk]\n\t" \
    "ds_read_b128 v[" #VAl ":" #VAl "+3], %[aV] offset:" #VOFF0 "\n\t" \
    "ds_read_b128 v[" #VBp ":" #VBp "+3], %[aV] offset:" #VOFF1 "\n\t" \
    "s_mov_b64 exec, -1\n\t" \
    EX(SC, 6) CV(SC, 2, 4, 5) EX(SC, 7) EX(SC, 8) CV(SC, 3, 6, 7) EX(SC, 9) EX(SC, 10) \
    "v_mfma_f32_32x32x16_bf16 v[192:207], v[" #VAc ":" #VAc "+3], v[248:251], v[192:207]\n\t" \
    CV(SC, 4, 8, 9) EX(SC, 11) EX(SC, 12) CV(SC, 5, 10, 11) EX(SC, 13) EX(SC, 14) CV(SC, 6, 12, 13) EX(SC, 15) \
    "s_waitcnt lgkmcnt(0)\n\t" \
    CV(SC, 7, 14, 15)

extern "C" __global__ void __launch_bounds__(512, 2)
fused_block(const char* __restrict__ gx, const char* __restrict__ gwqkv, const char* __restrict__ gwo, const char* __restrict__ gwg,
            const char* __restrict__ gwu, const char* __restrict__ gwd, char* __restrict__ gout, char* ws) {
    extern __shared__ __attribute__((aligned(16))) char smem[];
    const lds_t lds = (lds_t)smem;
    const int tid = threadIdx.x, lane = tid & 63, r = lane & 31, hh = lane >> 5;
    const int w = __builtin_amdgcn_readfirstlane(tid >> 6), wr = w & 3, g = w >> 2;
    const int b = blockIdx.x, xcd = b & 7, bi = b >> 3;
    u64 tag;
    { const u64 e = __hip_atomic_load((gu64*)(ws + WS_EPOCH), RLX_AGENT);
      tag = (((u64)(unsigned)__builtin_amdgcn_readfirstlane((unsigned)(e >> 32)) << 32) | (unsigned)__builtin_amdgcn_readfirstlane((unsigned)e)) + 0x9E3779B97F4A7C15ull; }
    const __amdgpu_buffer_rsrc_t wsr = __builtin_amdgcn_make_buffer_rsrc(ws, 0, WS_END, 0x00020000);

    const int rb = 4 * (xcd >> 1) + (bi >> 3), cg = 8 * (xcd & 1) + (bi & 7);
    const int nft = cg < 6 ? 6 : 5, ft0 = cg < 6 ? 6 * cg : 36 + 5 * (cg - 6);
    unsigned vx0, vx1, vw0, vw1, vg0, vg1, vg2;
    { int row = 16 * w + (lane >> 3); vx0 = row * 1024 + (((lane & 7) ^ ((row >> 1) & 7)) << 4);
      row += 8; vx1 = row * 1024 + (((lane & 7) ^ ((row >> 1) & 7)) << 4);
      int o = w * 1024 + lane * 16; vw0 = (o / 192) * 3072 + o % 192;
      o = ((w & 3) + 8) * 1024 + lane * 16; vw1 = (o / 192) * 3072 + o % 192;
      const int lim = nft * 32;
      o = w * 1024 + lane * 16; int cb = o % 192; vg0 = (o / 192) * 2752 + (cb >= lim ? cb - 32 : cb);
      o = (w < 4 ? w + 8 : w - 4) * 1024 + lane * 16; cb = o % 192; vg1 = (o / 192) * 2752 + (cb >= lim ? cb - 32 : cb);
      o = (w + 4) * 1024 + lane * 16; cb = o % 192; vg2 = (o / 192) * 2752 + (cb >= lim ? cb - 32 : cb); }
    const char* bx = gx + (size_t)rb * 128 * 1024;
    const char* bw = gwqkv + cg * 192;
    const char* bg0 = gwg + ft0 * 32;
    const char* bg1 = (w < 4 ? gwg : gwu) + ft0 * 32;
    const char* bg2 = gwu + ft0 * 32;
    const unsigned dg1 = w < 4 ? GIMG + (w + 8) * 1024 : UIMG + (w - 4) * 1024;
    auto issue = [&](int c, int gg) {
        const unsigned s = (c % 5) * SLOT;
        if (c < 8) {
            glds(vx0, bx + c * 128, s + XIMG + w * 2048);
            glds(vx1, bx + c * 128, s + XIMG + w * 2048 + 1024);
            glds(vw0, bw + c * (64 * 3072), s + WIMG + w * 1024);
            if (gg == 0) glds(vw1, bw + c * (64 * 3072), s + WIMG + (w + 8) * 1024);
        } else {
            glds(vg0, bg0 + (c - 8) * (64 * 2752), s + GIMG + w * 1024);
            glds(vg1, bg1 + (c - 8) * (64 * 2752), s + dg1);
            glds(vg2, bg2 + (c - 8) * (64 * 2752), s + UIMG + (w + 4) * 1024);
        }
    };
#pragma unroll
    for (int c = 0; c < 5; ++c) issue(c, g);

    const int xrow = 32 * wr + r, blk = (lane >> 4) & 1, tq = (lane & 15) >> 2, tp = lane & 3;
    const unsigned xa = xrow * 128 + ((hh ^ ((xrow >> 1) & 7)) << 4);
    const lds_t wqa = lds + WIMG + (8 * hh + tq) * 192 + blk * 32 + tp * 8 + (g ? 128 : 0);
    const lds_t wga = lds + (blk ? UIMG : GIMG) + (8 * hh + tq) * 192 + tp * 8 + g * 96;
    const int ct0 = 3 * cg + (g ? 2 : 0);

    bf16x8 xf[32];
    f32x16 a0 = {}, a1 = {}, a2 = {};
    float cs0 = 0.f, cs1 = 0.f, cs2 = 0.f, sx = 0.f, sq = 0.f;
    auto qkv_loop = [&](auto gconst, auto vconst) {
        constexpr int G = decltype(gconst)::value; constexpr bool V = decltype(vconst)::value;
#pragma unroll
        for (int i = 0; i < 8; ++i) {
            if (G == 0) { switch (i) { case 0: WAITVM(16); break; case 5: WAITVM(11); break; case 6: WAITVM(10); break; case 7: WAITVM(9); break; default: WAITVM(12); } }
            else { if (i == 0) WAITVM(12); else WAITVM(9); }
            BAR();
            if (i >= 1) issue(i + 4, G);
            const unsigned s = (i % 5) * SLOT;
#pragma unroll
            for (int ks = 0; ks < 4; ++ks) {
                const bf16x8 x = lds128(lds + s + XIMG + (xa ^ (ks << 5)));
                xf[4 * i + ks] = x;
                const bf16x8 w0 = trfrag(wqa + s + ks * 16 * 192, 4 * 192);
                a0 = V ? MFMA(x, w0, a0) : MFMA(w0, x, a0);
                if (G == 0) { const bf16x8 w1 = trfrag(wqa + s + ks * 16 * 192 + 64, 4 * 192); a1 = V ? MFMA(x, w1, a1) : MFMA(w1, x, a1); }
                else { u32x4 u = __builtin_bit_cast(u32x4, x);
#pragma unroll
                    for (int e = 0; e < 4; ++e) { sx = dot2(u[e], 0x3f803f80u, sx); sq = dot2(u[e], u[e], sq); } }
            }
            cs0 = frag_sum(trfrag(wqa + s + wr * 16 * 192, 4 * 192), cs0);
            if (G == 0) cs1 = frag_sum(trfrag(wqa + s + wr * 16 * 192 + 64, 4 * 192), cs1);
            asm volatile("" : "+v"(cs0), "+v"(cs1), "+v"(sx), "+v"(sq));
        }
    };
    if (g == 0) { if (ct0 >= 32) qkv_loop(IC<0>{}, BC<true>{}); else qkv_loop(IC<0>{}, BC<false>{}); }
    else { if (ct0 >= 32) qkv_loop(IC<1>{}, BC<true>{}); else qkv_loop(IC<1>{}, BC<false>{}); }
    *(__attribute__((address_space(3))) float*)(lds + L_CSQ + (g ? 2 : 0) * 1024 + lane * 16 + wr * 4) = cs0;
    if (g == 0) *(__attribute__((address_space(3))) float*)(lds + L_CSQ + 1024 + lane * 16 + wr * 4) = cs1;
    else {
        sx = swapadd(sx); sq = swapadd(sq);
        const float mean = sx * (1.f / 512.f), var = fmaxf(sq * (1.f / 512.f) - mean * mean, 0.f), rs = __builtin_amdgcn_rsqf(var + 1e-5f);
        if (hh == 0) { *(__attribute__((address_space(3))) f32x2*)(lds + L_STAT + xrow * 8) = f32x2{mean, rs};
                       *(__attribute__((address_space(3))) float*)(lds + L_RSTD + xrow * 4) = rs; }
    }
    float rstd; bf16x8 mfrag;
    const int rt = rb * 4 + wr;
#pragma unroll
    for (int i = 8; i < 16; ++i) {
        switch (i) { case 9: WAITVM(11); break; case 10: WAITVM(6); break; case 13: WAITVM(6); break; case 14: WAITVM(3); break; case 15: WAITVM(0); break; default: WAITVM(9); }
        BAR();
        if (i == 8) {
            const f32x2 st = *(const __attribute__((address_space(3))) f32x2*)(lds + L_STAT + xrow * 8);
            rstd = st[1]; mfrag = split_b(-st[0]);
            for (int t = 0; t < 2 - g; ++t) {
                const int ct = ct0 + t, hp = ct & 15, j = g ? 2 : t;
                const f32x4 p = lds128f(lds + L_CSQ + j * 1024 + lane * 16);
                const bf16x8 cf = split_a((p[0] + p[1]) + (p[2] + p[3]));
                u32x4 lo, hi; unsigned o0, o1;
                if (ct >= 32) {
                    a0 = MFMA(mfrag, cf, a0);
#pragma unroll
                    for (int q4 = 0; q4 < 4; ++q4) { const f32x4 rs = lds128f(lds + L_RSTD + (32 * wr + 8 * q4 + 4 * hh) * 4);
#pragma unroll
                        for (int e = 0; e < 4; ++e) a0[4 * q4 + e] *= rs[e]; }
                    o0 = WS_KV + ((2 * hp + blk) * 64 + rt) * 2048 + 1024 + hh * 256 + (lane & 15) * 16; o1 = o0 + 512;
                } else {
                    a0 = MFMA(cf, mfrag, a0);
                    const float sc = ct < 16 ? rstd * QSCALE : rstd;
#pragma unroll
                    for (int e = 0; e < 16; ++e) a0[e] *= sc;
                    o0 = (ct < 16 ? WS_QF + (2 * hp * 64 + rt) * 1024 : WS_KV + (2 * hp * 64 + rt) * 2048) + lane * 16;
                    o1 = o0 + (ct < 16 ? 64 * 1024 : 64 * 2048);
                }
#pragma unroll
                for (int e = 0; e < 4; ++e) { lo[e] = cvtpk(a0[2 * e], a0[2 * e + 1]); hi[e] = cvtpk(a0[8 + 2 * e], a0[9 + 2 * e]); }
                __builtin_amdgcn_raw_buffer_store_b128(lo, wsr, o0, 0, 16);
                __builtin_amdgcn_raw_buffer_store_b128(hi, wsr, o1, 0, 16);
                a0 = a1;
            }
            a0 = f32x16{}; a1 = f32x16{}; cs0 = 0.f; cs1 = 0.f;
            asm volatile("" ::: "memory");
        }
        if (i == 10 && tid == 0) __hip_atomic_store((gu64*)(ws + WS_F1 + (rb * 16 + cg) * 8), tag, RLX_AGENT);
        if (i + 4 < 16) issue(i + 4, 0);
        const unsigned s = (i % 5) * SLOT;
#pragma unroll
        for (int ks = 0; ks < 4; ++ks) {
            const bf16x8 w0 = trfrag(wga + s + ks * 16 * 192, 4 * 192), w1 = trfrag(wga + s + ks * 16 * 192 + 32, 4 * 192), w2 = trfrag(wga + s + ks * 16 * 192 + 64, 4 * 192);
            a0 = MFMA(w0, xf[4 * (i - 8) + ks], a0); a1 = MFMA(w1, xf[4 * (i - 8) + ks], a1); a2 = MFMA(w2, xf[4 * (i - 8) + ks], a2);
        }
        cs0 = frag_sum(trfrag(wga + s + wr * 16 * 192, 4 * 192), cs0); cs1 = frag_sum(trfrag(wga + s + wr * 16 * 192 + 32, 4 * 192), cs1);
        cs2 = frag_sum(trfrag(wga + s + wr * 16 * 192 + 64, 4 * 192), cs2);
        asm volatile("" : "+v"(cs0), "+v"(cs1), "+v"(cs2));
    }
    *(__attribute__((address_space(3))) float*)(lds + L_CSG + (3 * g + 0) * 1024 + lane * 16 + wr * 4) = cs0;
    *(__attribute__((address_space(3))) float*)(lds + L_CSG + (3 * g + 1) * 1024 + lane * 16 + wr * 4) = cs1;
    *(__attribute__((address_space(3))) float*)(lds + L_CSG + (3 * g + 2) * 1024 + lane * 16 + wr * 4) = cs2;

    int lane2 = __builtin_amdgcn_mbcnt_hi(~0u, __builtin_amdgcn_mbcnt_lo(~0u, 0u));
    asm volatile("" : "+v"(lane2));
#define lane lane2
    const int r2 = lane & 31, hh2 = lane >> 5, blk2 = (lane >> 4) & 1, tq2 = (lane & 15) >> 2, tp2 = lane & 3, tid2 = w * 64 + lane;
#define r r2
#define hh hh2
#define blk blk2
#define tq tq2
#define tp tp2
#define tid tid2
    const int head = 4 * xcd + (bi & 3), qb = bi >> 2;
    if (w == 0) {
        const int hp = head >> 1;
        const int frb = lane < 32 ? (lane & 15) : 2 * qb + (lane & 1), fct = lane < 16 ? 16 + hp : lane < 32 ? 32 + hp : hp;
        wait_tags(ws, WS_F1 + (frb * 16 + fct / 3) * 8, tag);
    }
    BAR();
    u32x4 qf;
    const unsigned l16 = lane * 16;
    LOAD_DEV(qf, l16, ws + WS_QF + (head * 64 + qb * 8 + w) * 1024);
    { const char* kv = ws + WS_KV + head * 131072 + w * 2048;
#pragma unroll
      for (int n = 0; n < 8; ++n) { glds_dev(l16, kv + n * 16384, n * 16384 + w * 2048); glds_dev(l16, kv + n * 16384 + 1024, n * 16384 + w * 2048 + 1024); } }
#pragma unroll 1
    for (int t = 0; t < 3; ++t) {
        const f32x4 p = lds128f(lds + L_CSG + (3 * g + t) * 1024 + lane * 16);
        a0 = MFMA(split_a((p[0] + p[1]) + (p[2] + p[3])), mfrag, a0);
        float hv[8];
#pragma unroll
        for (int e = 0; e < 8; ++e) {
            const float gg = a0[e] * rstd, uu = a0[8 + e] * rstd;
            const float z = gg * __builtin_fmaf(gg * gg, -2.0f * 0.7978845608028654f * 0.044715f * 1.4426950408889634f, -2.0f * 0.7978845608028654f * 1.4426950408889634f);
            hv[e] = gg * uu * __builtin_amdgcn_rcpf(1.0f + __builtin_amdgcn_exp2f(z));
        }
        const u32x4 hq = {cvtpk(hv[0], hv[1]), cvtpk(hv[2], hv[3]), cvtpk(hv[4], hv[5]), cvtpk(hv[6], hv[7])};
        if (3 * g + t < nft) __builtin_amdgcn_raw_buffer_store_b128(hq, wsr, WS_HF + (rt * 86 + ft0 + 3 * g + t) * 1024 + l16, 0, 16);
        a0 = a1; a1 = a2;
    }

    const int rt64 = 4 * xcd + (bi >> 3), ct64 = bi & 7;
    const unsigned vws = (lane >> 3) * 1024 + (((lane & 7) ^ (((lane >> 4) & 1) << 2)) << 4);
    const unsigned wsl = L_WSL + w * 4096;
    const lds_t wta = lds + wsl + (4 * hh + tq) * 128 + ((tq >> 1) & 1) * 64 + blk * 32 + tp * 8;
    const lds_t wtb = lds + wsl + (4 * hh + tq) * 128 + (((tq >> 1) & 1) ^ 1) * 64 + blk * 32 + tp * 8;
    const char* bwo = gwo + (size_t)w * 64 * 1024 + ct64 * 128;
    asm volatile("s_waitcnt vmcnt(16)" : "+v"(qf)::"memory");
    BAR();
#pragma unroll
    for (int n = 0; n < 4; ++n) glds(vws, bwo + n * 8192, wsl + n * 1024);
    const bf16x8 Q = __builtin_bit_cast(bf16x8, qf);
    f32x16 NM, SA, SB, O = {};
    { const f32x16 z = {};
      SA = MFMA(lds128(lds + l16), Q, z); SB = MFMA(lds128(lds + 2048 + l16), Q, z);
      float m = fmaxf(SA[0], SB[0]);
#pragma unroll
      for (int e = 1; e < 16; ++e) m = fmaxf(fmaxf(m, SA[e]), SB[e]);
      m = -(swapmax(m) + M_OFF);
#pragma unroll
      for (int e = 0; e < 16; ++e) NM[e] = m; }
    unsigned aK = l16, aV = 1024 + hh * 256 + (lane & 15) * 16;
    const u64 msk = 0x0000ffff0000ffffull;
    u32x4 K, VA0, VB0, VA1, VB1, PA = {}, PB = {};
    { const u32x4 one = {0x3f803f80u, 0x3f803f80u, 0x3f803f80u, 0x3f803f80u};
      const u32x4 va = __builtin_bit_cast(u32x4, lds128(lds + aV)), vb = __builtin_bit_cast(u32x4, lds128(lds + aV + 512));
      VA0 = blk ? one : va; VB0 = blk ? one : vb; VA1 = one; VB1 = one;
      SA = MFMA(lds128(lds + l16), Q, NM);
      K = __builtin_bit_cast(u32x4, lds128(lds + 2048 + l16)); }
    f32x16 d00 = {}, d01 = {}, d10 = {}, d11 = {};
    bf16x8 wo0a, wo0b, wo1a, wo1b, wo2a, wo2b, wo3a, wo3b;
    u32x4 h0, h1;
    const int fs0 = w < 6 ? 11 * w : 66 + 10 * (w - 6), nfs = w < 6 ? 11 : 10;
    const char* bh = ws + WS_HF + (size_t)(2 * rt64 * 86 + fs0) * 1024;
    const char* bwd = gwd + (size_t)fs0 * 16 * 1024 + ct64 * 128;
    asm volatile("s_nop 7\n\ts_nop 3" : "+v"(SA), "+v"(K), "+v"(VA0), "+v"(VB0));
#pragma unroll 1
    for (int seg = 0; seg < 16; ++seg) {
        if (seg == 2) {
            WAITVM(0); BAR();
            if (tid == 0) __hip_atomic_store((gu64*)(ws + WS_F2 + (rb * 16 + cg) * 8), tag, RLX_AGENT);
            wo0a = trfrag(wta, 1024); wo0b = trfrag(wtb, 1024); wo1a = trfrag(wta + 2048, 1024); wo1b = trfrag(wtb + 2048, 1024);
            asm volatile("s_waitcnt lgkmcnt(0)" : "+v"(wo0a), "+v"(wo0b), "+v"(wo1a), "+v"(wo1b)::"memory");
#pragma unroll
            for (int n = 0; n < 4; ++n) glds(vws, bwo + (4 + n) * 8192, wsl + n * 1024);
        }
        if (seg == 4) {
            WAITVM(0);
            wo2a = trfrag(wta, 1024); wo2b = trfrag(wtb, 1024); wo3a = trfrag(wta + 2048, 1024); wo3b = trfrag(wtb + 2048, 1024);
            asm volatile("s_waitcnt lgkmcnt(0)" : "+v"(wo2a), "+v"(wo2b), "+v"(wo3a), "+v"(wo3b)::"memory");
            wait_tags(ws, WS_F2 + ((rt64 >> 1) * 16 + (lane & 15)) * 8, tag);
            if (b == 0 && w == 0) {
                wait_tags(ws, WS_F1 + lane * 8, tag); wait_tags(ws, WS_F1 + 512 + lane * 8, tag);
                wait_tags(ws, WS_F1 + 1024 + lane * 8, tag); wait_tags(ws, WS_F1 + 1536 + lane * 8, tag);
                if (lane == 0) __hip_atomic_store((gu64*)(ws + WS_EPOCH), tag, RLX_AGENT);
            }
        }
        if (seg >= 5 && seg - 5 < nfs) {
            asm volatile("s_waitcnt vmcnt(0)" : "+v"(h0), "+v"(h1)::"memory");
            const bf16x8 wa = trfrag(wta, 1024), wb = trfrag(wtb, 1024);
            d00 = MFMA(wa, __builtin_bit_cast(bf16x8, h0), d00); d01 = MFMA(wb, __builtin_bit_cast(bf16x8, h0), d01);
            d10 = MFMA(wa, __builtin_bit_cast(bf16x8, h1), d10); d11 = MFMA(wb, __builtin_bit_cast(bf16x8, h1), d11);
            asm volatile("" : "+v"(d00), "+v"(d01), "+v"(d10), "+v"(d11));
        }
        if (seg >= 4 && seg - 4 < nfs) {
            LOAD_DEV(h0, l16, bh + (seg - 4) * 1024); LOAD_DEV(h1, l16, bh + (86 + seg - 4) * 1024);
            glds(vws, bwd + (seg - 4) * 16384, wsl); glds(vws, bwd + (seg - 4) * 16384 + 8192, wsl + 1024);
        }
        asm volatile(
            TILE(160, 176, 232, 244, 240, 4096, 2048, 2560)
            TILE(176, 160, 240, 236, 232, 6144, 4096, 4608)
            TILE(160, 176, 232, 244, 240, 8192, 6144, 6656)
            TILE(176, 160, 240, 236, 232, 10240, 8192, 8704)
            "v_add_u32 %[aK], 0x2000, %[aK]\n\tv_add_u32 %[aV], 0x2000, %[aV]"
            : "+{v[160:175]}"(SA), "+{v[176:191]}"(SB), "+{v[192:207]}"(O), "+{v[224:227]}"(K), "+{v[232:235]}"(VA0), "+{v[236:239]}"(VB0),
              "+{v[240:243]}"(VA1), "+{v[244:247]}"(VB1), "+{v[248:251]}"(PA), "+{v[252:255]}"(PB), [aK] "+v"(aK), [aV] "+v"(aV)
            : "{v[228:231]}"(Q), "{v[208:223]}"(NM), [msk] "s"(msk)
            : "memory");
    }
    asm volatile("s_nop 1" : "+v"(PB));
    O = MFMA(__builtin_bit_cast(bf16x8, VB1), __builtin_bit_cast(bf16x8, PB), O);
    {
      float chk = fabsf(O[8]);
#pragma unroll
      for (int e = 0; e < 8; ++e) chk = fmaxf(chk, fabsf(O[e])) + 0.f * O[e];
      if (__builtin_expect(__any(!(chk < 3.0e38f)), 0)) {
          const f32x16 z = {}; float m = -INFINITY;
#pragma unroll 1
          for (int t = 0; t < 64; ++t) { const f32x16 s = MFMA(lds128(lds + t * 2048 + l16), Q, z);
#pragma unroll
              for (int e = 0; e < 16; ++e) m = fmaxf(m, s[e]); }
          m = -swapmax(m);
#pragma unroll
          for (int e = 0; e < 16; ++e) NM[e] = m;
          O = z;
          const u32x4 one = {0x3f803f80u, 0x3f803f80u, 0x3f803f80u, 0x3f803f80u};
          const unsigned av = 1024 + hh * 256 + (lane & 15) * 16;
#pragma unroll 1
          for (int t = 0; t < 64; ++t) { f32x16 s = MFMA(lds128(lds + t * 2048 + l16), Q, NM);
#pragma unroll
              for (int e = 0; e < 16; ++e) s[e] = __builtin_amdgcn_exp2f(s[e]);
              u32x4 pa, pb;
#pragma unroll
              for (int e = 0; e < 4; ++e) { pa[e] = cvtpk(s[2 * e], s[2 * e + 1]); pb[e] = cvtpk(s[8 + 2 * e], s[9 + 2 * e]); }
              const u32x4 va = __builtin_bit_cast(u32x4, lds128(lds + t * 2048 + av)), vb = __builtin_bit_cast(u32x4, lds128(lds + t * 2048 + av + 512));
              O = MFMA(__builtin_bit_cast(bf16x8, blk ? one : va), __builtin_bit_cast(bf16x8, pa), O);
              O = MFMA(__builtin_bit_cast(bf16x8, blk ? one : vb), __builtin_bit_cast(bf16x8, pb), O); }
      } }
    { const float il = __builtin_amdgcn_rcpf(O[8]);
      const u32x4 oq = {cvtpk(O[0] * il, O[1] * il), cvtpk(O[2] * il, O[3] * il), cvtpk(O[4] * il, O[5] * il), cvtpk(O[6] * il, O[7] * il)};
      __builtin_amdgcn_raw_buffer_store_b128(oq, wsr, WS_OF + ((qb * 8 + w) * 32 + head) * 1024 + l16, 0, 16);
      WAITVM(0);
      if (lane == 0) __hip_atomic_store((gu64*)(ws + WS_F3 + ((qb * 8 + w) * 32 + head) * 8), tag, RLX_AGENT); }
    const int orow = tid >> 3, oc = tid & 7;
    const size_t ooff = (size_t)(rt64 * 64 + orow) * 1024 + ct64 * 128 + oc * 16;
    const u32x4 xres = *(const u32x4*)(gx + ooff);
    const lds_t pw = lds + w * 16384 + r * 256;
    const lds_t pr = lds + orow * 256;
    auto put = [&](const f32x16& v, int rtile, int ctile) {
#pragma unroll
        for (int q4 = 0; q4 < 4; ++q4)
            *(__attribute__((address_space(3))) f32x4*)(pw + rtile * 8192 + (((ctile * 8 + q4 * 2 + hh) ^ (r & 7)) << 4)) = f32x4{v[4 * q4], v[4 * q4 + 1], v[4 * q4 + 2], v[4 * q4 + 3]};
    };
    auto gather = [&](f32x4& lo, f32x4& hi) {
        lo = f32x4{}; hi = f32x4{};
#pragma unroll
        for (int p = 0; p < 8; ++p) { lo += lds128f(pr + p * 16384 + (((2 * oc) ^ (orow & 7)) << 4)); hi += lds128f(pr + p * 16384 + (((2 * oc + 1) ^ (orow & 7)) << 4)); }
    };
    BAR();
    put(d00, 0, 0); put(d01, 0, 1); put(d10, 1, 0); put(d11, 1, 1);
    BAR();
    f32x4 glo, ghi; gather(glo, ghi);
#pragma unroll
    for (int e = 0; e < 4; ++e) { glo[e] *= __builtin_amdgcn_rcpf(1.0f + __builtin_amdgcn_exp2f(glo[e] * -1.4426950408889634f));
                                  ghi[e] *= __builtin_amdgcn_rcpf(1.0f + __builtin_amdgcn_exp2f(ghi[e] * -1.4426950408889634f)); }
    asm volatile("" : "+v"(glo), "+v"(ghi));
    { int l3 = __builtin_amdgcn_mbcnt_hi(~0u, __builtin_amdgcn_mbcnt_lo(~0u, 0u)); asm volatile("" : "+v"(l3));
      wait_tags(ws, WS_F3 + ((2 * rt64 + ((l3 >> 2) & 1)) * 32 + 4 * w + (l3 & 3)) * 8, tag); }
    { const char* bo = ws + WS_OF + (size_t)(2 * rt64 * 32 + 4 * w) * 1024;
      u32x4 o00, o01, o02, o03, o10, o11, o12, o13;
      LOAD_DEV(o00, l16, bo); LOAD_DEV(o10, l16, bo + 32768); LOAD_DEV(o01, l16, bo + 1024); LOAD_DEV(o11, l16, bo + 32768 + 1024);
      LOAD_DEV(o02, l16, bo + 2048); LOAD_DEV(o12, l16, bo + 32768 + 2048); LOAD_DEV(o03, l16, bo + 3072); LOAD_DEV(o13, l16, bo + 32768 + 3072);
      const f32x16 z = {};
      asm volatile("s_waitcnt vmcnt(6)" : "+v"(o00), "+v"(o10)::"memory");
      d00 = MFMA(wo0a, __builtin_bit_cast(bf16x8, o00), z); d01 = MFMA(wo0b, __builtin_bit_cast(bf16x8, o00), z);
      d10 = MFMA(wo0a, __builtin_bit_cast(bf16x8, o10), z); d11 = MFMA(wo0b, __builtin_bit_cast(bf16x8, o10), z);
      asm volatile("s_waitcnt vmcnt(4)" : "+v"(o01), "+v"(o11), "+v"(d00)::"memory");
      d00 = MFMA(wo1a, __builtin_bit_cast(bf16x8, o01), d00); d01 = MFMA(wo1b, __builtin_bit_cast(bf16x8, o01), d01);
      d10 = MFMA(wo1a, __builtin_bit_cast(bf16x8, o11), d10); d11 = MFMA(wo1b, __builtin_bit_cast(bf16x8, o11), d11);
      asm volatile("s_waitcnt vmcnt(2)" : "+v"(o02), "+v"(o12), "+v"(d00)::"memory");
      d00 = MFMA(wo2a, __builtin_bit_cast(bf16x8, o02), d00); d01 = MFMA(wo2b, __builtin_bit_cast(bf16x8, o02), d01);
      d10 = MFMA(wo2a, __builtin_bit_cast(bf16x8, o12), d10); d11 = MFMA(wo2b, __builtin_bit_cast(bf16x8, o12), d11);
      asm volatile("s_waitcnt vmcnt(0)" : "+v"(o03), "+v"(o13), "+v"(d00)::"memory");
      d00 = MFMA(wo3a, __builtin_bit_cast(bf16x8, o03), d00); d01 = MFMA(wo3b, __builtin_bit_cast(bf16x8, o03), d01);
      d10 = MFMA(wo3a, __builtin_bit_cast(bf16x8, o13), d10); d11 = MFMA(wo3b, __builtin_bit_cast(bf16x8, o13), d11); }
    BAR();
    put(d00, 0, 0); put(d01, 0, 1); put(d10, 1, 0); put(d11, 1, 1);
    BAR();
    f32x4 slo, shi; gather(slo, shi);
    u32x4 res;
#pragma unroll
    for (int e = 0; e < 2; ++e) {
        res[e] = cvtpk(__builtin_fmaf(slo[2 * e], glo[2 * e], __uint_as_float(xres[e] << 16)), __builtin_fmaf(slo[2 * e + 1], glo[2 * e + 1], __uint_as_float(xres[e] & 0xffff0000u)));
        res[2 + e] = cvtpk(__builtin_fmaf(shi[2 * e], ghi[2 * e], __uint_as_float(xres[2 + e] << 16)), __builtin_fmaf(shi[2 * e + 1], ghi[2 * e + 1], __uint_as_float(xres[2 + e] & 0xffff0000u)));
    }
    *(u32x4*)(gout + ooff) = res;
#undef lane
#undef r
#undef hh
#undef blk
#undef tq
#undef tp
#undef tid
}

extern "C" void kernel_launch(void* const* d_in, const int* in_sizes, int n_in, void* d_out, int out_size, void* d_ws, size_t ws_size,
                              hipStream_t stream) {
    (void)hipFuncSetAttribute((const void*)fused_block, hipFuncAttributeMaxDynamicSharedMemorySize, LDS_BYTES);
    fused_block<<<256, 512, LDS_BYTES, stream>>>((const char*)d_in[0], (const char*)d_in[1], (const char*)d_in[2], (const char*)d_in[4],
                                                 (const char*)d_in[5], (const char*)d_in[6], (char*)d_out, (char*)d_ws);
}
```

```cpp
#define PF_CODE
#define PADA 0
#define PADB 1
#include <hip/hip_runtime.h>
#include <stdint.h>

typedef __attribute__((ext_vector_type(8))) short bf16x8;
typedef __attribute__((ext_vector_type(4))) short s16x4;
typedef __attribute__((ext_vector_type(2))) __bf16 bf2;
typedef __attribute__((ext_vector_type(16))) float f32x16;
typedef __attribute__((ext_vector_type(4))) float f32x4;
typedef __attribute__((ext_vector_type(2))) float f32x2;
typedef __attribute__((ext_vector_type(4))) unsigned u32x4;
typedef unsigned long long u64;
typedef __attribute__((address_space(3))) char* lds_t;
typedef __attribute__((address_space(1))) u64 gu64;

template <int N> struct IC { static constexpr int value = N; };
template <bool B> struct BC { static constexpr bool value = B; };
#define DI __device__ __forceinline__
#define MFMA(a, b, c) __builtin_amdgcn_mfma_f32_32x32x16_bf16((a), (b), (c), 0, 0, 0)
#define RLX_AGENT __ATOMIC_RELAXED, __HIP_MEMORY_SCOPE_AGENT

constexpr unsigned WS_EPOCH = 0;
constexpr unsigned WS_F1 = 4096;
constexpr unsigned WS_F2 = 8192;
constexpr unsigned WS_F3 = 16384;
constexpr unsigned WS_QF = 65536;
constexpr unsigned WS_KV = WS_QF + (2u << 20);
constexpr unsigned WS_HF = WS_KV + (4u << 20);
constexpr unsigned WS_OF = WS_HF + 64u * 86u * 1024u;
constexpr unsigned WS_END = WS_OF + (2u << 20);
constexpr unsigned SLOT = 28672, XIMG = 0, WIMG = 16384, GIMG = 0, UIMG = 12288 + 32;
constexpr unsigned L_STAT = 153600;
constexpr unsigned L_RSTD = L_STAT + 1024;
constexpr unsigned L_CSQ = L_RSTD + 512;
constexpr unsigned L_CSG = L_CSQ;
constexpr unsigned L_WSL = 131072;
constexpr unsigned LDS_BYTES = 163840;
constexpr float QSCALE = 0.25f * 1.4426950408889634f;
constexpr float M_OFF = 64.0f;

DI unsigned cvtpk(float lo, float hi) { return __builtin_bit_cast(unsigned, __builtin_convertvector(f32x2{lo, hi}, bf2)); }
DI float dot2(unsigned a, unsigned b, float c) { return __builtin_amdgcn_fdot2_f32_bf16(__builtin_bit_cast(bf2, a), __builtin_bit_cast(bf2, b), c, false); }
DI void glds(unsigned voff, const char* sbase, unsigned ldst) {
    asm volatile("s_mov_b32 m0, %2\n\ts_nop 0\n\tglobal_load_lds_dwordx4 %0, %1" ::"v"(voff), "s"(sbase), "s"(ldst) : "memory"); }
DI void glds_dev(unsigned voff, const char* sbase, unsigned ldst) {
    asm volatile("s_mov_b32 m0, %2\n\ts_nop 0\n\tglobal_load_lds_dwordx4 %0, %1 sc1" ::"v"(voff), "s"(sbase), "s"(ldst) : "memory"); }
#define LOAD_DEV(dst, voff, sbase) asm volatile("global_load_dwordx4 %0, %1, %2 sc1" : "=v"(dst) : "v"(voff), "s"(sbase) : "memory")
#define WAITVM(n) asm volatile("s_waitcnt vmcnt(" #n ")" ::: "memory")
#define BAR() asm volatile("s_waitcnt lgkmcnt(0)\n\ts_barrier" ::: "memory")
template <int N> DI void waitvm() { asm volatile("s_waitcnt vmcnt(%0)" ::"i"(N) : "memory"); }
DI bf16x8 lds128(lds_t p) { return *(const __attribute__((address_space(3))) bf16x8*)p; }
DI f32x4 lds128f(lds_t p) { return *(const __attribute__((address_space(3))) f32x4*)p; }
DI s16x4 ldstr(lds_t p) { return __builtin_amdgcn_ds_read_tr16_b64_v4i16((__attribute__((address_space(3))) s16x4*)p); }
DI bf16x8 trfrag(lds_t p, int step) { s16x4 a = ldstr(p), b = ldstr(p + step); return __builtin_shufflevector(a, b, 0, 1, 2, 3, 4, 5, 6, 7); }
DI float swapadd(float v) { auto r = __builtin_amdgcn_permlane32_swap(__float_as_uint(v), __float_as_uint(v), false, false); return __uint_as_float(r[0]) + __uint_as_float(r[1]); }
DI float swapmax(float v) { auto r = __builtin_amdgcn_permlane32_swap(__float_as_uint(v), __float_as_uint(v), false, false); return fmaxf(__uint_as_float(r[0]), __uint_as_float(r[1])); }
DI float frag_sum(bf16x8 f, float acc) { u32x4 u = __builtin_bit_cast(u32x4, f); acc = dot2(u[0], 0x3f803f80u, acc); acc = dot2(u[1], 0x3f803f80u, acc); acc = dot2(u[2], 0x3f803f80u, acc); return dot2(u[3], 0x3f803f80u, acc); }
DI bf16x8 split_a(float v) { unsigned h = cvtpk(v, v); float lo = v - __uint_as_float(h << 16); u32x4 u = {h, cvtpk(lo, lo), 0u, 0u}; return __builtin_bit_cast(bf16x8, u); }
DI bf16x8 split_b(float v) { unsigned h = cvtpk(v, v); float lo = v - __uint_as_float(h << 16); unsigned p = cvtpk(v, lo); u32x4 u = {p, p, 0u, 0u}; return __builtin_bit_cast(bf16x8, u); }

#define STAMP(n) do { if ((n) == PADA || (n) == PADB || (n) == 0) asm volatile("s_memrealtime %0\n\ts_waitcnt lgkmcnt(0)" : "=s"(stamps[n]) :: "memory"); } while (0)
#define STAMP_DECL u64 stamps[16]; u64 clk0 = __builtin_amdgcn_s_memtime();
#define STAMP_FLUSH do { u64 now_ = __builtin_amdgcn_s_memrealtime(); u64 tgt_ = PADA == PADB ? stamps[0] + 8000 : now_ + 3 * (stamps[PADB] - stamps[PADA]); \
    if (PADA == 15) { while (__builtin_amdgcn_s_memtime() - clk0 < 200000) __builtin_amdgcn_s_sleep(1); } else \
    while (__builtin_amdgcn_s_memrealtime() < tgt_) __builtin_amdgcn_s_sleep(1); } while (0)
DI void wait_tags(const char* ws, unsigned off, u64 tag) {
    const u64 t0 = __builtin_amdgcn_s_memrealtime();
    for (;;) {
        u64 v = __hip_atomic_load((gu64*)(ws + off), RLX_AGENT);
        if (__all(v == tag)) break;
        __builtin_amdgcn_s_sleep(1);
        if (__builtin_amdgcn_s_memrealtime() - t0 > 2000000ull) break;
    }
    asm volatile("" ::: "memory");
}

#define EX(S, i) "v_exp_f32 v[" #S "+" #i "], v[" #S "+" #i "]\n\t"
#define CV(S, p, i, j) "v_cvt_pk_bf16_f32 v[248+" #p "], v[" #S "+" #i "], v[" #S "+" #j "]\n\t"
#define TILE(SC, SN, VAc, VBp, VAl, KOFF, VOFF0, VOFF1) \
    "v_mfma_f32_32x32x16_bf16 v[" #SN ":" #SN "+15], v[224:227], v[228:231], v[208:223]\n\t" \
    EX(SC, 0) EX(SC, 1) EX(SC, 2) CV(SC, 0, 0, 1) EX(SC, 3) EX(SC, 4) CV(SC, 1, 2, 3) EX(SC, 5) \
    "v_mfma_f32_32x32x16_bf16 v[192:207], v[" #VBp ":" #VBp "+3], v[252:255], v[192:207]\n\t" \
    "ds_read_b128 v[224:227], %[aK] offset:" #KOFF "\n\t" \
    "s_mov_b64 exec, %[msk]\n\t" \
    "ds_read_b128 v[" #VAl ":" #VAl "+3], %[aV] offset:" #VOFF0 "\n\t" \
    "ds_read_b128 v[" #VBp ":" #VBp "+3], %[aV] offset:" #VOFF1 "\n\t" \
    "s_mov_b64 exec, -1\n\t" \
    EX(SC, 6) CV(SC, 2, 4, 5) EX(SC, 7) EX(SC, 8) CV(SC, 3, 6, 7) EX(SC, 9) EX(SC, 10) \
    "v_mfma_f32_32x32x16_bf16 v[192:207], v[" #VAc ":" #VAc "+3], v[248:251], v[192:207]\n\t" \
    CV(SC, 4, 8, 9) EX(SC, 11) EX(SC, 12) CV(SC, 5, 10, 11) EX(SC, 13) EX(SC, 14) CV(SC, 6, 12, 13) EX(SC, 15) \
    "s_waitcnt lgkmcnt(0)\n\t" \
    CV(SC, 7, 14, 15)

extern "C" __global__ void __launch_bounds__(512, 2)
fused_block(const char* __restrict__ gx, const char* __restrict__ gwqkv, const char* __restrict__ gwo, const char* __restrict__ gwg,
            const char* __restrict__ gwu, const char* __restrict__ gwd, char* __restrict__ gout, char* ws) {
    extern __shared__ __attribute__((aligned(16))) char smem[];
    const lds_t lds = (lds_t)smem;
    STAMP_DECL STAMP(0);
    const int tid = threadIdx.x, lane = tid & 63, r = lane & 31, hh = lane >> 5;
    const int w = __builtin_amdgcn_readfirstlane(tid >> 6), wr = w & 3, g = w >> 2;
    const int b = blockIdx.x, xcd = b & 7, bi = b >> 3;
    u64 epoch;
    asm volatile("s_load_dwordx2 %0, %1, 0x0" : "=s"(epoch) : "s"(ws) : "memory");
    const __amdgpu_buffer_rsrc_t wsr = __builtin_amdgcn_make_buffer_rsrc(ws, 0, WS_END, 0x00020000);

    const int rb = 4 * (xcd >> 1) + (bi >> 3), cg = 8 * (xcd & 1) + (bi & 7);
    const int nft = cg < 6 ? 6 : 5, ft0 = cg < 6 ? 6 * cg : 36 + 5 * (cg - 6);
    unsigned vx0, vx1, vw0, vw1, vg0, vg1, vg2;
    { int row = 16 * w + (lane >> 3); vx0 = row * 1024 + (((lane & 7) ^ ((row >> 1) & 7)) << 4);
      row += 8; vx1 = row * 1024 + (((lane & 7) ^ ((row >> 1) & 7)) << 4);
      int o = w * 1024 + lane * 16; vw0 = (o / 192) * 3072 + o % 192;
      o = ((w & 3) + 8) * 1024 + lane * 16; vw1 = (o / 192) * 3072 + o % 192;
      const int lim = nft * 32;
      o = w * 1024 + lane * 16; int cb = o % 192; vg0 = (o / 192) * 2752 + (cb >= lim ? cb - 32 : cb);
      o = (w < 4 ? w + 8 : w - 4) * 1024 + lane * 16; cb = o % 192; vg1 = (o / 192) * 2752 + (cb >= lim ? cb - 32 : cb);
      o = (w + 4) * 1024 + lane * 16; cb = o % 192; vg2 = (o / 192) * 2752 + (cb >= lim ? cb - 32 : cb); }
    const char* bx = gx + (size_t)rb * 128 * 1024;
    const char* bw = gwqkv + cg * 192;
    const char* bg0 = gwg + ft0 * 32;
    const char* bg1 = (w < 4 ? gwg : gwu) + ft0 * 32;
    const char* bg2 = gwu + ft0 * 32;
    const unsigned dg1 = w < 4 ? GIMG + (w + 8) * 1024 : UIMG + (w - 4) * 1024;
    auto issue = [&](int c, int gg) {
        const unsigned s = (c % 5) * SLOT;
        if (c < 8) {
            glds(vx0, bx + c * 128, s + XIMG + w * 2048);
            glds(vx1, bx + c * 128, s + XIMG + w * 2048 + 1024);
            glds(vw0, bw + c * (64 * 3072), s + WIMG + w * 1024);
            if (gg == 0) glds(vw1, bw + c * (64 * 3072), s + WIMG + (w + 8) * 1024);
        } else {
            glds(vg0, bg0 + (c - 8) * (64 * 2752), s + GIMG + w * 1024);
            glds(vg1, bg1 + (c - 8) * (64 * 2752), s + dg1);
            glds(vg2, bg2 + (c - 8) * (64 * 2752), s + UIMG + (w + 4) * 1024);
        }
    };
#pragma unroll
    for (int c = 0; c < 5; ++c) issue(c, g);
#define TOUCH(voff, base) asm volatile("s_mov_b32 m0, %2\n\ts_nop 0\n\tglobal_load_lds_dword %0, %1" ::"v"(voff), "s"(base), "s"(163840u - 256u) : "memory")
    { const char* pc; asm volatile("s_getpc_b64 %0" : "=s"(pc));
      const unsigned o = tid * 128;
      if (o < 39000) TOUCH(o, pc); }

    const int xrow = 32 * wr + r, blk = (lane >> 4) & 1, tq = (lane & 15) >> 2, tp = lane & 3;
    const unsigned xa = xrow * 128 + ((hh ^ ((xrow >> 1) & 7)) << 4);
    const lds_t wqa = lds + WIMG + (8 * hh + tq) * 192 + blk * 32 + tp * 8 + (g ? 128 : 0);
    const lds_t wga = lds + (blk ? UIMG : GIMG) + (8 * hh + tq) * 192 + tp * 8 + g * 96;
    const int ct0 = 3 * cg + (g ? 2 : 0);

    bf16x8 xf[32];
    f32x16 a0 = {}, a1 = {}, a2 = {};
    float cs0 = 0.f, cs1 = 0.f, cs2 = 0.f, sx = 0.f, sq = 0.f;
    auto qkv_loop = [&](auto gconst, auto vconst) {
        constexpr int G = decltype(gconst)::value; constexpr bool V = decltype(vconst)::value;
#pragma unroll
        for (int i = 0; i < 8; ++i) {
            if (G == 0) { switch (i) { case 0: WAITVM(16); break; case 5: WAITVM(11); break; case 6: WAITVM(10); break; case 7: WAITVM(9); break; default: WAITVM(12); } }
            else { if (i == 0) WAITVM(12); else WAITVM(9); }
            BAR();
            if (i == 0) STAMP(1);
            if (i == 4) STAMP(2);
            if (i >= 1) issue(i + 4, G);
            const unsigned s = (i % 5) * SLOT;
#pragma unroll
            for (int ks = 0; ks < 4; ++ks) {
                const bf16x8 x = lds128(lds + s + XIMG + (xa ^ (ks << 5)));
                xf[4 * i + ks] = x;
                const bf16x8 w0 = trfrag(wqa + s + ks * 16 * 192, 4 * 192);
                a0 = V ? MFMA(x, w0, a0) : MFMA(w0, x, a0);
                if (G == 0) { const bf16x8 w1 = trfrag(wqa + s + ks * 16 * 192 + 64, 4 * 192); a1 = V ? MFMA(x, w1, a1) : MFMA(w1, x, a1); }
                else { u32x4 u = __builtin_bit_cast(u32x4, x);
#pragma unroll
                    for (int e = 0; e < 4; ++e) { sx = dot2(u[e], 0x3f803f80u, sx); sq = dot2(u[e], u[e], sq); } }
            }
            cs0 = frag_sum(trfrag(wqa + s + wr * 16 * 192, 4 * 192), cs0);
            if (G == 0) cs1 = frag_sum(trfrag(wqa + s + wr * 16 * 192 + 64, 4 * 192), cs1);
            asm volatile("" : "+v"(cs0), "+v"(cs1), "+v"(sx), "+v"(sq));
        }
    };
    if (g == 0) { if (ct0 >= 32) qkv_loop(IC<0>{}, BC<true>{}); else qkv_loop(IC<0>{}, BC<false>{}); }
    else { if (ct0 >= 32) qkv_loop(IC<1>{}, BC<true>{}); else qkv_loop(IC<1>{}, BC<false>{}); }
    asm volatile("" : "+s"(epoch));
    const u64 tag = epoch + 0x9E3779B97F4A7C15ull;
    *(__attribute__((address_space(3))) float*)(lds + L_CSQ + (g ? 2 : 0) * 1024 + lane * 16 + wr * 4) = cs0;
    if (g == 0) *(__attribute__((address_space(3))) float*)(lds + L_CSQ + 1024 + lane * 16 + wr * 4) = cs1;
    else {
        sx = swapadd(sx); sq = swapadd(sq);
        const float mean = sx * (1.f / 512.f), var = fmaxf(sq * (1.f / 512.f) - mean * mean, 0.f), rs = __builtin_amdgcn_rsqf(var + 1e-5f);
        if (hh == 0) { *(__attribute__((address_space(3))) f32x2*)(lds + L_STAT + xrow * 8) = f32x2{mean, rs};
                       *(__attribute__((address_space(3))) float*)(lds + L_RSTD + xrow * 4) = rs; }
    }
    float rstd; bf16x8 mfrag;
    const int rt = rb * 4 + wr;
#pragma unroll
    for (int i = 8; i < 16; ++i) {
        switch (i) { case 9: WAITVM(11); break; case 10: WAITVM(6); break; case 13: WAITVM(6); break; case 14: WAITVM(3); break; case 15: WAITVM(0); break; default: WAITVM(9); }
        BAR();
        if (i == 8) STAMP(3);
        if (i == 9) STAMP(4);
        if (i == 8) {
            const f32x2 st = *(const __attribute__((address_space(3))) f32x2*)(lds + L_STAT + xrow * 8);
            rstd = st[1]; mfrag = split_b(-st[0]);
            for (int t = 0; t < 2 - g; ++t) {
                const int ct = ct0 + t, hp = ct & 15, j = g ? 2 : t;
                const f32x4 p = lds128f(lds + L_CSQ + j * 1024 + lane * 16);
                const bf16x8 cf = split_a((p[0] + p[1]) + (p[2] + p[3]));
                u32x4 lo, hi; unsigned o0, o1;
                if (ct >= 32) {
                    a0 = MFMA(mfrag, cf, a0);
#pragma unroll
                    for (int q4 = 0; q4 < 4; ++q4) { const f32x4 rs = lds128f(lds + L_RSTD + (32 * wr + 8 * q4 + 4 * hh) * 4);
#pragma unroll
                        for (int e = 0; e < 4; ++e) a0[4 * q4 + e] *= rs[e]; }
                    o0 = WS_KV + ((2 * hp + blk) * 64 + rt) * 2048 + 1024 + hh * 256 + (lane & 15) * 16; o1 = o0 + 512;
                } else {
                    a0 = MFMA(cf, mfrag, a0);
                    const float sc = ct < 16 ? rstd * QSCALE : rstd;
#pragma unroll
                    for (int e = 0; e < 16; ++e) a0[e] *= sc;
                    o0 = (ct < 16 ? WS_QF + (2 * hp * 64 + rt) * 1024 : WS_KV + (2 * hp * 64 + rt) * 2048) + lane * 16;
                    o1 = o0 + (ct < 16 ? 64 * 1024 : 64 * 2048);
                }
#pragma unroll
                for (int e = 0; e < 4; ++e) { lo[e] = cvtpk(a0[2 * e], a0[2 * e + 1]); hi[e] = cvtpk(a0[8 + 2 * e], a0[9 + 2 * e]); }
                __builtin_amdgcn_raw_buffer_store_b128(lo, wsr, o0, 0, 16);
                __builtin_amdgcn_raw_buffer_store_b128(hi, wsr, o1, 0, 16);
                a0 = a1;
            }
            a0 = f32x16{}; a1 = f32x16{}; cs0 = 0.f; cs1 = 0.f;
            asm volatile("" ::: "memory");
        }
        if (i == 10 && tid == 0) __hip_atomic_store((gu64*)(ws + WS_F1 + (rb * 16 + cg) * 8), tag, RLX_AGENT);
        if (i + 4 < 16) issue(i + 4, 0);
        const unsigned s = (i % 5) * SLOT;
#pragma unroll
        for (int ks = 0; ks < 4; ++ks) {
            const bf16x8 w0 = trfrag(wga + s + ks * 16 * 192, 4 * 192), w1 = trfrag(wga + s + ks * 16 * 192 + 32, 4 * 192), w2 = trfrag(wga + s + ks * 16 * 192 + 64, 4 * 192);
            a0 = MFMA(w0, xf[4 * (i - 8) + ks], a0); a1 = MFMA(w1, xf[4 * (i - 8) + ks], a1); a2 = MFMA(w2, xf[4 * (i - 8) + ks], a2);
        }
        cs0 = frag_sum(trfrag(wga + s + wr * 16 * 192, 4 * 192), cs0); cs1 = frag_sum(trfrag(wga + s + wr * 16 * 192 + 32, 4 * 192), cs1);
        cs2 = frag_sum(trfrag(wga + s + wr * 16 * 192 + 64, 4 * 192), cs2);
        asm volatile("" : "+v"(cs0), "+v"(cs1), "+v"(cs2));
    }
    *(__attribute__((address_space(3))) float*)(lds + L_CSG + (3 * g + 0) * 1024 + lane * 16 + wr * 4) = cs0;
    *(__attribute__((address_space(3))) float*)(lds + L_CSG + (3 * g + 1) * 1024 + lane * 16 + wr * 4) = cs1;
    *(__attribute__((address_space(3))) float*)(lds + L_CSG + (3 * g + 2) * 1024 + lane * 16 + wr * 4) = cs2;

    int lane2 = __builtin_amdgcn_mbcnt_hi(~0u, __builtin_amdgcn_mbcnt_lo(~0u, 0u));
    asm volatile("" : "+v"(lane2));
#define lane lane2
    const int r2 = lane & 31, hh2 = lane >> 5, blk2 = (lane >> 4) & 1, tq2 = (lane & 15) >> 2, tp2 = lane & 3, tid2 = w * 64 + lane;
#define r r2
#define hh hh2
#define blk blk2
#define tq tq2
#define tp tp2
#define tid tid2
    STAMP(5);
    const int head = 4 * xcd + (bi & 3), qb = bi >> 2;
    if (w == 0) {
        const int hp = head >> 1;
        const int frb = lane < 32 ? (lane & 15) : 2 * qb + (lane & 1), fct = lane < 16 ? 16 + hp : lane < 32 ? 32 + hp : hp;
        wait_tags(ws, WS_F1 + (frb * 16 + fct / 3) * 8, tag);
    }
    BAR();
    STAMP(6);
    u32x4 qf;
    const unsigned l16 = lane * 16;
    LOAD_DEV(qf, l16, ws + WS_QF + (head * 64 + qb * 8 + w) * 1024);
    { const char* kv = ws + WS_KV + head * 131072 + w * 2048;
#pragma unroll
      for (int n = 0; n < 8; ++n) { glds_dev(l16, kv + n * 16384, n * 16384 + w * 2048); glds_dev(l16, kv + n * 16384 + 1024, n * 16384 + w * 2048 + 1024); } }
#pragma unroll 1
    for (int t = 0; t < 3; ++t) {
        const f32x4 p = lds128f(lds + L_CSG + (3 * g + t) * 1024 + lane * 16);
        a0 = MFMA(split_a((p[0] + p[1]) + (p[2] + p[3])), mfrag, a0);
        float hv[8];
#pragma unroll
        for (int e = 0; e < 8; ++e) {
            const float gg = a0[e] * rstd, uu = a0[8 + e] * rstd;
            const float z = gg * __builtin_fmaf(gg * gg, -2.0f * 0.7978845608028654f * 0.044715f * 1.4426950408889634f, -2.0f * 0.7978845608028654f * 1.4426950408889634f);
            hv[e] = gg * uu * __builtin_amdgcn_rcpf(1.0f + __builtin_amdgcn_exp2f(z));
        }
        const u32x4 hq = {cvtpk(hv[0], hv[1]), cvtpk(hv[2], hv[3]), cvtpk(hv[4], hv[5]), cvtpk(hv[6], hv[7])};
        if (3 * g + t < nft) __builtin_amdgcn_raw_buffer_store_b128(hq, wsr, WS_HF + (rt * 86 + ft0 + 3 * g + t) * 1024 + l16, 0, 16);
        a0 = a1; a1 = a2;
    }

    const int rt64 = 4 * xcd + (bi >> 3), ct64 = bi & 7;
    const unsigned vws = (lane >> 3) * 1024 + (((lane & 7) ^ (((lane >> 4) & 1) << 2)) << 4);
    const unsigned wsl = L_WSL + w * 4096;
    const lds_t wta = lds + wsl + (4 * hh + tq) * 128 + ((tq >> 1) & 1) * 64 + blk * 32 + tp * 8;
    const lds_t wtb = lds + wsl + (4 * hh + tq) * 128 + (((tq >> 1) & 1) ^ 1) * 64 + blk * 32 + tp * 8;
    const char* bwo = gwo + (size_t)w * 64 * 1024 + ct64 * 128;
    STAMP(7);
    asm volatile("s_waitcnt vmcnt(16)" : "+v"(qf)::"memory");
    BAR();
    STAMP(8);
#pragma unroll
    for (int n = 0; n < 4; ++n) glds(vws, bwo + n * 8192, wsl + n * 1024);
    const bf16x8 Q = __builtin_bit_cast(bf16x8, qf);
    f32x16 NM, SA, SB, O = {};
    { const f32x16 z = {};
      SA = MFMA(lds128(lds + l16), Q, z); SB = MFMA(lds128(lds + 2048 + l16), Q, z);
      float m = fmaxf(SA[0], SB[0]);
#pragma unroll
      for (int e = 1; e < 16; ++e) m = fmaxf(fmaxf(m, SA[e]), SB[e]);
      m = -(swapmax(m) + M_OFF);
#pragma unroll
      for (int e = 0; e < 16; ++e) NM[e] = m; }
    unsigned aK = l16, aV = 1024 + hh * 256 + (lane & 15) * 16;
    const u64 msk = 0x0000ffff0000ffffull;
    u32x4 K, VA0, VB0, VA1, VB1, PA = {}, PB = {};
    { const u32x4 one = {0x3f803f80u, 0x3f803f80u, 0x3f803f80u, 0x3f803f80u};
      const u32x4 va = __builtin_bit_cast(u32x4, lds128(lds + aV)), vb = __builtin_bit_cast(u32x4, lds128(lds + aV + 512));
      VA0 = blk ? one : va; VB0 = blk ? one : vb; VA1 = one; VB1 = one;
      SA = MFMA(lds128(lds + l16), Q, NM);
      K = __builtin_bit_cast(u32x4, lds128(lds + 2048 + l16)); }
    f32x16 d00 = {}, d01 = {}, d10 = {}, d11 = {};
    bf16x8 wo0a, wo0b, wo1a, wo1b, wo2a, wo2b, wo3a, wo3b;
    u32x4 h0, h1;
    const int fs0 = w < 6 ? 11 * w : 66 + 10 * (w - 6), nfs = w < 6 ? 11 : 10;
    const char* bh = ws + WS_HF + (size_t)(2 * rt64 * 86 + fs0) * 1024;
    const char* bwd = gwd + (size_t)fs0 * 16 * 1024 + ct64 * 128;
    asm volatile("s_nop 7\n\ts_nop 3" : "+v"(SA), "+v"(K), "+v"(VA0), "+v"(VB0));
#pragma unroll 1
    for (int seg = 0; seg < 16; ++seg) {
        if (seg == 2) {
            STAMP(9);
            WAITVM(0); BAR();
            if (tid == 0) __hip_atomic_store((gu64*)(ws + WS_F2 + (rb * 16 + cg) * 8), tag, RLX_AGENT);
            wo0a = trfrag(wta, 1024); wo0b = trfrag(wtb, 1024); wo1a = trfrag(wta + 2048, 1024); wo1b = trfrag(wtb + 2048, 1024);
            asm volatile("s_waitcnt lgkmcnt(0)" : "+v"(wo0a), "+v"(wo0b), "+v"(wo1a), "+v"(wo1b)::"memory");
#pragma unroll
            for (int n = 0; n < 4; ++n) glds(vws, bwo + (4 + n) * 8192, wsl + n * 1024);
        }
        if (seg == 4) {
            WAITVM(0);
            wo2a = trfrag(wta, 1024); wo2b = trfrag(wtb, 1024); wo3a = trfrag(wta + 2048, 1024); wo3b = trfrag(wtb + 2048, 1024);
            asm volatile("s_waitcnt lgkmcnt(0)" : "+v"(wo2a), "+v"(wo2b), "+v"(wo3a), "+v"(wo3b)::"memory");
            wait_tags(ws, WS_F2 + ((rt64 >> 1) * 16 + (lane & 15)) * 8, tag);
            if (b == 0 && w == 0) {
                wait_tags(ws, WS_F1 + lane * 8, tag); wait_tags(ws, WS_F1 + 512 + lane * 8, tag);
                wait_tags(ws, WS_F1 + 1024 + lane * 8, tag); wait_tags(ws, WS_F1 + 1536 + lane * 8, tag);
                if (lane == 0) __hip_atomic_store((gu64*)(ws + WS_EPOCH), tag, RLX_AGENT);
            }
        }
        if (seg == 4) STAMP(10);
        if (seg >= 5 && seg - 5 < nfs) {
            asm volatile("s_waitcnt vmcnt(0)" : "+v"(h0), "+v"(h1)::"memory");
            const bf16x8 wa = trfrag(wta, 1024), wb = trfrag(wtb, 1024);
            d00 = MFMA(wa, __builtin_bit_cast(bf16x8, h0), d00); d01 = MFMA(wb, __builtin_bit_cast(bf16x8, h0), d01);
            d10 = MFMA(wa, __builtin_bit_cast(bf16x8, h1), d10); d11 = MFMA(wb, __builtin_bit_cast(bf16x8, h1), d11);
            asm volatile("" : "+v"(d00), "+v"(d01), "+v"(d10), "+v"(d11));
        }
        if (seg >= 4 && seg - 4 < nfs) {
            LOAD_DEV(h0, l16, bh + (seg - 4) * 1024); LOAD_DEV(h1, l16, bh + (86 + seg - 4) * 1024);
            glds(vws, bwd + (seg - 4) * 16384, wsl); glds(vws, bwd + (seg - 4) * 16384 + 8192, wsl + 1024);
        }
        asm volatile(
            TILE(160, 176, 232, 244, 240, 4096, 2048, 2560)
            TILE(176, 160, 240, 236, 232, 6144, 4096, 4608)
            TILE(160, 176, 232, 244, 240, 8192, 6144, 6656)
            TILE(176, 160, 240, 236, 232, 10240, 8192, 8704)
            "v_add_u32 %[aK], 0x2000, %[aK]\n\tv_add_u32 %[aV], 0x2000, %[aV]"
            : "+{v[160:175]}"(SA), "+{v[176:191]}"(SB), "+{v[192:207]}"(O), "+{v[224:227]}"(K), "+{v[232:235]}"(VA0), "+{v[236:239]}"(VB0),
              "+{v[240:243]}"(VA1), "+{v[244:247]}"(VB1), "+{v[248:251]}"(PA), "+{v[252:255]}"(PB), [aK] "+v"(aK), [aV] "+v"(aV)
            : "{v[228:231]}"(Q), "{v[208:223]}"(NM), [msk] "s"(msk)
            : "memory");
    }
    STAMP(11);
    asm volatile("s_nop 1" : "+v"(PB));
    O = MFMA(__builtin_bit_cast(bf16x8, VB1), __builtin_bit_cast(bf16x8, PB), O);
    {
      float chk = fabsf(O[8]);
#pragma unroll
      for (int e = 0; e < 8; ++e) chk = fmaxf(chk, fabsf(O[e])) + 0.f * O[e];
      if (__builtin_expect(__any(!(chk < 3.0e38f)), 0)) {
          const f32x16 z = {}; float m = -INFINITY;
#pragma unroll 1
          for (int t = 0; t < 64; ++t) { const f32x16 s = MFMA(lds128(lds + t * 2048 + l16), Q, z);
#pragma unroll
              for (int e = 0; e < 16; ++e) m = fmaxf(m, s[e]); }
          m = -swapmax(m);
#pragma unroll
          for (int e = 0; e < 16; ++e) NM[e] = m;
          O = z;
          const u32x4 one = {0x3f803f80u, 0x3f803f80u, 0x3f803f80u, 0x3f803f80u};
          const unsigned av = 1024 + hh * 256 + (lane & 15) * 16;
#pragma unroll 1
          for (int t = 0; t < 64; ++t) { f32x16 s = MFMA(lds128(lds + t * 2048 + l16), Q, NM);
#pragma unroll
              for (int e = 0; e < 16; ++e) s[e] = __builtin_amdgcn_exp2f(s[e]);
              u32x4 pa, pb;
#pragma unroll
              for (int e = 0; e < 4; ++e) { pa[e] = cvtpk(s[2 * e], s[2 * e + 1]); pb[e] = cvtpk(s[8 + 2 * e], s[9 + 2 * e]); }
              const u32x4 va = __builtin_bit_cast(u32x4, lds128(lds + t * 2048 + av)), vb = __builtin_bit_cast(u32x4, lds128(lds + t * 2048 + av + 512));
              O = MFMA(__builtin_bit_cast(bf16x8, blk ? one : va), __builtin_bit_cast(bf16x8, pa), O);
              O = MFMA(__builtin_bit_cast(bf16x8, blk ? one : vb), __builtin_bit_cast(bf16x8, pb), O); }
      } }
    { const float il = __builtin_amdgcn_rcpf(O[8]);
      const u32x4 oq = {cvtpk(O[0] * il, O[1] * il), cvtpk(O[2] * il, O[3] * il), cvtpk(O[4] * il, O[5] * il), cvtpk(O[6] * il, O[7] * il)};
      __builtin_amdgcn_raw_buffer_store_b128(oq, wsr, WS_OF + ((qb * 8 + w) * 32 + head) * 1024 + l16, 0, 16);
      WAITVM(0);
      if (lane == 0) __hip_atomic_store((gu64*)(ws + WS_F3 + ((qb * 8 + w) * 32 + head) * 8), tag, RLX_AGENT); }
    STAMP(12);
    const int orow = tid >> 3, oc = tid & 7;
    const size_t ooff = (size_t)(rt64 * 64 + orow) * 1024 + ct64 * 128 + oc * 16;
    const u32x4 xres = *(const u32x4*)(gx + ooff);
    const lds_t pw = lds + w * 16384 + r * 256;
    const lds_t pr = lds + orow * 256;
    auto put = [&](const f32x16& v, int rtile, int ctile) {
#pragma unroll
        for (int q4 = 0; q4 < 4; ++q4)
            *(__attribute__((address_space(3))) f32x4*)(pw + rtile * 8192 + (((ctile * 8 + q4 * 2 + hh) ^ (r & 7)) << 4)) = f32x4{v[4 * q4], v[4 * q4 + 1], v[4 * q4 + 2], v[4 * q4 + 3]};
    };
    auto gather = [&](f32x4& lo, f32x4& hi) {
        lo = f32x4{}; hi = f32x4{};
#pragma unroll
        for (int p = 0; p < 8; ++p) { lo += lds128f(pr + p * 16384 + (((2 * oc) ^ (orow & 7)) << 4)); hi += lds128f(pr + p * 16384 + (((2 * oc + 1) ^ (orow & 7)) << 4)); }
    };
    BAR();
    put(d00, 0, 0); put(d01, 0, 1); put(d10, 1, 0); put(d11, 1, 1);
    BAR();
    f32x4 glo, ghi; gather(glo, ghi);
#pragma unroll
    for (int e = 0; e < 4; ++e) { glo[e] *= __builtin_amdgcn_rcpf(1.0f + __builtin_amdgcn_exp2f(glo[e] * -1.4426950408889634f));
                                  ghi[e] *= __builtin_amdgcn_rcpf(1.0f + __builtin_amdgcn_exp2f(ghi[e] * -1.4426950408889634f)); }
    asm volatile("" : "+v"(glo), "+v"(ghi));
    STAMP(13);
    { int l3 = __builtin_amdgcn_mbcnt_hi(~0u, __builtin_amdgcn_mbcnt_lo(~0u, 0u)); asm volatile("" : "+v"(l3));
      wait_tags(ws, WS_F3 + ((2 * rt64 + ((l3 >> 2) & 1)) * 32 + 4 * w + (l3 & 3)) * 8, tag); }
    STAMP(14);
    { const char* bo = ws + WS_OF + (size_t)(2 * rt64 * 32 + 4 * w) * 1024;
      u32x4 o00, o01, o02, o03, o10, o11, o12, o13;
      LOAD_DEV(o00, l16, bo); LOAD_DEV(o10, l16, bo + 32768); LOAD_DEV(o01, l16, bo + 1024); LOAD_DEV(o11, l16, bo + 32768 + 1024);
      LOAD_DEV(o02, l16, bo + 2048); LOAD_DEV(o12, l16, bo + 32768 + 2048); LOAD_DEV(o03, l16, bo + 3072); LOAD_DEV(o13, l16, bo + 32768 + 3072);
      const f32x16 z = {};
      asm volatile("s_waitcnt vmcnt(6)" : "+v"(o00), "+v"(o10)::"memory");
      d00 = MFMA(wo0a, __builtin_bit_cast(bf16x8, o00), z); d01 = MFMA(wo0b, __builtin_bit_cast(bf16x8, o00), z);
      d10 = MFMA(wo0a, __builtin_bit_cast(bf16x8, o10), z); d11 = MFMA(wo0b, __builtin_bit_cast(bf16x8, o10), z);
      asm volatile("s_waitcnt vmcnt(4)" : "+v"(o01), "+v"(o11), "+v"(d00)::"memory");
      d00 = MFMA(wo1a, __builtin_bit_cast(bf16x8, o01), d00); d01 = MFMA(wo1b, __builtin_bit_cast(bf16x8, o01), d01);
      d10 = MFMA(wo1a, __builtin_bit_cast(bf16x8, o11), d10); d11 = MFMA(wo1b, __builtin_bit_cast(bf16x8, o11), d11);
      asm volatile("s_waitcnt vmcnt(2)" : "+v"(o02), "+v"(o12), "+v"(d00)::"memory");
      d00 = MFMA(wo2a, __builtin_bit_cast(bf16x8, o02), d00); d01 = MFMA(wo2b, __builtin_bit_cast(bf16x8, o02), d01);
      d10 = MFMA(wo2a, __builtin_bit_cast(bf16x8, o12), d10); d11 = MFMA(wo2b, __builtin_bit_cast(bf16x8, o12), d11);
      asm volatile("s_waitcnt vmcnt(0)" : "+v"(o03), "+v"(o13), "+v"(d00)::"memory");
      d00 = MFMA(wo3a, __builtin_bit_cast(bf16x8, o03), d00); d01 = MFMA(wo3b, __builtin_bit_cast(bf16x8, o03), d01);
      d10 = MFMA(wo3a, __builtin_bit_cast(bf16x8, o13), d10); d11 = MFMA(wo3b, __builtin_bit_cast(bf16x8, o13), d11); }
    BAR();
    put(d00, 0, 0); put(d01, 0, 1); put(d10, 1, 0); put(d11, 1, 1);
    BAR();
    f32x4 slo, shi; gather(slo, shi);
    u32x4 res;
#pragma unroll
    for (int e = 0; e < 2; ++e) {
        res[e] = cvtpk(__builtin_fmaf(slo[2 * e], glo[2 * e], __uint_as_float(xres[e] << 16)), __builtin_fmaf(slo[2 * e + 1], glo[2 * e + 1], __uint_as_float(xres[e] & 0xffff0000u)));
        res[2 + e] = cvtpk(__builtin_fmaf(shi[2 * e], ghi[2 * e], __uint_as_float(xres[2 + e] << 16)), __builtin_fmaf(shi[2 * e + 1], ghi[2 * e + 1], __uint_as_float(xres[2 + e] & 0xffff0000u)));
    }
    *(u32x4*)(gout + ooff) = res;
    STAMP(15);
    STAMP_FLUSH;
#undef lane
#undef r
#undef hh
#undef blk
#undef tq
#undef tp
#undef tid
}

extern "C" void kernel_launch(void* const* d_in, const int* in_sizes, int n_in, void* d_out, int out_size, void* d_ws, size_t ws_size,
                              hipStream_t stream) {
    (void)hipFuncSetAttribute((const void*)fused_block, hipFuncAttributeMaxDynamicSharedMemorySize, LDS_BYTES);
    fused_block<<<256, 512, LDS_BYTES, stream>>>((const char*)d_in[0], (const char*)d_in[1], (const char*)d_in[2], (const char*)d_in[4],
                                                 (const char*)d_in[5], (const char*)d_in[6], (char*)d_out, (char*)d_ws);
}
```

```cpp
#define PF_CODE
#define PADREF 2
#include <hip/hip_runtime.h>
#include <stdint.h>

typedef __attribute__((ext_vector_type(8))) short bf16x8;
typedef __attribute__((ext_vector_type(4))) short s16x4;
typedef __attribute__((ext_vector_type(2))) __bf16 bf2;
typedef __attribute__((ext_vector_type(16))) float f32x16;
typedef __attribute__((ext_vector_type(4))) float f32x4;
typedef __attribute__((ext_vector_type(2))) float f32x2;
typedef __attribute__((ext_vector_type(4))) unsigned u32x4;
typedef unsigned long long u64;
typedef __attribute__((address_space(3))) char* lds_t;
typedef __attribute__((address_space(1))) u64 gu64;

template <int N> struct IC { static constexpr int value = N; };
template <bool B> struct BC { static constexpr bool value = B; };
#define DI __device__ __forceinline__
#define MFMA(a, b, c) __builtin_amdgcn_mfma_f32_32x32x16_bf16((a), (b), (c), 0, 0, 0)
#define RLX_AGENT __ATOMIC_RELAXED, __HIP_MEMORY_SCOPE_AGENT

constexpr unsigned WS_EPOCH = 0;
constexpr unsigned WS_F1 = 4096;
constexpr unsigned WS_F2 = 8192;
constexpr unsigned WS_F3 = 16384;
constexpr unsigned WS_QF = 65536;
constexpr unsigned WS_KV = WS_QF + (2u << 20);
constexpr unsigned WS_HF = WS_KV + (4u << 20);
constexpr unsigned WS_OF = WS_HF + 64u * 86u * 1024u;
constexpr unsigned WS_END = WS_OF + (2u << 20);
constexpr unsigned SLOT = 28672, XIMG = 0, WIMG = 16384, GIMG = 0, UIMG = 12288 + 32;
constexpr unsigned L_STAT = 153600;
constexpr unsigned L_RSTD = L_STAT + 1024;
constexpr unsigned L_CSQ = L_RSTD + 512;
constexpr unsigned L_CSG = L_CSQ;
constexpr unsigned L_WSL = 131072;
constexpr unsigned LDS_BYTES = 163840;
constexpr float QSCALE = 0.25f * 1.4426950408889634f;
constexpr float M_OFF = 64.0f;

DI unsigned cvtpk(float lo, float hi) { return __builtin_bit_cast(unsigned, __builtin_convertvector(f32x2{lo, hi}, bf2)); }
DI float dot2(unsigned a, unsigned b, float c) { return __builtin_amdgcn_fdot2_f32_bf16(__builtin_bit_cast(bf2, a), __builtin_bit_cast(bf2, b), c, false); }
DI void glds(unsigned voff, const char* sbase, unsigned ldst) {
    asm volatile("s_mov_b32 m0, %2\n\ts_nop 0\n\tglobal_load_lds_dwordx4 %0, %1" ::"v"(voff), "s"(sbase), "s"(ldst) : "memory"); }
DI void glds_dev(unsigned voff, const char* sbase, unsigned ldst) {
    asm volatile("s_mov_b32 m0, %2\n\ts_nop 0\n\tglobal_load_lds_dwordx4 %0, %1 sc1" ::"v"(voff), "s"(sbase), "s"(ldst) : "memory"); }
#define LOAD_DEV(dst, voff, sbase) asm volatile("global_load_dwordx4 %0, %1, %2 sc1" : "=v"(dst) : "v"(voff), "s"(sbase) : "memory")
#define WAITVM(n) asm volatile("s_waitcnt vmcnt(" #n ")" ::: "memory")
#define BAR() asm volatile("s_waitcnt lgkmcnt(0)\n\ts_barrier" ::: "memory")
template <int N> DI void waitvm() { asm volatile("s_waitcnt vmcnt(%0)" ::"i"(N) : "memory"); }
DI bf16x8 lds128(lds_t p) { return *(const __attribute__((address_space(3))) bf16x8*)p; }
DI f32x4 lds128f(lds_t p) { return *(const __attribute__((address_space(3))) f32x4*)p; }
DI s16x4 ldstr(lds_t p) { return __builtin_amdgcn_ds_read_tr16_b64_v4i16((__attribute__((address_space(3))) s16x4*)p); }
DI bf16x8 trfrag(lds_t p, int step) { s16x4 a = ldstr(p), b = ldstr(p + step); return __builtin_shufflevector(a, b, 0, 1, 2, 3, 4, 5, 6, 7); }
DI float swapadd(float v) { auto r = __builtin_amdgcn_permlane32_swap(__float_as_uint(v), __float_as_uint(v), false, false); return __uint_as_float(r[0]) + __uint_as_float(r[1]); }
DI float swapmax(float v) { auto r = __builtin_amdgcn_permlane32_swap(__float_as_uint(v), __float_as_uint(v), false, false); return fmaxf(__uint_as_float(r[0]), __uint_as_float(r[1])); }
DI float frag_sum(bf16x8 f, float acc) { u32x4 u = __builtin_bit_cast(u32x4, f); acc = dot2(u[0], 0x3f803f80u, acc); acc = dot2(u[1], 0x3f803f80u, acc); acc = dot2(u[2], 0x3f803f80u, acc); return dot2(u[3], 0x3f803f80u, acc); }
DI bf16x8 split_a(float v) { unsigned h = cvtpk(v, v); float lo = v - __uint_as_float(h << 16); u32x4 u = {h, cvtpk(lo, lo), 0u, 0u}; return __builtin_bit_cast(bf16x8, u); }
DI bf16x8 split_b(float v) { unsigned h = cvtpk(v, v); float lo = v - __uint_as_float(h << 16); unsigned p = cvtpk(v, lo); u32x4 u = {p, p, 0u, 0u}; return __builtin_bit_cast(bf16x8, u); }

#define STAMP(n) do { if ((n) == 0) asm volatile("s_memrealtime %0\n\ts_waitcnt lgkmcnt(0)" : "=s"(stamps[n]) :: "memory"); \
    if ((n) == 4 && blockIdx.x == PADREF && threadIdx.x == 0) { __hip_atomic_store((gu64*)(ws + 1024), stamps[0], RLX_AGENT); asm volatile("s_waitcnt vmcnt(0)" ::: "memory"); } } while (0)
#define STAMP_DECL u64 stamps[16];
#define STAMP_FLUSH do { u64 tgt_ = __hip_atomic_load((gu64*)(ws + 1024), RLX_AGENT) + 8000; u64 n_ = __builtin_amdgcn_s_memrealtime(); if (tgt_ > n_ + 8000) tgt_ = n_; \
    while (__builtin_amdgcn_s_memrealtime() < tgt_) __builtin_amdgcn_s_sleep(1); } while (0)
DI void wait_tags(const char* ws, unsigned off, u64 tag) {
    const u64 t0 = __builtin_amdgcn_s_memrealtime();
    for (;;) {
        u64 v = __hip_atomic_load((gu64*)(ws + off), RLX_AGENT);
        if (__all(v == tag)) break;
        __builtin_amdgcn_s_sleep(1);
        if (__builtin_amdgcn_s_memrealtime() - t0 > 2000000ull) break;
    }
    asm volatile("" ::: "memory");
}

#define EX(S, i) "v_exp_f32 v[" #S "+" #i "], v[" #S "+" #i "]\n\t"
#define CV(S, p, i, j) "v_cvt_pk_bf16_f32 v[248+" #p "], v[" #S "+" #i "], v[" #S "+" #j "]\n\t"
#define TILE(SC, SN, VAc, VBp, VAl, KOFF, VOFF0, VOFF1) \
    "v_mfma_f32_32x32x16_bf16 v[" #SN ":" #SN "+15], v[224:227], v[228:231], v[208:223]\n\t" \
    EX(SC, 0) EX(SC, 1) EX(SC, 2) CV(SC, 0, 0, 1) EX(SC, 3) EX(SC, 4) CV(SC, 1, 2, 3) EX(SC, 5) \
    "v_mfma_f32_32x32x16_bf16 v[192:207], v[" #VBp ":" #VBp "+3], v[252:255], v[192:207]\n\t" \
    "ds_read_b128 v[224:227], %[aK] offset:" #KOFF "\n\t" \
    "s_mov_b64 exec, %[msk]\n\t" \
    "ds_read_b128 v[" #VAl ":" #VAl "+3], %[aV] offset:" #VOFF0 "\n\t" \
    "ds_read_b128 v[" #VBp ":" #VBp "+3], %[aV] offset:" #VOFF1 "\n\t" \
    "s_mov_b64 exec, -1\n\t" \
    EX(SC, 6) CV(SC, 2, 4, 5) EX(SC, 7) EX(SC, 8) CV(SC, 3, 6, 7) EX(SC, 9) EX(SC, 10) \
    "v_mfma_f32_32x32x16_bf16 v[192:207], v[" #VAc ":" #VAc "+3], v[248:251], v[192:207]\n\t" \
    CV(SC, 4, 8, 9) EX(SC, 11) EX(SC, 12) CV(SC, 5, 10, 11) EX(SC, 13) EX(SC, 14) CV(SC, 6, 12, 13) EX(SC, 15) \
    "s_waitcnt lgkmcnt(0)\n\t" \
    CV(SC, 7, 14, 15)

extern "C" __global__ void __launch_bounds__(512, 2)
fused_block(const char* __restrict__ gx, const char* __restrict__ gwqkv, const char* __restrict__ gwo, const char* __restrict__ gwg,
            const char* __restrict__ gwu, const char* __restrict__ gwd, char* __restrict__ gout, char* ws) {
    extern __shared__ __attribute__((aligned(16))) char smem[];
    const lds_t lds = (lds_t)smem;
    STAMP_DECL STAMP(0);
    const int tid = threadIdx.x, lane = tid & 63, r = lane & 31, hh = lane >> 5;
    const int w = __builtin_amdgcn_readfirstlane(tid >> 6), wr = w & 3, g = w >> 2;
    const int b = blockIdx.x, xcd = b & 7, bi = b >> 3;
    u64 epoch;
    asm volatile("s_load_dwordx2 %0, %1, 0x0" : "=s"(epoch) : "s"(ws) : "memory");
    const __amdgpu_buffer_rsrc_t wsr = __builtin_amdgcn_make_buffer_rsrc(ws, 0, WS_END, 0x00020000);

    const int rb = 4 * (xcd >> 1) + (bi >> 3), cg = 8 * (xcd & 1) + (bi & 7);
    const int nft = cg < 6 ? 6 : 5, ft0 = cg < 6 ? 6 * cg : 36 + 5 * (cg - 6);
    unsigned vx0, vx1, vw0, vw1, vg0, vg1, vg2;
    { int row = 16 * w + (lane >> 3); vx0 = row * 1024 + (((lane & 7) ^ ((row >> 1) & 7)) << 4);
      row += 8; vx1 = row * 1024 + (((lane & 7) ^ ((row >> 1) & 7)) << 4);
      int o = w * 1024 + lane * 16; vw0 = (o / 192) * 3072 + o % 192;
      o = ((w & 3) + 8) * 1024 + lane * 16; vw1 = (o / 192) * 3072 + o % 192;
      const int lim = nft * 32;
      o = w * 1024 + lane * 16; int cb = o % 192; vg0 = (o / 192) * 2752 + (cb >= lim ? cb - 32 : cb);
      o = (w < 4 ? w + 8 : w - 4) * 1024 + lane * 16; cb = o % 192; vg1 = (o / 192) * 2752 + (cb >= lim ? cb - 32 : cb);
      o = (w + 4) * 1024 + lane * 16; cb = o % 192; vg2 = (o / 192) * 2752 + (cb >= lim ? cb - 32 : cb); }
    const char* bx = gx + (size_t)rb * 128 * 1024;
    const char* bw = gwqkv + cg * 192;
    const char* bg0 = gwg + ft0 * 32;
    const char* bg1 = (w < 4 ? gwg : gwu) + ft0 * 32;
    const char* bg2 = gwu + ft0 * 32;
    const unsigned dg1 = w < 4 ? GIMG + (w + 8) * 1024 : UIMG + (w - 4) * 1024;
    auto issue = [&](int c, int gg) {
        const unsigned s = (c % 5) * SLOT;
        if (c < 8) {
            glds(vx0, bx + c * 128, s + XIMG + w * 2048);
            glds(vx1, bx + c * 128, s + XIMG + w * 2048 + 1024);
            glds(vw0, bw + c * (64 * 3072), s + WIMG + w * 1024);
            if (gg == 0) glds(vw1, bw + c * (64 * 3072), s + WIMG + (w + 8) * 1024);
        } else {
            glds(vg0, bg0 + (c - 8) * (64 * 2752), s + GIMG + w * 1024);
            glds(vg1, bg1 + (c - 8) * (64 * 2752), s + dg1);
            glds(vg2, bg2 + (c - 8) * (64 * 2752), s + UIMG + (w + 4) * 1024);
        }
    };
#pragma unroll
    for (int c = 0; c < 5; ++c) issue(c, g);
#define TOUCH(voff, base) asm volatile("s_mov_b32 m0, %2\n\ts_nop 0\n\tglobal_load_lds_dword %0, %1" ::"v"(voff), "s"(base), "s"(163840u - 256u) : "memory")
    { const char* pc; asm volatile("s_getpc_b64 %0" : "=s"(pc));
      const unsigned o = tid * 128;
      if (o < 39000) TOUCH(o, pc); }

    const int xrow = 32 * wr + r, blk = (lane >> 4) & 1, tq = (lane & 15) >> 2, tp = lane & 3;
    const unsigned xa = xrow * 128 + ((hh ^ ((xrow >> 1) & 7)) << 4);
    const lds_t wqa = lds + WIMG + (8 * hh + tq) * 192 + blk * 32 + tp * 8 + (g ? 128 : 0);
    const lds_t wga = lds + (blk ? UIMG : GIMG) + (8 * hh + tq) * 192 + tp * 8 + g * 96;
    const int ct0 = 3 * cg + (g ? 2 : 0);

    bf16x8 xf[32];
    f32x16 a0 = {}, a1 = {}, a2 = {};
    float cs0 = 0.f, cs1 = 0.f, cs2 = 0.f, sx = 0.f, sq = 0.f;
    auto qkv_loop = [&](auto gconst, auto vconst) {
        constexpr int G = decltype(gconst)::value; constexpr bool V = decltype(vconst)::value;
#pragma unroll
        for (int i = 0; i < 8; ++i) {
            if (G == 0) { switch (i) { case 0: WAITVM(16); break; case 5: WAITVM(11); break; case 6: WAITVM(10); break; case 7: WAITVM(9); break; default: WAITVM(12); } }
            else { if (i == 0) WAITVM(12); else WAITVM(9); }
            BAR();
            if (i == 0) STAMP(1);
            if (i == 4) STAMP(2);
            if (i >= 1) issue(i + 4, G);
            const unsigned s = (i % 5) * SLOT;
#pragma unroll
            for (int ks = 0; ks < 4; ++ks) {
                const bf16x8 x = lds128(lds + s + XIMG + (xa ^ (ks << 5)));
                xf[4 * i + ks] = x;
                const bf16x8 w0 = trfrag(wqa + s + ks * 16 * 192, 4 * 192);
                a0 = V ? MFMA(x, w0, a0) : MFMA(w0, x, a0);
                if (G == 0) { const bf16x8 w1 = trfrag(wqa + s + ks * 16 * 192 + 64, 4 * 192); a1 = V ? MFMA(x, w1, a1) : MFMA(w1, x, a1); }
                else { u32x4 u = __builtin_bit_cast(u32x4, x);
#pragma unroll
                    for (int e = 0; e < 4; ++e) { sx = dot2(u[e], 0x3f803f80u, sx); sq = dot2(u[e], u[e], sq); } }
            }
            cs0 = frag_sum(trfrag(wqa + s + wr * 16 * 192, 4 * 192), cs0);
            if (G == 0) cs1 = frag_sum(trfrag(wqa + s + wr * 16 * 192 + 64, 4 * 192), cs1);
            asm volatile("" : "+v"(cs0), "+v"(cs1), "+v"(sx), "+v"(sq));
        }
    };
    if (g == 0) { if (ct0 >= 32) qkv_loop(IC<0>{}, BC<true>{}); else qkv_loop(IC<0>{}, BC<false>{}); }
    else { if (ct0 >= 32) qkv_loop(IC<1>{}, BC<true>{}); else qkv_loop(IC<1>{}, BC<false>{}); }
    asm volatile("" : "+s"(epoch));
    const u64 tag = epoch + 0x9E3779B97F4A7C15ull;
    *(__attribute__((address_space(3))) float*)(lds + L_CSQ + (g ? 2 : 0) * 1024 + lane * 16 + wr * 4) = cs0;
    if (g == 0) *(__attribute__((address_space(3))) float*)(lds + L_CSQ + 1024 + lane * 16 + wr * 4) = cs1;
    else {
        sx = swapadd(sx); sq = swapadd(sq);
        const float mean = sx * (1.f / 512.f), var = fmaxf(sq * (1.f / 512.f) - mean * mean, 0.f), rs = __builtin_amdgcn_rsqf(var + 1e-5f);
        if (hh == 0) { *(__attribute__((address_space(3))) f32x2*)(lds + L_STAT + xrow * 8) = f32x2{mean, rs};
                       *(__attribute__((address_space(3))) float*)(lds + L_RSTD + xrow * 4) = rs; }
    }
    float rstd; bf16x8 mfrag;
    const int rt = rb * 4 + wr;
#pragma unroll
    for (int i = 8; i < 16; ++i) {
        switch (i) { case 9: WAITVM(11); break; case 10: WAITVM(6); break; case 13: WAITVM(6); break; case 14: WAITVM(3); break; case 15: WAITVM(0); break; default: WAITVM(9); }
        BAR();
        if (i == 8) STAMP(3);
        if (i == 9) STAMP(4);
        if (i == 8) {
            const f32x2 st = *(const __attribute__((address_space(3))) f32x2*)(lds + L_STAT + xrow * 8);
            rstd = st[1]; mfrag = split_b(-st[0]);
            for (int t = 0; t < 2 - g; ++t) {
                const int ct = ct0 + t, hp = ct & 15, j = g ? 2 : t;
                const f32x4 p = lds128f(lds + L_CSQ + j * 1024 + lane * 16);
                const bf16x8 cf = split_a((p[0] + p[1]) + (p[2] + p[3]));
                u32x4 lo, hi; unsigned o0, o1;
                if (ct >= 32) {
                    a0 = MFMA(mfrag, cf, a0);
#pragma unroll
                    for (int q4 = 0; q4 < 4; ++q4) { const f32x4 rs = lds128f(lds + L_RSTD + (32 * wr + 8 * q4 + 4 * hh) * 4);
#pragma unroll
                        for (int e = 0; e < 4; ++e) a0[4 * q4 + e] *= rs[e]; }
                    o0 = WS_KV + ((2 * hp + blk) * 64 + rt) * 2048 + 1024 + hh * 256 + (lane & 15) * 16; o1 = o0 + 512;
                } else {
                    a0 = MFMA(cf, mfrag, a0);
                    const float sc = ct < 16 ? rstd * QSCALE : rstd;
#pragma unroll
                    for (int e = 0; e < 16; ++e) a0[e] *= sc;
                    o0 = (ct < 16 ? WS_QF + (2 * hp * 64 + rt) * 1024 : WS_KV + (2 * hp * 64 + rt) * 2048) + lane * 16;
                    o1 = o0 + (ct < 16 ? 64 * 1024 : 64 * 2048);
                }
#pragma unroll
                for (int e = 0; e < 4; ++e) { lo[e] = cvtpk(a0[2 * e], a0[2 * e + 1]); hi[e] = cvtpk(a0[8 + 2 * e], a0[9 + 2 * e]); }
                __builtin_amdgcn_raw_buffer_store_b128(lo, wsr, o0, 0, 16);
                __builtin_amdgcn_raw_buffer_store_b128(hi, wsr, o1, 0, 16);
                a0 = a1;
            }
            a0 = f32x16{}; a1 = f32x16{}; cs0 = 0.f; cs1 = 0.f;
            asm volatile("" ::: "memory");
        }
        if (i == 10 && tid == 0) __hip_atomic_store((gu64*)(ws + WS_F1 + (rb * 16 + cg) * 8), tag, RLX_AGENT);
        if (i + 4 < 16) issue(i + 4, 0);
        const unsigned s = (i % 5) * SLOT;
#pragma unroll
        for (int ks = 0; ks < 4; ++ks) {
            const bf16x8 w0 = trfrag(wga + s + ks * 16 * 192, 4 * 192), w1 = trfrag(wga + s + ks * 16 * 192 + 32, 4 * 192), w2 = trfrag(wga + s + ks * 16 * 192 + 64, 4 * 192);
            a0 = MFMA(w0, xf[4 * (i - 8) + ks], a0); a1 = MFMA(w1, xf[4 * (i - 8) + ks], a1); a2 = MFMA(w2, xf[4 * (i - 8) + ks], a2);
        }
        cs0 = frag_sum(trfrag(wga + s + wr * 16 * 192, 4 * 192), cs0); cs1 = frag_sum(trfrag(wga + s + wr * 16 * 192 + 32, 4 * 192), cs1);
        cs2 = frag_sum(trfrag(wga + s + wr * 16 * 192 + 64, 4 * 192), cs2);
        asm volatile("" : "+v"(cs0), "+v"(cs1), "+v"(cs2));
    }
    *(__attribute__((address_space(3))) float*)(lds + L_CSG + (3 * g + 0) * 1024 + lane * 16 + wr * 4) = cs0;
    *(__attribute__((address_space(3))) float*)(lds + L_CSG + (3 * g + 1) * 1024 + lane * 16 + wr * 4) = cs1;
    *(__attribute__((address_space(3))) float*)(lds + L_CSG + (3 * g + 2) * 1024 + lane * 16 + wr * 4) = cs2;

    int lane2 = __builtin_amdgcn_mbcnt_hi(~0u, __builtin_amdgcn_mbcnt_lo(~0u, 0u));
    asm volatile("" : "+v"(lane2));
#define lane lane2
    const int r2 = lane & 31, hh2 = lane >> 5, blk2 = (lane >> 4) & 1, tq2 = (lane & 15) >> 2, tp2 = lane & 3, tid2 = w * 64 + lane;
#define r r2
#define hh hh2
#define blk blk2
#define tq tq2
#define tp tp2
#define tid tid2
    STAMP(5);
    const int head = 4 * xcd + (bi & 3), qb = bi >> 2;
    if (w == 0) {
        const int hp = head >> 1;
        const int frb = lane < 32 ? (lane & 15) : 2 * qb + (lane & 1), fct = lane < 16 ? 16 + hp : lane < 32 ? 32 + hp : hp;
        wait_tags(ws, WS_F1 + (frb * 16 + fct / 3) * 8, tag);
    }
    BAR();
    STAMP(6);
    u32x4 qf;
    const unsigned l16 = lane * 16;
    LOAD_DEV(qf, l16, ws + WS_QF + (head * 64 + qb * 8 + w) * 1024);
    { const char* kv = ws + WS_KV + head * 131072 + w * 2048;
#pragma unroll
      for (int n = 0; n < 8; ++n) { glds_dev(l16, kv + n * 16384, n * 16384 + w * 2048); glds_dev(l16, kv + n * 16384 + 1024, n * 16384 + w * 2048 + 1024); } }
#pragma unroll 1
    for (int t = 0; t < 3; ++t) {
        const f32x4 p = lds128f(lds + L_CSG + (3 * g + t) * 1024 + lane * 16);
        a0 = MFMA(split_a((p[0] + p[1]) + (p[2] + p[3])), mfrag, a0);
        float hv[8];
#pragma unroll
        for (int e = 0; e < 8; ++e) {
            const float gg = a0[e] * rstd, uu = a0[8 + e] * rstd;
            const float z = gg * __builtin_fmaf(gg * gg, -2.0f * 0.7978845608028654f * 0.044715f * 1.4426950408889634f, -2.0f * 0.7978845608028654f * 1.4426950408889634f);
            hv[e] = gg * uu * __builtin_amdgcn_rcpf(1.0f + __builtin_amdgcn_exp2f(z));
        }
        const u32x4 hq = {cvtpk(hv[0], hv[1]), cvtpk(hv[2], hv[3]), cvtpk(hv[4], hv[5]), cvtpk(hv[6], hv[7])};
        if (3 * g + t < nft) __builtin_amdgcn_raw_buffer_store_b128(hq, wsr, WS_HF + (rt * 86 + ft0 + 3 * g + t) * 1024 + l16, 0, 16);
        a0 = a1; a1 = a2;
    }

    const int rt64 = 4 * xcd + (bi >> 3), ct64 = bi & 7;
    const unsigned vws = (lane >> 3) * 1024 + (((lane & 7) ^ (((lane >> 4) & 1) << 2)) << 4);
    const unsigned wsl = L_WSL + w * 4096;
    const lds_t wta = lds + wsl + (4 * hh + tq) * 128 + ((tq >> 1) & 1) * 64 + blk * 32 + tp * 8;
    const lds_t wtb = lds + wsl + (4 * hh + tq) * 128 + (((tq >> 1) & 1) ^ 1) * 64 + blk * 32 + tp * 8;
    const char* bwo = gwo + (size_t)w * 64 * 1024 + ct64 * 128;
    STAMP(7);
    asm volatile("s_waitcnt vmcnt(16)" : "+v"(qf)::"memory");
    BAR();
    STAMP(8);
#pragma unroll
    for (int n = 0; n < 4; ++n) glds(vws, bwo + n * 8192, wsl + n * 1024);
    const bf16x8 Q = __builtin_bit_cast(bf16x8, qf);
    f32x16 NM, SA, SB, O = {};
    { const f32x16 z = {};
      SA = MFMA(lds128(lds + l16), Q, z); SB = MFMA(lds128(lds + 2048 + l16), Q, z);
      float m = fmaxf(SA[0], SB[0]);
#pragma unroll
      for (int e = 1; e < 16; ++e) m = fmaxf(fmaxf(m, SA[e]), SB[e]);
      m = -(swapmax(m) + M_OFF);
#pragma unroll
      for (int e = 0; e < 16; ++e) NM[e] = m; }
    unsigned aK = l16, aV = 1024 + hh * 256 + (lane & 15) * 16;
    const u64 msk = 0x0000ffff0000ffffull;
    u32x4 K, VA0, VB0, VA1, VB1, PA = {}, PB = {};
    { const u32x4 one = {0x3f803f80u, 0x3f803f80u, 0x3f803f80u, 0x3f803f80u};
      const u32x4 va = __builtin_bit_cast(u32x4, lds128(lds + aV)), vb = __builtin_bit_cast(u32x4, lds128(lds + aV + 512));
      VA0 = blk ? one : va; VB0 = blk ? one : vb; VA1 = one; VB1 = one;
      SA = MFMA(lds128(lds + l16), Q, NM);
      K = __builtin_bit_cast(u32x4, lds128(lds + 2048 + l16)); }
    f32x16 d00 = {}, d01 = {}, d10 = {}, d11 = {};
    bf16x8 wo0a, wo0b, wo1a, wo1b, wo2a, wo2b, wo3a, wo3b;
    u32x4 h0, h1;
    const int fs0 = w < 6 ? 11 * w : 66 + 10 * (w - 6), nfs = w < 6 ? 11 : 10;
    const char* bh = ws + WS_HF + (size_t)(2 * rt64 * 86 + fs0) * 1024;
    const char* bwd = gwd + (size_t)fs0 * 16 * 1024 + ct64 * 128;
    asm volatile("s_nop 7\n\ts_nop 3" : "+v"(SA), "+v"(K), "+v"(VA0), "+v"(VB0));
#pragma unroll 1
    for (int seg = 0; seg < 16; ++seg) {
        if (seg == 2) {
            STAMP(9);
            WAITVM(0); BAR();
            if (tid == 0) __hip_atomic_store((gu64*)(ws + WS_F2 + (rb * 16 + cg) * 8), tag, RLX_AGENT);
            wo0a = trfrag(wta, 1024); wo0b = trfrag(wtb, 1024); wo1a = trfrag(wta + 2048, 1024); wo1b = trfrag(wtb + 2048, 1024);
            asm volatile("s_waitcnt lgkmcnt(0)" : "+v"(wo0a), "+v"(wo0b), "+v"(wo1a), "+v"(wo1b)::"memory");
#pragma unroll
            for (int n = 0; n < 4; ++n) glds(vws, bwo + (4 + n) * 8192, wsl + n * 1024);
        }
        if (seg == 4) {
            WAITVM(0);
            wo2a = trfrag(wta, 1024); wo2b = trfrag(wtb, 1024); wo3a = trfrag(wta + 2048, 1024); wo3b = trfrag(wtb + 2048, 1024);
            asm volatile("s_waitcnt lgkmcnt(0)" : "+v"(wo2a), "+v"(wo2b), "+v"(wo3a), "+v"(wo3b)::"memory");
            wait_tags(ws, WS_F2 + ((rt64 >> 1) * 16 + (lane & 15)) * 8, tag);
            if (b == 0 && w == 0) {
                wait_tags(ws, WS_F1 + lane * 8, tag); wait_tags(ws, WS_F1 + 512 + lane * 8, tag);
                wait_tags(ws, WS_F1 + 1024 + lane * 8, tag); wait_tags(ws, WS_F1 + 1536 + lane * 8, tag);
                if (lane == 0) __hip_atomic_store((gu64*)(ws + WS_EPOCH), tag, RLX_AGENT);
            }
        }
        if (seg == 4) STAMP(10);
        if (seg >= 5 && seg - 5 < nfs) {
            asm volatile("s_waitcnt vmcnt(0)" : "+v"(h0), "+v"(h1)::"memory");
            const bf16x8 wa = trfrag(wta, 1024), wb = trfrag(wtb, 1024);
            d00 = MFMA(wa, __builtin_bit_cast(bf16x8, h0), d00); d01 = MFMA(wb, __builtin_bit_cast(bf16x8, h0), d01);
            d10 = MFMA(wa, __builtin_bit_cast(bf16x8, h1), d10); d11 = MFMA(wb, __builtin_bit_cast(bf16x8, h1), d11);
            asm volatile("" : "+v"(d00), "+v"(d01), "+v"(d10), "+v"(d11));
        }
        if (seg >= 4 && seg - 4 < nfs) {
            LOAD_DEV(h0, l16, bh + (seg - 4) * 1024); LOAD_DEV(h1, l16, bh + (86 + seg - 4) * 1024);
            glds(vws, bwd + (seg - 4) * 16384, wsl); glds(vws, bwd + (seg - 4) * 16384 + 8192, wsl + 1024);
        }
        asm volatile(
            TILE(160, 176, 232, 244, 240, 4096, 2048, 2560)
            TILE(176, 160, 240, 236, 232, 6144, 4096, 4608)
            TILE(160, 176, 232, 244, 240, 8192, 6144, 6656)
            TILE(176, 160, 240, 236, 232, 10240, 8192, 8704)
            "v_add_u32 %[aK], 0x2000, %[aK]\n\tv_add_u32 %[aV], 0x2000, %[aV]"
            : "+{v[160:175]}"(SA), "+{v[176:191]}"(SB), "+{v[192:207]}"(O), "+{v[224:227]}"(K), "+{v[232:235]}"(VA0), "+{v[236:239]}"(VB0),
              "+{v[240:243]}"(VA1), "+{v[244:247]}"(VB1), "+{v[248:251]}"(PA), "+{v[252:255]}"(PB), [aK] "+v"(aK), [aV] "+v"(aV)
            : "{v[228:231]}"(Q), "{v[208:223]}"(NM), [msk] "s"(msk)
            : "memory");
    }
    STAMP(11);
    asm volatile("s_nop 1" : "+v"(PB));
    O = MFMA(__builtin_bit_cast(bf16x8, VB1), __builtin_bit_cast(bf16x8, PB), O);
    {
      float chk = fabsf(O[8]);
#pragma unroll
      for (int e = 0; e < 8; ++e) chk = fmaxf(chk, fabsf(O[e])) + 0.f * O[e];
      if (__builtin_expect(__any(!(chk < 3.0e38f)), 0)) {
          const f32x16 z = {}; float m = -INFINITY;
#pragma unroll 1
          for (int t = 0; t < 64; ++t) { const f32x16 s = MFMA(lds128(lds + t * 2048 + l16), Q, z);
#pragma unroll
              for (int e = 0; e < 16; ++e) m = fmaxf(m, s[e]); }
          m = -swapmax(m);
#pragma unroll
          for (int e = 0; e < 16; ++e) NM[e] = m;
          O = z;
          const u32x4 one = {0x3f803f80u, 0x3f803f80u, 0x3f803f80u, 0x3f803f80u};
          const unsigned av = 1024 + hh * 256 + (lane & 15) * 16;
#pragma unroll 1
          for (int t = 0; t < 64; ++t) { f32x16 s = MFMA(lds128(lds + t * 2048 + l16), Q, NM);
#pragma unroll
              for (int e = 0; e < 16; ++e) s[e] = __builtin_amdgcn_exp2f(s[e]);
              u32x4 pa, pb;
#pragma unroll
              for (int e = 0; e < 4; ++e) { pa[e] = cvtpk(s[2 * e], s[2 * e + 1]); pb[e] = cvtpk(s[8 + 2 * e], s[9 + 2 * e]); }
              const u32x4 va = __builtin_bit_cast(u32x4, lds128(lds + t * 2048 + av)), vb = __builtin_bit_cast(u32x4, lds128(lds + t * 2048 + av + 512));
              O = MFMA(__builtin_bit_cast(bf16x8, blk ? one : va), __builtin_bit_cast(bf16x8, pa), O);
              O = MFMA(__builtin_bit_cast(bf16x8, blk ? one : vb), __builtin_bit_cast(bf16x8, pb), O); }
      } }
    { const float il = __builtin_amdgcn_rcpf(O[8]);
      const u32x4 oq = {cvtpk(O[0] * il, O[1] * il), cvtpk(O[2] * il, O[3] * il), cvtpk(O[4] * il, O[5] * il), cvtpk(O[6] * il, O[7] * il)};
      __builtin_amdgcn_raw_buffer_store_b128(oq, wsr, WS_OF + ((qb * 8 + w) * 32 + head) * 1024 + l16, 0, 16);
      WAITVM(0);
      if (lane == 0) __hip_atomic_store((gu64*)(ws + WS_F3 + ((qb * 8 + w) * 32 + head) * 8), tag, RLX_AGENT); }
    STAMP(12);
    const int orow = tid >> 3, oc = tid & 7;
    const size_t ooff = (size_t)(rt64 * 64 + orow) * 1024 + ct64 * 128 + oc * 16;
    const u32x4 xres = *(const u32x4*)(gx + ooff);
    const lds_t pw = lds + w * 16384 + r * 256;
    const lds_t pr = lds + orow * 256;
    auto put = [&](const f32x16& v, int rtile, int ctile) {
#pragma unroll
        for (int q4 = 0; q4 < 4; ++q4)
            *(__attribute__((address_space(3))) f32x4*)(pw + rtile * 8192 + (((ctile * 8 + q4 * 2 + hh) ^ (r & 7)) << 4)) = f32x4{v[4 * q4], v[4 * q4 + 1], v[4 * q4 + 2], v[4 * q4 + 3]};
    };
    auto gather = [&](f32x4& lo, f32x4& hi) {
        lo = f32x4{}; hi = f32x4{};
#pragma unroll
        for (int p = 0; p < 8; ++p) { lo += lds128f(pr + p * 16384 + (((2 * oc) ^ (orow & 7)) << 4)); hi += lds128f(pr + p * 16384 + (((2 * oc + 1) ^ (orow & 7)) << 4)); }
    };
    BAR();
    put(d00, 0, 0); put(d01, 0, 1); put(d10, 1, 0); put(d11, 1, 1);
    BAR();
    f32x4 glo, ghi; gather(glo, ghi);
#pragma unroll
    for (int e = 0; e < 4; ++e) { glo[e] *= __builtin_amdgcn_rcpf(1.0f + __builtin_amdgcn_exp2f(glo[e] * -1.4426950408889634f));
                                  ghi[e] *= __builtin_amdgcn_rcpf(1.0f + __builtin_amdgcn_exp2f(ghi[e] * -1.4426950408889634f)); }
    asm volatile("" : "+v"(glo), "+v"(ghi));
    STAMP(13);
    { int l3 = __builtin_amdgcn_mbcnt_hi(~0u, __builtin_amdgcn_mbcnt_lo(~0u, 0u)); asm volatile("" : "+v"(l3));
      wait_tags(ws, WS_F3 + ((2 * rt64 + ((l3 >> 2) & 1)) * 32 + 4 * w + (l3 & 3)) * 8, tag); }
    STAMP(14);
    { const char* bo = ws + WS_OF + (size_t)(2 * rt64 * 32 + 4 * w) * 1024;
      u32x4 o00, o01, o02, o03, o10, o11, o12, o13;
      LOAD_DEV(o00, l16, bo); LOAD_DEV(o10, l16, bo + 32768); LOAD_DEV(o01, l16, bo + 1024); LOAD_DEV(o11, l16, bo + 32768 + 1024);
      LOAD_DEV(o02, l16, bo + 2048); LOAD_DEV(o12, l16, bo + 32768 + 2048); LOAD_DEV(o03, l16, bo + 3072); LOAD_DEV(o13, l16, bo + 32768 + 3072);
      const f32x16 z = {};
      asm volatile("s_waitcnt vmcnt(6)" : "+v"(o00), "+v"(o10)::"memory");
      d00 = MFMA(wo0a, __builtin_bit_cast(bf16x8, o00), z); d01 = MFMA(wo0b, __builtin_bit_cast(bf16x8, o00), z);
      d10 = MFMA(wo0a, __builtin_bit_cast(bf16x8, o10), z); d11 = MFMA(wo0b, __builtin_bit_cast(bf16x8, o10), z);
      asm volatile("s_waitcnt vmcnt(4)" : "+v"(o01), "+v"(o11), "+v"(d00)::"memory");
      d00 = MFMA(wo1a, __builtin_bit_cast(bf16x8, o01), d00); d01 = MFMA(wo1b, __builtin_bit_cast(bf16x8, o01), d01);
      d10 = MFMA(wo1a, __builtin_bit_cast(bf16x8, o11), d10); d11 = MFMA(wo1b, __builtin_bit_cast(bf16x8, o11), d11);
      asm volatile("s_waitcnt vmcnt(2)" : "+v"(o02), "+v"(o12), "+v"(d00)::"memory");
      d00 = MFMA(wo2a, __builtin_bit_cast(bf16x8, o02), d00); d01 = MFMA(wo2b, __builtin_bit_cast(bf16x8, o02), d01);
      d10 = MFMA(wo2a, __builtin_bit_cast(bf16x8, o12), d10); d11 = MFMA(wo2b, __builtin_bit_cast(bf16x8, o12), d11);
      asm volatile("s_waitcnt vmcnt(0)" : "+v"(o03), "+v"(o13), "+v"(d00)::"memory");
      d00 = MFMA(wo3a, __builtin_bit_cast(bf16x8, o03), d00); d01 = MFMA(wo3b, __builtin_bit_cast(bf16x8, o03), d01);
      d10 = MFMA(wo3a, __builtin_bit_cast(bf16x8, o13), d10); d11 = MFMA(wo3b, __builtin_bit_cast(bf16x8, o13), d11); }
    BAR();
    put(d00, 0, 0); put(d01, 0, 1); put(d10, 1, 0); put(d11, 1, 1);
    BAR();
    f32x4 slo, shi; gather(slo, shi);
    u32x4 res;
#pragma unroll
    for (int e = 0; e < 2; ++e) {
        res[e] = cvtpk(__builtin_fmaf(slo[2 * e], glo[2 * e], __uint_as_float(xres[e] << 16)), __builtin_fmaf(slo[2 * e + 1], glo[2 * e + 1], __uint_as_float(xres[e] & 0xffff0000u)));
        res[2 + e] = cvtpk(__builtin_fmaf(shi[2 * e], ghi[2 * e], __uint_as_float(xres[2 + e] << 16)), __builtin_fmaf(shi[2 * e + 1], ghi[2 * e + 1], __uint_as_float(xres[2 + e] & 0xffff0000u)));
    }
    *(u32x4*)(gout + ooff) = res;
    STAMP(15);
    STAMP_FLUSH;
#undef lane
#undef r
#undef hh
#undef blk
#undef tq
#undef tp
#undef tid
}

extern "C" void kernel_launch(void* const* d_in, const int* in_sizes, int n_in, void* d_out, int out_size, void* d_ws, size_t ws_size,
                              hipStream_t stream) {
    (void)hipFuncSetAttribute((const void*)fused_block, hipFuncAttributeMaxDynamicSharedMemorySize, LDS_BYTES);
    fused_block<<<256, 512, LDS_BYTES, stream>>>((const char*)d_in[0], (const char*)d_in[1], (const char*)d_in[2], (const char*)d_in[4],
                                                 (const char*)d_in[5], (const char*)d_in[6], (char*)d_out, (char*)d_ws);
}
```

```cpp
#define PF_CODE
#define OUT_AUX 17
#include <hip/hip_runtime.h>
#include <stdint.h>

typedef __attribute__((ext_vector_type(8))) short bf16x8;
typedef __attribute__((ext_vector_type(4))) short s16x4;
typedef __attribute__((ext_vector_type(2))) __bf16 bf2;
typedef __attribute__((ext_vector_type(16))) float f32x16;
typedef __attribute__((ext_vector_type(4))) float f32x4;
typedef __attribute__((ext_vector_type(2))) float f32x2;
typedef __attribute__((ext_vector_type(4))) unsigned u32x4;
typedef unsigned long long u64;
typedef __attribute__((address_space(3))) char* lds_t;
typedef __attribute__((address_space(1))) u64 gu64;

template <int N> struct IC { static constexpr int value = N; };
template <bool B> struct BC { static constexpr bool value = B; };
#define DI __device__ __forceinline__
#define MFMA(a, b, c) __builtin_amdgcn_mfma_f32_32x32x16_bf16((a), (b), (c), 0, 0, 0)
#define RLX_AGENT __ATOMIC_RELAXED, __HIP_MEMORY_SCOPE_AGENT

constexpr unsigned WS_EPOCH = 0;
constexpr unsigned WS_F1 = 4096;
constexpr unsigned WS_F2 = 8192;
constexpr unsigned WS_F3 = 16384;
constexpr unsigned WS_QF = 65536;
constexpr unsigned WS_KV = WS_QF + (2u << 20);
constexpr unsigned WS_HF = WS_KV + (4u << 20);
constexpr unsigned WS_OF = WS_HF + 64u * 86u * 1024u;
constexpr unsigned WS_END = WS_OF + (2u << 20);
constexpr unsigned SLOT = 28672, XIMG = 0, WIMG = 16384, GIMG = 0, UIMG = 12288 + 32;
constexpr unsigned L_STAT = 153600;
constexpr unsigned L_RSTD = L_STAT + 1024;
constexpr unsigned L_CSQ = L_RSTD + 512;
constexpr unsigned L_CSG = L_CSQ;
constexpr unsigned L_WSL = 131072;
constexpr unsigned LDS_BYTES = 163840;
constexpr float QSCALE = 0.25f * 1.4426950408889634f;
constexpr float M_OFF = 64.0f;

DI unsigned cvtpk(float lo, float hi) { return __builtin_bit_cast(unsigned, __builtin_convertvector(f32x2{lo, hi}, bf2)); }
DI float dot2(unsigned a, unsigned b, float c) { return __builtin_amdgcn_fdot2_f32_bf16(__builtin_bit_cast(bf2, a), __builtin_bit_cast(bf2, b), c, false); }
DI void glds(unsigned voff, const char* sbase, unsigned ldst) {
    asm volatile("s_mov_b32 m0, %2\n\ts_nop 0\n\tglobal_load_lds_dwordx4 %0, %1" ::"v"(voff), "s"(sbase), "s"(ldst) : "memory"); }
DI void glds_dev(unsigned voff, const char* sbase, unsigned ldst) {
    asm volatile("s_mov_b32 m0, %2\n\ts_nop 0\n\tglobal_load_lds_dwordx4 %0, %1 sc1" ::"v"(voff), "s"(sbase), "s"(ldst) : "memory"); }
#define LOAD_DEV(dst, voff, sbase) asm volatile("global_load_dwordx4 %0, %1, %2 sc1" : "=v"(dst) : "v"(voff), "s"(sbase) : "memory")
#define WAITVM(n) asm volatile("s_waitcnt vmcnt(" #n ")" ::: "memory")
#define BAR() asm volatile("s_waitcnt lgkmcnt(0)\n\ts_barrier" ::: "memory")
template <int N> DI void waitvm() { asm volatile("s_waitcnt vmcnt(%0)" ::"i"(N) : "memory"); }
DI bf16x8 lds128(lds_t p) { return *(const __attribute__((address_space(3))) bf16x8*)p; }
DI f32x4 lds128f(lds_t p) { return *(const __attribute__((address_space(3))) f32x4*)p; }
DI s16x4 ldstr(lds_t p) { return __builtin_amdgcn_ds_read_tr16_b64_v4i16((__attribute__((address_space(3))) s16x4*)p); }
DI bf16x8 trfrag(lds_t p, int step) { s16x4 a = ldstr(p), b = ldstr(p + step); return __builtin_shufflevector(a, b, 0, 1, 2, 3, 4, 5, 6, 7); }
DI float swapadd(float v) { auto r = __builtin_amdgcn_permlane32_swap(__float_as_uint(v), __float_as_uint(v), false, false); return __uint_as_float(r[0]) + __uint_as_float(r[1]); }
DI float swapmax(float v) { auto r = __builtin_amdgcn_permlane32_swap(__float_as_uint(v), __float_as_uint(v), false, false); return fmaxf(__uint_as_float(r[0]), __uint_as_float(r[1])); }
DI float frag_sum(bf16x8 f, float acc) { u32x4 u = __builtin_bit_cast(u32x4, f); acc = dot2(u[0], 0x3f803f80u, acc); acc = dot2(u[1], 0x3f803f80u, acc); acc = dot2(u[2], 0x3f803f80u, acc); return dot2(u[3], 0x3f803f80u, acc); }
DI bf16x8 split_a(float v) { unsigned h = cvtpk(v, v); float lo = v - __uint_as_float(h << 16); u32x4 u = {h, cvtpk(lo, lo), 0u, 0u}; return __builtin_bit_cast(bf16x8, u); }
DI bf16x8 split_b(float v) { unsigned h = cvtpk(v, v); float lo = v - __uint_as_float(h << 16); unsigned p = cvtpk(v, lo); u32x4 u = {p, p, 0u, 0u}; return __builtin_bit_cast(bf16x8, u); }

#define STAMP(n)
#define STAMP_DECL
#define STAMP_FLUSH
DI void wait_tags(const char* ws, unsigned off, u64 tag) {
    const u64 t0 = __builtin_amdgcn_s_memrealtime();
    for (;;) {
        u64 v = __hip_atomic_load((gu64*)(ws + off), RLX_AGENT);
        if (__all(v == tag)) break;
        __builtin_amdgcn_s_sleep(1);
        if (__builtin_amdgcn_s_memrealtime() - t0 > 2000000ull) break;
    }
    asm volatile("" ::: "memory");
}

#define EX(S, i) "v_exp_f32 v[" #S "+" #i "], v[" #S "+" #i "]\n\t"
#define CV(S, p, i, j) "v_cvt_pk_bf16_f32 v[248+" #p "], v[" #S "+" #i "], v[" #S "+" #j "]\n\t"
#define TILE(SC, SN, VAc, VBp, VAl, KOFF, VOFF0, VOFF1) \
    "v_mfma_f32_32x32x16_bf16 v[" #SN ":" #SN "+15], v[224:227], v[228:231], v[208:223]\n\t" \
    EX(SC, 0) EX(SC, 1) EX(SC, 2) CV(SC, 0, 0, 1) EX(SC, 3) EX(SC, 4) CV(SC, 1, 2, 3) EX(SC, 5) \
    "v_mfma_f32_32x32x16_bf16 v[192:207], v[" #VBp ":" #VBp "+3], v[252:255], v[192:207]\n\t" \
    "ds_read_b128 v[224:227], %[aK] offset:" #KOFF "\n\t" \
    "s_mov_b64 exec, %[msk]\n\t" \
    "ds_read_b128 v[" #VAl ":" #VAl "+3], %[aV] offset:" #VOFF0 "\n\t" \
    "ds_read_b128 v[" #VBp ":" #VBp "+3], %[aV] offset:" #VOFF1 "\n\t" \
    "s_mov_b64 exec, -1\n\t" \
    EX(SC, 6) CV(SC, 2, 4, 5) EX(SC, 7) EX(SC, 8) CV(SC, 3, 6, 7) EX(SC, 9) EX(SC, 10) \
    "v_mfma_f32_32x32x16_bf16 v[192:207], v[" #VAc ":" #VAc "+3], v[248:251], v[192:207]\n\t" \
    CV(SC, 4, 8, 9) EX(SC, 11) EX(SC, 12) CV(SC, 5, 10, 11) EX(SC, 13) EX(SC, 14) CV(SC, 6, 12, 13) EX(SC, 15) \
    "s_waitcnt lgkmcnt(0)\n\t" \
    CV(SC, 7, 14, 15)

extern "C" __global__ void __launch_bounds__(512, 2)
fused_block(const char* __restrict__ gx, const char* __restrict__ gwqkv, const char* __restrict__ gwo, const char* __restrict__ gwg,
            const char* __restrict__ gwu, const char* __restrict__ gwd, char* __restrict__ gout, char* ws) {
    extern __shared__ __attribute__((aligned(16))) char smem[];
    const lds_t lds = (lds_t)smem;
    STAMP_DECL STAMP(0);
    const int tid = threadIdx.x, lane = tid & 63, r = lane & 31, hh = lane >> 5;
    const int w = __builtin_amdgcn_readfirstlane(tid >> 6), wr = w & 3, g = w >> 2;
    const int b = blockIdx.x, xcd = b & 7, bi = b >> 3;
    u64 epoch;
    asm volatile("s_load_dwordx2 %0, %1, 0x0" : "=s"(epoch) : "s"(ws) : "memory");
    const __amdgpu_buffer_rsrc_t wsr = __builtin_amdgcn_make_buffer_rsrc(ws, 0, WS_END, 0x00020000);

    const int rb = 4 * (xcd >> 1) + (bi >> 3), cg = 8 * (xcd & 1) + (bi & 7);
    const int nft = cg < 6 ? 6 : 5, ft0 = cg < 6 ? 6 * cg : 36 + 5 * (cg - 6);
    unsigned vx0, vx1, vw0, vw1, vg0, vg1, vg2;
    { int row = 16 * w + (lane >> 3); vx0 = row * 1024 + (((lane & 7) ^ ((row >> 1) & 7)) << 4);
      row += 8; vx1 = row * 1024 + (((lane & 7) ^ ((row >> 1) & 7)) << 4);
      int o = w * 1024 + lane * 16; vw0 = (o / 192) * 3072 + o % 192;
      o = ((w & 3) + 8) * 1024 + lane * 16; vw1 = (o / 192) * 3072 + o % 192;
      const int lim = nft * 32;
      o = w * 1024 + lane * 16; int cb = o % 192; vg0 = (o / 192) * 2752 + (cb >= lim ? cb - 32 : cb);
      o = (w < 4 ? w + 8 : w - 4) * 1024 + lane * 16; cb = o % 192; vg1 = (o / 192) * 2752 + (cb >= lim ? cb - 32 : cb);
      o = (w + 4) * 1024 + lane * 16; cb = o % 192; vg2 = (o / 192) * 2752 + (cb >= lim ? cb - 32 : cb); }
    const char* bx = gx + (size_t)rb * 128 * 1024;
    const char* bw = gwqkv + cg * 192;
    const char* bg0 = gwg + ft0 * 32;
    const char* bg1 = (w < 4 ? gwg : gwu) + ft0 * 32;
    const char* bg2 = gwu + ft0 * 32;
    const unsigned dg1 = w < 4 ? GIMG + (w + 8) * 1024 : UIMG + (w - 4) * 1024;
    auto issue = [&](int c, int gg) {
        const unsigned s = (c % 5) * SLOT;
        if (c < 8) {
            glds(vx0, bx + c * 128, s + XIMG + w * 2048);
            glds(vx1, bx + c * 128, s + XIMG + w * 2048 + 1024);
            glds(vw0, bw + c * (64 * 3072), s + WIMG + w * 1024);
            if (gg == 0) glds(vw1, bw + c * (64 * 3072), s + WIMG + (w + 8) * 1024);
        } else {
            glds(vg0, bg0 + (c - 8) * (64 * 2752), s + GIMG + w * 1024);
            glds(vg1, bg1 + (c - 8) * (64 * 2752), s + dg1);
            glds(vg2, bg2 + (c - 8) * (64 * 2752), s + UIMG + (w + 4) * 1024);
        }
    };
#pragma unroll
    for (int c = 0; c < 5; ++c) issue(c, g);
#define TOUCH(voff, base) asm volatile("s_mov_b32 m0, %2\n\ts_nop 0\n\tglobal_load_lds_dword %0, %1" ::"v"(voff), "s"(base), "s"(163840u - 256u) : "memory")
    { const char* pc; asm volatile("s_getpc_b64 %0" : "=s"(pc));
      const unsigned o = tid * 128;
      if (o < 39000) TOUCH(o, pc); }

    const int xrow = 32 * wr + r, blk = (lane >> 4) & 1, tq = (lane & 15) >> 2, tp = lane & 3;
    const unsigned xa = xrow * 128 + ((hh ^ ((xrow >> 1) & 7)) << 4);
    const lds_t wqa = lds + WIMG + (8 * hh + tq) * 192 + blk * 32 + tp * 8 + (g ? 128 : 0);
    const lds_t wga = lds + (blk ? UIMG : GIMG) + (8 * hh + tq) * 192 + tp * 8 + g * 96;
    const int ct0 = 3 * cg + (g ? 2 : 0);

    bf16x8 xf[32];
    f32x16 a0 = {}, a1 = {}, a2 = {};
    float cs0 = 0.f, cs1 = 0.f, cs2 = 0.f, sx = 0.f, sq = 0.f;
    auto qkv_loop = [&](auto gconst, auto vconst) {
        constexpr int G = decltype(gconst)::value; constexpr bool V = decltype(vconst)::value;
#pragma unroll
        for (int i = 0; i < 8; ++i) {
            if (G == 0) { switch (i) { case 0: WAITVM(16); break; case 5: WAITVM(11); break; case 6: WAITVM(10); break; case 7: WAITVM(9); break; default: WAITVM(12); } }
            else { if (i == 0) WAITVM(12); else WAITVM(9); }
            BAR();
            if (i == 0) STAMP(1);
            if (i == 4) STAMP(2);
            if (i >= 1) issue(i + 4, G);
            const unsigned s = (i % 5) * SLOT;
#pragma unroll
            for (int ks = 0; ks < 4; ++ks) {
                const bf16x8 x = lds128(lds + s + XIMG + (xa ^ (ks << 5)));
                xf[4 * i + ks] = x;
                const bf16x8 w0 = trfrag(wqa + s + ks * 16 * 192, 4 * 192);
                a0 = V ? MFMA(x, w0, a0) : MFMA(w0, x, a0);
                if (G == 0) { const bf16x8 w1 = trfrag(wqa + s + ks * 16 * 192 + 64, 4 * 192); a1 = V ? MFMA(x, w1, a1) : MFMA(w1, x, a1); }
                else { u32x4 u = __builtin_bit_cast(u32x4, x);
#pragma unroll
                    for (int e = 0; e < 4; ++e) { sx = dot2(u[e], 0x3f803f80u, sx); sq = dot2(u[e], u[e], sq); } }
            }
            cs0 = frag_sum(trfrag(wqa + s + wr * 16 * 192, 4 * 192), cs0);
            if (G == 0) cs1 = frag_sum(trfrag(wqa + s + wr * 16 * 192 + 64, 4 * 192), cs1);
            asm volatile("" : "+v"(cs0), "+v"(cs1), "+v"(sx), "+v"(sq));
        }
    };
    if (g == 0) { if (ct0 >= 32) qkv_loop(IC<0>{}, BC<true>{}); else qkv_loop(IC<0>{}, BC<false>{}); }
    else { if (ct0 >= 32) qkv_loop(IC<1>{}, BC<true>{}); else qkv_loop(IC<1>{}, BC<false>{}); }
    asm volatile("" : "+s"(epoch));
    const u64 tag = epoch + 0x9E3779B97F4A7C15ull;
    *(__attribute__((address_space(3))) float*)(lds + L_CSQ + (g ? 2 : 0) * 1024 + lane * 16 + wr * 4) = cs0;
    if (g == 0) *(__attribute__((address_space(3))) float*)(lds + L_CSQ + 1024 + lane * 16 + wr * 4) = cs1;
    else {
        sx = swapadd(sx); sq = swapadd(sq);
        const float mean = sx * (1.f / 512.f), var = fmaxf(sq * (1.f / 512.f) - mean * mean, 0.f), rs = __builtin_amdgcn_rsqf(var + 1e-5f);
        if (hh == 0) { *(__attribute__((address_space(3))) f32x2*)(lds + L_STAT + xrow * 8) = f32x2{mean, rs};
                       *(__attribute__((address_space(3))) float*)(lds + L_RSTD + xrow * 4) = rs; }
    }
    float rstd; bf16x8 mfrag;
    const int rt = rb * 4 + wr;
#pragma unroll
    for (int i = 8; i < 16; ++i) {
        switch (i) { case 9: WAITVM(11); break; case 10: WAITVM(6); break; case 13: WAITVM(6); break; case 14: WAITVM(3); break; case 15: WAITVM(0); break; default: WAITVM(9); }
        BAR();
        if (i == 8) STAMP(3);
        if (i == 9) STAMP(4);
        if (i == 8) {
            const f32x2 st = *(const __attribute__((address_space(3))) f32x2*)(lds + L_STAT + xrow * 8);
            rstd = st[1]; mfrag = split_b(-st[0]);
            for (int t = 0; t < 2 - g; ++t) {
                const int ct = ct0 + t, hp = ct & 15, j = g ? 2 : t;
                const f32x4 p = lds128f(lds + L_CSQ + j * 1024 + lane * 16);
                const bf16x8 cf = split_a((p[0] + p[1]) + (p[2] + p[3]));
                u32x4 lo, hi; unsigned o0, o1;
                if (ct >= 32) {
                    a0 = MFMA(mfrag, cf, a0);
#pragma unroll
                    for (int q4 = 0; q4 < 4; ++q4) { const f32x4 rs = lds128f(lds + L_RSTD + (32 * wr + 8 * q4 + 4 * hh) * 4);
#pragma unroll
                        for (int e = 0; e < 4; ++e) a0[4 * q4 + e] *= rs[e]; }
                    o0 = WS_KV + ((2 * hp + blk) * 64 + rt) * 2048 + 1024 + hh * 256 + (lane & 15) * 16; o1 = o0 + 512;
                } else {
                    a0 = MFMA(cf, mfrag, a0);
                    const float sc = ct < 16 ? rstd * QSCALE : rstd;
#pragma unroll
                    for (int e = 0; e < 16; ++e) a0[e] *= sc;
                    o0 = (ct < 16 ? WS_QF + (2 * hp * 64 + rt) * 1024 : WS_KV + (2 * hp * 64 + rt) * 2048) + lane * 16;
                    o1 = o0 + (ct < 16 ? 64 * 1024 : 64 * 2048);
                }
#pragma unroll
                for (int e = 0; e < 4; ++e) { lo[e] = cvtpk(a0[2 * e], a0[2 * e + 1]); hi[e] = cvtpk(a0[8 + 2 * e], a0[9 + 2 * e]); }
                __builtin_amdgcn_raw_buffer_store_b128(lo, wsr, o0, 0, 16);
                __builtin_amdgcn_raw_buffer_store_b128(hi, wsr, o1, 0, 16);
                a0 = a1;
            }
            a0 = f32x16{}; a1 = f32x16{}; cs0 = 0.f; cs1 = 0.f;
            asm volatile("" ::: "memory");
        }
        if (i == 10 && tid == 0) __hip_atomic_store((gu64*)(ws + WS_F1 + (rb * 16 + cg) * 8), tag, RLX_AGENT);
        if (i + 4 < 16) issue(i + 4, 0);
        const unsigned s = (i % 5) * SLOT;
#pragma unroll
        for (int ks = 0; ks < 4; ++ks) {
            const bf16x8 w0 = trfrag(wga + s + ks * 16 * 192, 4 * 192), w1 = trfrag(wga + s + ks * 16 * 192 + 32, 4 * 192), w2 = trfrag(wga + s + ks * 16 * 192 + 64, 4 * 192);
            a0 = MFMA(w0, xf[4 * (i - 8) + ks], a0); a1 = MFMA(w1, xf[4 * (i - 8) + ks], a1); a2 = MFMA(w2, xf[4 * (i - 8) + ks], a2);
        }
        cs0 = frag_sum(trfrag(wga + s + wr * 16 * 192, 4 * 192), cs0); cs1 = frag_sum(trfrag(wga + s + wr * 16 * 192 + 32, 4 * 192), cs1);
        cs2 = frag_sum(trfrag(wga + s + wr * 16 * 192 + 64, 4 * 192), cs2);
        asm volatile("" : "+v"(cs0), "+v"(cs1), "+v"(cs2));
    }
    *(__attribute__((address_space(3))) float*)(lds + L_CSG + (3 * g + 0) * 1024 + lane * 16 + wr * 4) = cs0;
    *(__attribute__((address_space(3))) float*)(lds + L_CSG + (3 * g + 1) * 1024 + lane * 16 + wr * 4) = cs1;
    *(__attribute__((address_space(3))) float*)(lds + L_CSG + (3 * g + 2) * 1024 + lane * 16 + wr * 4) = cs2;

    int lane2 = __builtin_amdgcn_mbcnt_hi(~0u, __builtin_amdgcn_mbcnt_lo(~0u, 0u));
    asm volatile("" : "+v"(lane2));
#define lane lane2
    const int r2 = lane & 31, hh2 = lane >> 5, blk2 = (lane >> 4) & 1, tq2 = (lane & 15) >> 2, tp2 = lane & 3, tid2 = w * 64 + lane;
#define r r2
#define hh hh2
#define blk blk2
#define tq tq2
#define tp tp2
#define tid tid2
    STAMP(5);
    const int head = 4 * xcd + (bi & 3), qb = bi >> 2;
    if (w == 0) {
        const int hp = head >> 1;
        const int frb = lane < 32 ? (lane & 15) : 2 * qb + (lane & 1), fct = lane < 16 ? 16 + hp : lane < 32 ? 32 + hp : hp;
        wait_tags(ws, WS_F1 + (frb * 16 + fct / 3) * 8, tag);
    }
    BAR();
    STAMP(6);
    u32x4 qf;
    const unsigned l16 = lane * 16;
    LOAD_DEV(qf, l16, ws + WS_QF + (head * 64 + qb * 8 + w) * 1024);
    { const char* kv = ws + WS_KV + head * 131072 + w * 2048;
#pragma unroll
      for (int n = 0; n < 8; ++n) { glds_dev(l16, kv + n * 16384, n * 16384 + w * 2048); glds_dev(l16, kv + n * 16384 + 1024, n * 16384 + w * 2048 + 1024); } }
#pragma unroll 1
    for (int t = 0; t < 3; ++t) {
        const f32x4 p = lds128f(lds + L_CSG + (3 * g + t) * 1024 + lane * 16);
        a0 = MFMA(split_a((p[0] + p[1]) + (p[2] + p[3])), mfrag, a0);
        float hv[8];
#pragma unroll
        for (int e = 0; e < 8; ++e) {
            const float gg = a0[e] * rstd, uu = a0[8 + e] * rstd;
            const float z = gg * __builtin_fmaf(gg * gg, -2.0f * 0.7978845608028654f * 0.044715f * 1.4426950408889634f, -2.0f * 0.7978845608028654f * 1.4426950408889634f);
            hv[e] = gg * uu * __builtin_amdgcn_rcpf(1.0f + __builtin_amdgcn_exp2f(z));
        }
        const u32x4 hq = {cvtpk(hv[0], hv[1]), cvtpk(hv[2], hv[3]), cvtpk(hv[4], hv[5]), cvtpk(hv[6], hv[7])};
        if (3 * g + t < nft) __builtin_amdgcn_raw_buffer_store_b128(hq, wsr, WS_HF + (rt * 86 + ft0 + 3 * g + t) * 1024 + l16, 0, 16);
        a0 = a1; a1 = a2;
    }

    const int rt64 = 4 * xcd + (bi >> 3), ct64 = bi & 7;
    const unsigned vws = (lane >> 3) * 1024 + (((lane & 7) ^ (((lane >> 4) & 1) << 2)) << 4);
    const unsigned wsl = L_WSL + w * 4096;
    const lds_t wta = lds + wsl + (4 * hh + tq) * 128 + ((tq >> 1) & 1) * 64 + blk * 32 + tp * 8;
    const lds_t wtb = lds + wsl + (4 * hh + tq) * 128 + (((tq >> 1) & 1) ^ 1) * 64 + blk * 32 + tp * 8;
    const char* bwo = gwo + (size_t)w * 64 * 1024 + ct64 * 128;
    STAMP(7);
    asm volatile("s_waitcnt vmcnt(16)" : "+v"(qf)::"memory");
    BAR();
    STAMP(8);
#pragma unroll
    for (int n = 0; n < 4; ++n) glds(vws, bwo + n * 8192, wsl + n * 1024);
    const bf16x8 Q = __builtin_bit_cast(bf16x8, qf);
    f32x16 NM, SA, SB, O = {};
    { const f32x16 z = {};
      SA = MFMA(lds128(lds + l16), Q, z); SB = MFMA(lds128(lds + 2048 + l16), Q, z);
      float m = fmaxf(SA[0], SB[0]);
#pragma unroll
      for (int e = 1; e < 16; ++e) m = fmaxf(fmaxf(m, SA[e]), SB[e]);
      m = -(swapmax(m) + M_OFF);
#pragma unroll
      for (int e = 0; e < 16; ++e) NM[e] = m; }
    unsigned aK = l16, aV = 1024 + hh * 256 + (lane & 15) * 16;
    const u64 msk = 0x0000ffff0000ffffull;
    u32x4 K, VA0, VB0, VA1, VB1, PA = {}, PB = {};
    { const u32x4 one = {0x3f803f80u, 0x3f803f80u, 0x3f803f80u, 0x3f803f80u};
      const u32x4 va = __builtin_bit_cast(u32x4, lds128(lds + aV)), vb = __builtin_bit_cast(u32x4, lds128(lds + aV + 512));
      VA0 = blk ? one : va; VB0 = blk ? one : vb; VA1 = one; VB1 = one;
      SA = MFMA(lds128(lds + l16), Q, NM);
      K = __builtin_bit_cast(u32x4, lds128(lds + 2048 + l16)); }
    f32x16 d00 = {}, d01 = {}, d10 = {}, d11 = {};
    bf16x8 wo0a, wo0b, wo1a, wo1b, wo2a, wo2b, wo3a, wo3b;
    u32x4 h0, h1;
    const int fs0 = w < 6 ? 11 * w : 66 + 10 * (w - 6), nfs = w < 6 ? 11 : 10;
    const char* bh = ws + WS_HF + (size_t)(2 * rt64 * 86 + fs0) * 1024;
    const char* bwd = gwd + (size_t)fs0 * 16 * 1024 + ct64 * 128;
    asm volatile("s_nop 7\n\ts_nop 3" : "+v"(SA), "+v"(K), "+v"(VA0), "+v"(VB0));
#pragma unroll 1
    for (int seg = 0; seg < 16; ++seg) {
        if (seg == 2) {
            STAMP(9);
            WAITVM(0); BAR();
            if (tid == 0) __hip_atomic_store((gu64*)(ws + WS_F2 + (rb * 16 + cg) * 8), tag, RLX_AGENT);
            wo0a = trfrag(wta, 1024); wo0b = trfrag(wtb, 1024); wo1a = trfrag(wta + 2048, 1024); wo1b = trfrag(wtb + 2048, 1024);
            asm volatile("s_waitcnt lgkmcnt(0)" : "+v"(wo0a), "+v"(wo0b), "+v"(wo1a), "+v"(wo1b)::"memory");
#pragma unroll
            for (int n = 0; n < 4; ++n) glds(vws, bwo + (4 + n) * 8192, wsl + n * 1024);
        }
        if (seg == 4) {
            WAITVM(0);
            wo2a = trfrag(wta, 1024); wo2b = trfrag(wtb, 1024); wo3a = trfrag(wta + 2048, 1024); wo3b = trfrag(wtb + 2048, 1024);
            asm volatile("s_waitcnt lgkmcnt(0)" : "+v"(wo2a), "+v"(wo2b), "+v"(wo3a), "+v"(wo3b)::"memory");
            wait_tags(ws, WS_F2 + ((rt64 >> 1) * 16 + (lane & 15)) * 8, tag);
            if (b == 0 && w == 0) {
                wait_tags(ws, WS_F1 + lane * 8, tag); wait_tags(ws, WS_F1 + 512 + lane * 8, tag);
                wait_tags(ws, WS_F1 + 1024 + lane * 8, tag); wait_tags(ws, WS_F1 + 1536 + lane * 8, tag);
                if (lane == 0) __hip_atomic_store((gu64*)(ws + WS_EPOCH), tag, RLX_AGENT);
            }
        }
        if (seg == 4) STAMP(10);
        if (seg >= 5 && seg - 5 < nfs) {
            asm volatile("s_waitcnt vmcnt(0)" : "+v"(h0), "+v"(h1)::"memory");
            const bf16x8 wa = trfrag(wta, 1024), wb = trfrag(wtb, 1024);
            d00 = MFMA(wa, __builtin_bit_cast(bf16x8, h0), d00); d01 = MFMA(wb, __builtin_bit_cast(bf16x8, h0), d01);
            d10 = MFMA(wa, __builtin_bit_cast(bf16x8, h1), d10); d11 = MFMA(wb, __builtin_bit_cast(bf16x8, h1), d11);
            asm volatile("" : "+v"(d00), "+v"(d01), "+v"(d10), "+v"(d11));
        }
        if (seg >= 4 && seg - 4 < nfs) {
            LOAD_DEV(h0, l16, bh + (seg - 4) * 1024); LOAD_DEV(h1, l16, bh + (86 + seg - 4) * 1024);
            glds(vws, bwd + (seg - 4) * 16384, wsl); glds(vws, bwd + (seg - 4) * 16384 + 8192, wsl + 1024);
        }
        asm volatile(
            TILE(160, 176, 232, 244, 240, 4096, 2048, 2560)
            TILE(176, 160, 240, 236, 232, 6144, 4096, 4608)
            TILE(160, 176, 232, 244, 240, 8192, 6144, 6656)
            TILE(176, 160, 240, 236, 232, 10240, 8192, 8704)
            "v_add_u32 %[aK], 0x2000, %[aK]\n\tv_add_u32 %[aV], 0x2000, %[aV]"
            : "+{v[160:175]}"(SA), "+{v[176:191]}"(SB), "+{v[192:207]}"(O), "+{v[224:227]}"(K), "+{v[232:235]}"(VA0), "+{v[236:239]}"(VB0),
              "+{v[240:243]}"(VA1), "+{v[244:247]}"(VB1), "+{v[248:251]}"(PA), "+{v[252:255]}"(PB), [aK] "+v"(aK), [aV] "+v"(aV)
            : "{v[228:231]}"(Q), "{v[208:223]}"(NM), [msk] "s"(msk)
            : "memory");
    }
    STAMP(11);
    asm volatile("s_nop 1" : "+v"(PB));
    O = MFMA(__builtin_bit_cast(bf16x8, VB1), __builtin_bit_cast(bf16x8, PB), O);
    {
      float chk = fabsf(O[8]);
#pragma unroll
      for (int e = 0; e < 8; ++e) chk = fmaxf(chk, fabsf(O[e])) + 0.f * O[e];
      if (__builtin_expect(__any(!(chk < 3.0e38f)), 0)) {
          const f32x16 z = {}; float m = -INFINITY;
#pragma unroll 1
          for (int t = 0; t < 64; ++t) { const f32x16 s = MFMA(lds128(lds + t * 2048 + l16), Q, z);
#pragma unroll
              for (int e = 0; e < 16; ++e) m = fmaxf(m, s[e]); }
          m = -swapmax(m);
#pragma unroll
          for (int e = 0; e < 16; ++e) NM[e] = m;
          O = z;
          const u32x4 one = {0x3f803f80u, 0x3f803f80u, 0x3f803f80u, 0x3f803f80u};
          const unsigned av = 1024 + hh * 256 + (lane & 15) * 16;
#pragma unroll 1
          for (int t = 0; t < 64; ++t) { f32x16 s = MFMA(lds128(lds + t * 2048 + l16), Q, NM);
#pragma unroll
              for (int e = 0; e < 16; ++e) s[e] = __builtin_amdgcn_exp2f(s[e]);
              u32x4 pa, pb;
#pragma unroll
              for (int e = 0; e < 4; ++e) { pa[e] = cvtpk(s[2 * e], s[2 * e + 1]); pb[e] = cvtpk(s[8 + 2 * e], s[9 + 2 * e]); }
              const u32x4 va = __builtin_bit_cast(u32x4, lds128(lds + t * 2048 + av)), vb = __builtin_bit_cast(u32x4, lds128(lds + t * 2048 + av + 512));
              O = MFMA(__builtin_bit_cast(bf16x8, blk ? one : va), __builtin_bit_cast(bf16x8, pa), O);
              O = MFMA(__builtin_bit_cast(bf16x8, blk ? one : vb), __builtin_bit_cast(bf16x8, pb), O); }
      } }
    { const float il = __builtin_amdgcn_rcpf(O[8]);
      const u32x4 oq = {cvtpk(O[0] * il, O[1] * il), cvtpk(O[2] * il, O[3] * il), cvtpk(O[4] * il, O[5] * il), cvtpk(O[6] * il, O[7] * il)};
      __builtin_amdgcn_raw_buffer_store_b128(oq, wsr, WS_OF + ((qb * 8 + w) * 32 + head) * 1024 + l16, 0, 16);
      WAITVM(0);
      if (lane == 0) __hip_atomic_store((gu64*)(ws + WS_F3 + ((qb * 8 + w) * 32 + head) * 8), tag, RLX_AGENT); }
    STAMP(12);
    const int orow = tid >> 3, oc = tid & 7;
    const size_t ooff = (size_t)(rt64 * 64 + orow) * 1024 + ct64 * 128 + oc * 16;
    const u32x4 xres = *(const u32x4*)(gx + ooff);
    const lds_t pw = lds + w * 16384 + r * 256;
    const lds_t pr = lds + orow * 256;
    auto put = [&](const f32x16& v, int rtile, int ctile) {
#pragma unroll
        for (int q4 = 0; q4 < 4; ++q4)
            *(__attribute__((address_space(3))) f32x4*)(pw + rtile * 8192 + (((ctile * 8 + q4 * 2 + hh) ^ (r & 7)) << 4)) = f32x4{v[4 * q4], v[4 * q4 + 1], v[4 * q4 + 2], v[4 * q4 + 3]};
    };
    auto gather = [&](f32x4& lo, f32x4& hi) {
        lo = f32x4{}; hi = f32x4{};
#pragma unroll
        for (int p = 0; p < 8; ++p) { lo += lds128f(pr + p * 16384 + (((2 * oc) ^ (orow & 7)) << 4)); hi += lds128f(pr + p * 16384 + (((2 * oc + 1) ^ (orow & 7)) << 4)); }
    };
    BAR();
    put(d00, 0, 0); put(d01, 0, 1); put(d10, 1, 0); put(d11, 1, 1);
    BAR();
    f32x4 glo, ghi; gather(glo, ghi);
#pragma unroll
    for (int e = 0; e < 4; ++e) { glo[e] *= __builtin_amdgcn_rcpf(1.0f + __builtin_amdgcn_exp2f(glo[e] * -1.4426950408889634f));
                                  ghi[e] *= __builtin_amdgcn_rcpf(1.0f + __builtin_amdgcn_exp2f(ghi[e] * -1.4426950408889634f)); }
    asm volatile("" : "+v"(glo), "+v"(ghi));
    STAMP(13);
    { int l3 = __builtin_amdgcn_mbcnt_hi(~0u, __builtin_amdgcn_mbcnt_lo(~0u, 0u)); asm volatile("" : "+v"(l3));
      wait_tags(ws, WS_F3 + ((2 * rt64 + ((l3 >> 2) & 1)) * 32 + 4 * w + (l3 & 3)) * 8, tag); }
    STAMP(14);
    { const char* bo = ws + WS_OF + (size_t)(2 * rt64 * 32 + 4 * w) * 1024;
      u32x4 o00, o01, o02, o03, o10, o11, o12, o13;
      LOAD_DEV(o00, l16, bo); LOAD_DEV(o10, l16, bo + 32768); LOAD_DEV(o01, l16, bo + 1024); LOAD_DEV(o11, l16, bo + 32768 + 1024);
      LOAD_DEV(o02, l16, bo + 2048); LOAD_DEV(o12, l16, bo + 32768 + 2048); LOAD_DEV(o03, l16, bo + 3072); LOAD_DEV(o13, l16, bo + 32768 + 3072);
      const f32x16 z = {};
      asm volatile("s_waitcnt vmcnt(6)" : "+v"(o00), "+v"(o10)::"memory");
      d00 = MFMA(wo0a, __builtin_bit_cast(bf16x8, o00), z); d01 = MFMA(wo0b, __builtin_bit_cast(bf16x8, o00), z);
      d10 = MFMA(wo0a, __builtin_bit_cast(bf16x8, o10), z); d11 = MFMA(wo0b, __builtin_bit_cast(bf16x8, o10), z);
      asm volatile("s_waitcnt vmcnt(4)" : "+v"(o01), "+v"(o11), "+v"(d00)::"memory");
      d00 = MFMA(wo1a, __builtin_bit_cast(bf16x8, o01), d00); d01 = MFMA(wo1b, __builtin_bit_cast(bf16x8, o01), d01);
      d10 = MFMA(wo1a, __builtin_bit_cast(bf16x8, o11), d10); d11 = MFMA(wo1b, __builtin_bit_cast(bf16x8, o11), d11);
      asm volatile("s_waitcnt vmcnt(2)" : "+v"(o02), "+v"(o12), "+v"(d00)::"memory");
      d00 = MFMA(wo2a, __builtin_bit_cast(bf16x8, o02), d00); d01 = MFMA(wo2b, __builtin_bit_cast(bf16x8, o02), d01);
      d10 = MFMA(wo2a, __builtin_bit_cast(bf16x8, o12), d10); d11 = MFMA(wo2b, __builtin_bit_cast(bf16x8, o12), d11);
      asm volatile("s_waitcnt vmcnt(0)" : "+v"(o03), "+v"(o13), "+v"(d00)::"memory");
      d00 = MFMA(wo3a, __builtin_bit_cast(bf16x8, o03), d00); d01 = MFMA(wo3b, __builtin_bit_cast(bf16x8, o03), d01);
      d10 = MFMA(wo3a, __builtin_bit_cast(bf16x8, o13), d10); d11 = MFMA(wo3b, __builtin_bit_cast(bf16x8, o13), d11); }
    BAR();
    put(d00, 0, 0); put(d01, 0, 1); put(d10, 1, 0); put(d11, 1, 1);
    BAR();
    f32x4 slo, shi; gather(slo, shi);
    u32x4 res;
#pragma unroll
    for (int e = 0; e < 2; ++e) {
        res[e] = cvtpk(__builtin_fmaf(slo[2 * e], glo[2 * e], __uint_as_float(xres[e] << 16)), __builtin_fmaf(slo[2 * e + 1], glo[2 * e + 1], __uint_as_float(xres[e] & 0xffff0000u)));
        res[2 + e] = cvtpk(__builtin_fmaf(shi[2 * e], ghi[2 * e], __uint_as_float(xres[2 + e] << 16)), __builtin_fmaf(shi[2 * e + 1], ghi[2 * e + 1], __uint_as_float(xres[2 + e] & 0xffff0000u)));
    }
    __builtin_amdgcn_raw_buffer_store_b128(res, __builtin_amdgcn_make_buffer_rsrc(gout, 0, 2048 * 1024, 0x00020000), (unsigned)ooff, 0, OUT_AUX);
    STAMP(15);
    STAMP_FLUSH;
#undef lane
#undef r
#undef hh
#undef blk
#undef tq
#undef tp
#undef tid
}

extern "C" void kernel_launch(void* const* d_in, const int* in_sizes, int n_in, void* d_out, int out_size, void* d_ws, size_t ws_size,
                              hipStream_t stream) {
    (void)hipFuncSetAttribute((const void*)fused_block, hipFuncAttributeMaxDynamicSharedMemorySize, LDS_BYTES);
    fused_block<<<256, 512, LDS_BYTES, stream>>>((const char*)d_in[0], (const char*)d_in[1], (const char*)d_in[2], (const char*)d_in[4],
                                                 (const char*)d_in[5], (const char*)d_in[6], (char*)d_out, (char*)d_ws);
}
```

```cpp
#define PF_CODE
#define PADA 8
#define PADB 11
#include <hip/hip_runtime.h>
#include <stdint.h>

typedef __attribute__((ext_vector_type(8))) short bf16x8;
typedef __attribute__((ext_vector_type(4))) short s16x4;
typedef __attribute__((ext_vector_type(2))) __bf16 bf2;
typedef __attribute__((ext_vector_type(16))) float f32x16;
typedef __attribute__((ext_vector_type(4))) float f32x4;
typedef __attribute__((ext_vector_type(2))) float f32x2;
typedef __attribute__((ext_vector_type(4))) unsigned u32x4;
typedef unsigned long long u64;
typedef __attribute__((address_space(3))) char* lds_t;
typedef __attribute__((address_space(1))) u64 gu64;

template <int N> struct IC { static constexpr int value = N; };
template <bool B> struct BC { static constexpr bool value = B; };
#define DI __device__ __forceinline__
#define MFMA(a, b, c) __builtin_amdgcn_mfma_f32_32x32x16_bf16((a), (b), (c), 0, 0, 0)
#define RLX_AGENT __ATOMIC_RELAXED, __HIP_MEMORY_SCOPE_AGENT

constexpr unsigned WS_EPOCH = 0;
constexpr unsigned WS_F1 = 4096;
constexpr unsigned WS_F2 = 8192;
constexpr unsigned WS_F3 = 16384;
constexpr unsigned WS_QF = 65536;
constexpr unsigned WS_KV = WS_QF + (2u << 20);
constexpr unsigned WS_HF = WS_KV + (4u << 20);
constexpr unsigned WS_OF = WS_HF + 64u * 86u * 1024u;
constexpr unsigned WS_END = WS_OF + (2u << 20);
constexpr unsigned SLOT = 28672, XIMG = 0, WIMG = 16384, GIMG = 0, UIMG = 12288 + 32;
constexpr unsigned L_STAT = 153600;
constexpr unsigned L_RSTD = L_STAT + 1024;
constexpr unsigned L_CSQ = L_RSTD + 512;
constexpr unsigned L_CSG = L_CSQ;
constexpr unsigned L_WSL = 131072;
constexpr unsigned LDS_BYTES = 163840;
constexpr float QSCALE = 0.25f * 1.4426950408889634f;
constexpr float M_OFF = 64.0f;

DI unsigned cvtpk(float lo, float hi) { return __builtin_bit_cast(unsigned, __builtin_convertvector(f32x2{lo, hi}, bf2)); }
DI float dot2(unsigned a, unsigned b, float c) { return __builtin_amdgcn_fdot2_f32_bf16(__builtin_bit_cast(bf2, a), __builtin_bit_cast(bf2, b), c, false); }
DI void glds(unsigned voff, const char* sbase, unsigned ldst) {
    asm volatile("s_mov_b32 m0, %2\n\ts_nop 0\n\tglobal_load_lds_dwordx4 %0, %1" ::"v"(voff), "s"(sbase), "s"(ldst) : "memory"); }
DI void glds_dev(unsigned voff, const char* sbase, unsigned ldst) {
    asm volatile("s_mov_b32 m0, %2\n\ts_nop 0\n\tglobal_load_lds_dwordx4 %0, %1 sc1" ::"v"(voff), "s"(sbase), "s"(ldst) : "memory"); }
#define LOAD_DEV(dst, voff, sbase) asm volatile("global_load_dwordx4 %0, %1, %2 sc1" : "=v"(dst) : "v"(voff), "s"(sbase) : "memory")
#define WAITVM(n) asm volatile("s_waitcnt vmcnt(" #n ")" ::: "memory")
#define BAR() asm volatile("s_waitcnt lgkmcnt(0)\n\ts_barrier" ::: "memory")
template <int N> DI void waitvm() { asm volatile("s_waitcnt vmcnt(%0)" ::"i"(N) : "memory"); }
DI bf16x8 lds128(lds_t p) { return *(const __attribute__((address_space(3))) bf16x8*)p; }
DI f32x4 lds128f(lds_t p) { return *(const __attribute__((address_space(3))) f32x4*)p; }
DI s16x4 ldstr(lds_t p) { return __builtin_amdgcn_ds_read_tr16_b64_v4i16((__attribute__((address_space(3))) s16x4*)p); }
DI bf16x8 trfrag(lds_t p, int step) { s16x4 a = ldstr(p), b = ldstr(p + step); return __builtin_shufflevector(a, b, 0, 1, 2, 3, 4, 5, 6, 7); }
DI float swapadd(float v) { auto r = __builtin_amdgcn_permlane32_swap(__float_as_uint(v), __float_as_uint(v), false, false); return __uint_as_float(r[0]) + __uint_as_float(r[1]); }
DI float swapmax(float v) { auto r = __builtin_amdgcn_permlane32_swap(__float_as_uint(v), __float_as_uint(v), false, false); return fmaxf(__uint_as_float(r[0]), __uint_as_float(r[1])); }
DI float frag_sum(bf16x8 f, float acc) { u32x4 u = __builtin_bit_cast(u32x4, f); acc = dot2(u[0], 0x3f803f80u, acc); acc = dot2(u[1], 0x3f803f80u, acc); acc = dot2(u[2], 0x3f803f80u, acc); return dot2(u[3], 0x3f803f80u, acc); }
DI bf16x8 split_a(float v) { unsigned h = cvtpk(v, v); float lo = v - __uint_as_float(h << 16); u32x4 u = {h, cvtpk(lo, lo), 0u, 0u}; return __builtin_bit_cast(bf16x8, u); }
DI bf16x8 split_b(float v) { unsigned h = cvtpk(v, v); float lo = v - __uint_as_float(h << 16); unsigned p = cvtpk(v, lo); u32x4 u = {p, p, 0u, 0u}; return __builtin_bit_cast(bf16x8, u); }

#define STAMP(n) do { if ((n) == PADA || (n) == PADB || (n) == 0) asm volatile("s_memrealtime %0\n\ts_waitcnt lgkmcnt(0)" : "=s"(stamps[n]) :: "memory"); } while (0)
#define STAMP_DECL u64 stamps[16]; u64 clk0 = __builtin_amdgcn_s_memtime();
#define STAMP_FLUSH do { u64 now_ = __builtin_amdgcn_s_memrealtime(); u64 tgt_ = PADA == PADB ? stamps[0] + 8000 : now_ + 3 * (stamps[PADB] - stamps[PADA]); \
    if (PADA == 15) { while (__builtin_amdgcn_s_memtime() - clk0 < 200000) __builtin_amdgcn_s_sleep(1); } else \
    while (__builtin_amdgcn_s_memrealtime() < tgt_) __builtin_amdgcn_s_sleep(1); } while (0)
DI void wait_tags(const char* ws, unsigned off, u64 tag) {
    const u64 t0 = __builtin_amdgcn_s_memrealtime();
    for (;;) {
        u64 v = __hip_atomic_load((gu64*)(ws + off), RLX_AGENT);
        if (__all(v == tag)) break;
        __builtin_amdgcn_s_sleep(1);
        if (__builtin_amdgcn_s_memrealtime() - t0 > 2000000ull) break;
    }
    asm volatile("" ::: "memory");
}

#define EX(S, i) "v_exp_f32 v[" #S "+" #i "], v[" #S "+" #i "]\n\t"
#define CV(S, p, i, j) "v_cvt_pk_bf16_f32 v[248+" #p "], v[" #S "+" #i "], v[" #S "+" #j "]\n\t"
#define TILE(SC, SN, VAc, VBp, VAl, KOFF, VOFF0, VOFF1) \
    "v_mfma_f32_32x32x16_bf16 v[" #SN ":" #SN "+15], v[224:227], v[228:231], v[208:223]\n\t" \
    EX(SC, 0) EX(SC, 1) EX(SC, 2) CV(SC, 0, 0, 1) EX(SC, 3) EX(SC, 4) CV(SC, 1, 2, 3) EX(SC, 5) \
    "v_mfma_f32_32x32x16_bf16 v[192:207], v[" #VBp ":" #VBp "+3], v[252:255], v[192:207]\n\t" \
    "ds_read_b128 v[224:227], %[aK] offset:" #KOFF "\n\t" \
    "s_mov_b64 exec, %[msk]\n\t" \
    "ds_read_b128 v[" #VAl ":" #VAl "+3], %[aV] offset:" #VOFF0 "\n\t" \
    "ds_read_b128 v[" #VBp ":" #VBp "+3], %[aV] offset:" #VOFF1 "\n\t" \
    "s_mov_b64 exec, -1\n\t" \
    EX(SC, 6) CV(SC, 2, 4, 5) EX(SC, 7) EX(SC, 8) CV(SC, 3, 6, 7) EX(SC, 9) EX(SC, 10) \
    "v_mfma_f32_32x32x16_bf16 v[192:207], v[" #VAc ":" #VAc "+3], v[248:251], v[192:207]\n\t" \
    CV(SC, 4, 8, 9) EX(SC, 11) EX(SC, 12) CV(SC, 5, 10, 11) EX(SC, 13) EX(SC, 14) CV(SC, 6, 12, 13) EX(SC, 15) \
    "s_waitcnt lgkmcnt(0)\n\t" \
    CV(SC, 7, 14, 15)

extern "C" __global__ void __launch_bounds__(512, 2)
fused_block(const char* __restrict__ gx, const char* __restrict__ gwqkv, const char* __restrict__ gwo, const char* __restrict__ gwg,
            const char* __restrict__ gwu, const char* __restrict__ gwd, char* __restrict__ gout, char* ws) {
    extern __shared__ __attribute__((aligned(16))) char smem[];
    const lds_t lds = (lds_t)smem;
    STAMP_DECL STAMP(0);
    const int tid = threadIdx.x, lane = tid & 63, r = lane & 31, hh = lane >> 5;
    const int w = __builtin_amdgcn_readfirstlane(tid >> 6), wr = w & 3, g = w >> 2;
    const int b = blockIdx.x, xcd = b & 7, bi = b >> 3;
    u64 epoch;
    asm volatile("s_load_dwordx2 %0, %1, 0x0" : "=s"(epoch) : "s"(ws) : "memory");
    const __amdgpu_buffer_rsrc_t wsr = __builtin_amdgcn_make_buffer_rsrc(ws, 0, WS_END, 0x00020000);

    const int rb = 4 * (xcd >> 1) + (bi >> 3), cg = 8 * (xcd & 1) + (bi & 7);
    const int nft = cg < 6 ? 6 : 5, ft0 = cg < 6 ? 6 * cg : 36 + 5 * (cg - 6);
    unsigned vx0, vx1, vw0, vw1, vg0, vg1, vg2;
    { int row = 16 * w + (lane >> 3); vx0 = row * 1024 + (((lane & 7) ^ ((row >> 1) & 7)) << 4);
      row += 8; vx1 = row * 1024 + (((lane & 7) ^ ((row >> 1) & 7)) << 4);
      int o = w * 1024 + lane * 16; vw0 = (o / 192) * 3072 + o % 192;
      o = ((w & 3) + 8) * 1024 + lane * 16; vw1 = (o / 192) * 3072 + o % 192;
      const int lim = nft * 32;
      o = w * 1024 + lane * 16; int cb = o % 192; vg0 = (o / 192) * 2752 + (cb >= lim ? cb - 32 : cb);
      o = (w < 4 ? w + 8 : w - 4) * 1024 + lane * 16; cb = o % 192; vg1 = (o / 192) * 2752 + (cb >= lim ? cb - 32 : cb);
      o = (w + 4) * 1024 + lane * 16; cb = o % 192; vg2 = (o / 192) * 2752 + (cb >= lim ? cb - 32 : cb); }
    const char* bx = gx + (size_t)rb * 128 * 1024;
    const char* bw = gwqkv + cg * 192;
    const char* bg0 = gwg + ft0 * 32;
    const char* bg1 = (w < 4 ? gwg : gwu) + ft0 * 32;
    const char* bg2 = gwu + ft0 * 32;
    const unsigned dg1 = w < 4 ? GIMG + (w + 8) * 1024 : UIMG + (w - 4) * 1024;
    auto issue = [&](int c, int gg) {
        const unsigned s = (c % 5) * SLOT;
        if (c < 8) {
            glds(vx0, bx + c * 128, s + XIMG + w * 2048);
            glds(vx1, bx + c * 128, s + XIMG + w * 2048 + 1024);
            glds(vw0, bw + c * (64 * 3072), s + WIMG + w * 1024);
            if (gg == 0) glds(vw1, bw + c * (64 * 3072), s + WIMG + (w + 8) * 1024);
        } else {
            glds(vg0, bg0 + (c - 8) * (64 * 2752), s + GIMG + w * 1024);
            glds(vg1, bg1 + (c - 8) * (64 * 2752), s + dg1);
            glds(vg2, bg2 + (c - 8) * (64 * 2752), s + UIMG + (w + 4) * 1024);
        }
    };
#pragma unroll
    for (int c = 0; c < 5; ++c) issue(c, g);
#define TOUCH(voff, base) asm volatile("s_mov_b32 m0, %2\n\ts_nop 0\n\tglobal_load_lds_dword %0, %1" ::"v"(voff), "s"(base), "s"(163840u - 256u) : "memory")
    { const char* pc; asm volatile("s_getpc_b64 %0" : "=s"(pc));
      const unsigned o = tid * 128;
      if (o < 39000) TOUCH(o, pc); }

    const int xrow = 32 * wr + r, blk = (lane >> 4) & 1, tq = (lane & 15) >> 2, tp = lane & 3;
    const unsigned xa = xrow * 128 + ((hh ^ ((xrow >> 1) & 7)) << 4);
    const lds_t wqa = lds + WIMG + (8 * hh + tq) * 192 + blk * 32 + tp * 8 + (g ? 128 : 0);
    const lds_t wga = lds + (blk ? UIMG : GIMG) + (8 * hh + tq) * 192 + tp * 8 + g * 96;
    const int ct0 = 3 * cg + (g ? 2 : 0);

    bf16x8 xf[32];
    f32x16 a0 = {}, a1 = {}, a2 = {};
    float cs0 = 0.f, cs1 = 0.f, cs2 = 0.f, sx = 0.f, sq = 0.f;
    auto qkv_loop = [&](auto gconst, auto vconst) {
        constexpr int G = decltype(gconst)::value; constexpr bool V = decltype(vconst)::value;
#pragma unroll
        for (int i = 0; i < 8; ++i) {
            if (G == 0) { switch (i) { case 0: WAITVM(16); break; case 5: WAITVM(11); break; case 6: WAITVM(10); break; case 7: WAITVM(9); break; default: WAITVM(12); } }
            else { if (i == 0) WAITVM(12); else WAITVM(9); }
            BAR();
            if (i == 0) STAMP(1);
            if (i == 4) STAMP(2);
            if (i >= 1) issue(i + 4, G);
            const unsigned s = (i % 5) * SLOT;
#pragma unroll
            for (int ks = 0; ks < 4; ++ks) {
                const bf16x8 x = lds128(lds + s + XIMG + (xa ^ (ks << 5)));
                xf[4 * i + ks] = x;
                const bf16x8 w0 = trfrag(wqa + s + ks * 16 * 192, 4 * 192);
                a0 = V ? MFMA(x, w0, a0) : MFMA(w0, x, a0);
                if (G == 0) { const bf16x8 w1 = trfrag(wqa + s + ks * 16 * 192 + 64, 4 * 192); a1 = V ? MFMA(x, w1, a1) : MFMA(w1, x, a1); }
                else { u32x4 u = __builtin_bit_cast(u32x4, x);
#pragma unroll
                    for (int e = 0; e < 4; ++e) { sx = dot2(u[e], 0x3f803f80u, sx); sq = dot2(u[e], u[e], sq); } }
            }
            cs0 = frag_sum(trfrag(wqa + s + wr * 16 * 192, 4 * 192), cs0);
            if (G == 0) cs1 = frag_sum(trfrag(wqa + s + wr * 16 * 192 + 64, 4 * 192), cs1);
            asm volatile("" : "+v"(cs0), "+v"(cs1), "+v"(sx), "+v"(sq));
        }
    };
    if (g == 0) { if (ct0 >= 32) qkv_loop(IC<0>{}, BC<true>{}); else qkv_loop(IC<0>{}, BC<false>{}); }
    else { if (ct0 >= 32) qkv_loop(IC<1>{}, BC<true>{}); else qkv_loop(IC<1>{}, BC<false>{}); }
    asm volatile("" : "+s"(epoch));
    const u64 tag = epoch + 0x9E3779B97F4A7C15ull;
    *(__attribute__((address_space(3))) float*)(lds + L_CSQ + (g ? 2 : 0) * 1024 + lane * 16 + wr * 4) = cs0;
    if (g == 0) *(__attribute__((address_space(3))) float*)(lds + L_CSQ + 1024 + lane * 16 + wr * 4) = cs1;
    else {
        sx = swapadd(sx); sq = swapadd(sq);
        const float mean = sx * (1.f / 512.f), var = fmaxf(sq * (1.f / 512.f) - mean * mean, 0.f), rs = __builtin_amdgcn_rsqf(var + 1e-5f);
        if (hh == 0) { *(__attribute__((address_space(3))) f32x2*)(lds + L_STAT + xrow * 8) = f32x2{mean, rs};
                       *(__attribute__((address_space(3))) float*)(lds + L_RSTD + xrow * 4) = rs; }
    }
    float rstd; bf16x8 mfrag;
    const int rt = rb * 4 + wr;
#pragma unroll
    for (int i = 8; i < 16; ++i) {
        switch (i) { case 9: WAITVM(11); break; case 10: WAITVM(6); break; case 13: WAITVM(6); break; case 14: WAITVM(3); break; case 15: WAITVM(0); break; default: WAITVM(9); }
        BAR();
        if (i == 8) STAMP(3);
        if (i == 9) STAMP(4);
        if (i == 8) {
            const f32x2 st = *(const __attribute__((address_space(3))) f32x2*)(lds + L_STAT + xrow * 8);
            rstd = st[1]; mfrag = split_b(-st[0]);
            for (int t = 0; t < 2 - g; ++t) {
                const int ct = ct0 + t, hp = ct & 15, j = g ? 2 : t;
                const f32x4 p = lds128f(lds + L_CSQ + j * 1024 + lane * 16);
                const bf16x8 cf = split_a((p[0] + p[1]) + (p[2] + p[3]));
                u32x4 lo, hi; unsigned o0, o1;
                if (ct >= 32) {
                    a0 = MFMA(mfrag, cf, a0);
#pragma unroll
                    for (int q4 = 0; q4 < 4; ++q4) { const f32x4 rs = lds128f(lds + L_RSTD + (32 * wr + 8 * q4 + 4 * hh) * 4);
#pragma unroll
                        for (int e = 0; e < 4; ++e) a0[4 * q4 + e] *= rs[e]; }
                    o0 = WS_KV + ((2 * hp + blk) * 64 + rt) * 2048 + 1024 + hh * 256 + (lane & 15) * 16; o1 = o0 + 512;
                } else {
                    a0 = MFMA(cf, mfrag, a0);
                    const float sc = ct < 16 ? rstd * QSCALE : rstd;
#pragma unroll
                    for (int e = 0; e < 16; ++e) a0[e] *= sc;
                    o0 = (ct < 16 ? WS_QF + (2 * hp * 64 + rt) * 1024 : WS_KV + (2 * hp * 64 + rt) * 2048) + lane * 16;
                    o1 = o0 + (ct < 16 ? 64 * 1024 : 64 * 2048);
                }
#pragma unroll
                for (int e = 0; e < 4; ++e) { lo[e] = cvtpk(a0[2 * e], a0[2 * e + 1]); hi[e] = cvtpk(a0[8 + 2 * e], a0[9 + 2 * e]); }
                __builtin_amdgcn_raw_buffer_store_b128(lo, wsr, o0, 0, 16);
                __builtin_amdgcn_raw_buffer_store_b128(hi, wsr, o1, 0, 16);
                a0 = a1;
            }
            a0 = f32x16{}; a1 = f32x16{}; cs0 = 0.f; cs1 = 0.f;
            asm volatile("" ::: "memory");
        }
        if (i == 10 && tid == 0) __hip_atomic_store((gu64*)(ws + WS_F1 + (rb * 16 + cg) * 8), tag, RLX_AGENT);
        if (i + 4 < 16) issue(i + 4, 0);
        const unsigned s = (i % 5) * SLOT;
#pragma unroll
        for (int ks = 0; ks < 4; ++ks) {
            const bf16x8 w0 = trfrag(wga + s + ks * 16 * 192, 4 * 192), w1 = trfrag(wga + s + ks * 16 * 192 + 32, 4 * 192), w2 = trfrag(wga + s + ks * 16 * 192 + 64, 4 * 192);
            a0 = MFMA(w0, xf[4 * (i - 8) + ks], a0); a1 = MFMA(w1, xf[4 * (i - 8) + ks], a1); a2 = MFMA(w2, xf[4 * (i - 8) + ks], a2);
        }
        cs0 = frag_sum(trfrag(wga + s + wr * 16 * 192, 4 * 192), cs0); cs1 = frag_sum(trfrag(wga + s + wr * 16 * 192 + 32, 4 * 192), cs1);
        cs2 = frag_sum(trfrag(wga + s + wr * 16 * 192 + 64, 4 * 192), cs2);
        asm volatile("" : "+v"(cs0), "+v"(cs1), "+v"(cs2));
    }
    *(__attribute__((address_space(3))) float*)(lds + L_CSG + (3 * g + 0) * 1024 + lane * 16 + wr * 4) = cs0;
    *(__attribute__((address_space(3))) float*)(lds + L_CSG + (3 * g + 1) * 1024 + lane * 16 + wr * 4) = cs1;
    *(__attribute__((address_space(3))) float*)(lds + L_CSG + (3 * g + 2) * 1024 + lane * 16 + wr * 4) = cs2;

    int lane2 = __builtin_amdgcn_mbcnt_hi(~0u, __builtin_amdgcn_mbcnt_lo(~0u, 0u));
    asm volatile("" : "+v"(lane2));
#define lane lane2
    const int r2 = lane & 31, hh2 = lane >> 5, blk2 = (lane >> 4) & 1, tq2 = (lane & 15) >> 2, tp2 = lane & 3, tid2 = w * 64 + lane;
#define r r2
#define hh hh2
#define blk blk2
#define tq tq2
#define tp tp2
#define tid tid2
    STAMP(5);
    const int head = 4 * xcd + (bi & 3), qb = bi >> 2;
    if (w == 0) {
        const int hp = head >> 1;
        const int frb = lane < 32 ? (lane & 15) : 2 * qb + (lane & 1), fct = lane < 16 ? 16 + hp : lane < 32 ? 32 + hp : hp;
        wait_tags(ws, WS_F1 + (frb * 16 + fct / 3) * 8, tag);
    }
    BAR();
    STAMP(6);
    u32x4 qf;
    const unsigned l16 = lane * 16;
    LOAD_DEV(qf, l16, ws + WS_QF + (head * 64 + qb * 8 + w) * 1024);
    { const char* kv = ws + WS_KV + head * 131072 + w * 2048;
#pragma unroll
      for (int n = 0; n < 8; ++n) { glds_dev(l16, kv + n * 16384, n * 16384 + w * 2048); glds_dev(l16, kv + n * 16384 + 1024, n * 16384 + w * 2048 + 1024); } }
#pragma unroll 1
    for (int t = 0; t < 3; ++t) {
        const f32x4 p = lds128f(lds + L_CSG + (3 * g + t) * 1024 + lane * 16);
        a0 = MFMA(split_a((p[0] + p[1]) + (p[2] + p[3])), mfrag, a0);
        float hv[8];
#pragma unroll
        for (int e = 0; e < 8; ++e) {
            const float gg = a0[e] * rstd, uu = a0[8 + e] * rstd;
            const float z = gg * __builtin_fmaf(gg * gg, -2.0f * 0.7978845608028654f * 0.044715f * 1.4426950408889634f, -2.0f * 0.7978845608028654f * 1.4426950408889634f);
            hv[e] = gg * uu * __builtin_amdgcn_rcpf(1.0f + __builtin_amdgcn_exp2f(z));
        }
        const u32x4 hq = {cvtpk(hv[0], hv[1]), cvtpk(hv[2], hv[3]), cvtpk(hv[4], hv[5]), cvtpk(hv[6], hv[7])};
        if (3 * g + t < nft) __builtin_amdgcn_raw_buffer_store_b128(hq, wsr, WS_HF + (rt * 86 + ft0 + 3 * g + t) * 1024 + l16, 0, 16);
        a0 = a1; a1 = a2;
    }

    const int rt64 = 4 * xcd + (bi >> 3), ct64 = bi & 7;
    const unsigned vws = (lane >> 3) * 1024 + (((lane & 7) ^ (((lane >> 4) & 1) << 2)) << 4);
    const unsigned wsl = L_WSL + w * 4096;
    const lds_t wta = lds + wsl + (4 * hh + tq) * 128 + ((tq >> 1) & 1) * 64 + blk * 32 + tp * 8;
    const lds_t wtb = lds + wsl + (4 * hh + tq) * 128 + (((tq >> 1) & 1) ^ 1) * 64 + blk * 32 + tp * 8;
    const char* bwo = gwo + (size_t)w * 64 * 1024 + ct64 * 128;
    STAMP(7);
    asm volatile("s_waitcnt vmcnt(16)" : "+v"(qf)::"memory");
    BAR();
    STAMP(8);
#pragma unroll
    for (int n = 0; n < 4; ++n) glds(vws, bwo + n * 8192, wsl + n * 1024);
    const bf16x8 Q = __builtin_bit_cast(bf16x8, qf);
    f32x16 NM, SA, SB, O = {};
    { const f32x16 z = {};
      SA = MFMA(lds128(lds + l16), Q, z); SB = MFMA(lds128(lds + 2048 + l16), Q, z);
      float m = fmaxf(SA[0], SB[0]);
#pragma unroll
      for (int e = 1; e < 16; ++e) m = fmaxf(fmaxf(m, SA[e]), SB[e]);
      m = -(swapmax(m) + M_OFF);
#pragma unroll
      for (int e = 0; e < 16; ++e) NM[e] = m; }
    unsigned aK = l16, aV = 1024 + hh * 256 + (lane & 15) * 16;
    const u64 msk = 0x0000ffff0000ffffull;
    u32x4 K, VA0, VB0, VA1, VB1, PA = {}, PB = {};
    { const u32x4 one = {0x3f803f80u, 0x3f803f80u, 0x3f803f80u, 0x3f803f80u};
      const u32x4 va = __builtin_bit_cast(u32x4, lds128(lds + aV)), vb = __builtin_bit_cast(u32x4, lds128(lds + aV + 512));
      VA0 = blk ? one : va; VB0 = blk ? one : vb; VA1 = one; VB1 = one;
      SA = MFMA(lds128(lds + l16), Q, NM);
      K = __builtin_bit_cast(u32x4, lds128(lds + 2048 + l16)); }
    f32x16 d00 = {}, d01 = {}, d10 = {}, d11 = {};
    bf16x8 wo0a, wo0b, wo1a, wo1b, wo2a, wo2b, wo3a, wo3b;
    u32x4 h0, h1;
    const int fs0 = w < 6 ? 11 * w : 66 + 10 * (w - 6), nfs = w < 6 ? 11 : 10;
    const char* bh = ws + WS_HF + (size_t)(2 * rt64 * 86 + fs0) * 1024;
    const char* bwd = gwd + (size_t)fs0 * 16 * 1024 + ct64 * 128;
    asm volatile("s_nop 7\n\ts_nop 3" : "+v"(SA), "+v"(K), "+v"(VA0), "+v"(VB0));
#pragma unroll 1
    for (int seg = 0; seg < 16; ++seg) {
        if (seg == 2) {
            STAMP(9);
            WAITVM(0); BAR();
            if (tid == 0) __hip_atomic_store((gu64*)(ws + WS_F2 + (rb * 16 + cg) * 8), tag, RLX_AGENT);
            wo0a = trfrag(wta, 1024); wo0b = trfrag(wtb, 1024); wo1a = trfrag(wta + 2048, 1024); wo1b = trfrag(wtb + 2048, 1024);
            asm volatile("s_waitcnt lgkmcnt(0)" : "+v"(wo0a), "+v"(wo0b), "+v"(wo1a), "+v"(wo1b)::"memory");
#pragma unroll
            for (int n = 0; n < 4; ++n) glds(vws, bwo + (4 + n) * 8192, wsl + n * 1024);
        }
        if (seg == 4) {
            WAITVM(0);
            wo2a = trfrag(wta, 1024); wo2b = trfrag(wtb, 1024); wo3a = trfrag(wta + 2048, 1024); wo3b = trfrag(wtb + 2048, 1024);
            asm volatile("s_waitcnt lgkmcnt(0)" : "+v"(wo2a), "+v"(wo2b), "+v"(wo3a), "+v"(wo3b)::"memory");
            wait_tags(ws, WS_F2 + ((rt64 >> 1) * 16 + (lane & 15)) * 8, tag);
            if (b == 0 && w == 0) {
                wait_tags(ws, WS_F1 + lane * 8, tag); wait_tags(ws, WS_F1 + 512 + lane * 8, tag);
                wait_tags(ws, WS_F1 + 1024 + lane * 8, tag); wait_tags(ws, WS_F1 + 1536 + lane * 8, tag);
                if (lane == 0) __hip_atomic_store((gu64*)(ws + WS_EPOCH), tag, RLX_AGENT);
            }
        }
        if (seg == 4) STAMP(10);
        if (seg >= 5 && seg - 5 < nfs) {
            asm volatile("s_waitcnt vmcnt(0)" : "+v"(h0), "+v"(h1)::"memory");
            const bf16x8 wa = trfrag(wta, 1024), wb = trfrag(wtb, 1024);
            d00 = MFMA(wa, __builtin_bit_cast(bf16x8, h0), d00); d01 = MFMA(wb, __builtin_bit_cast(bf16x8, h0), d01);
            d10 = MFMA(wa, __builtin_bit_cast(bf16x8, h1), d10); d11 = MFMA(wb, __builtin_bit_cast(bf16x8, h1), d11);
            asm volatile("" : "+v"(d00), "+v"(d01), "+v"(d10), "+v"(d11));
        }
        if (seg >= 4 && seg - 4 < nfs) {
            LOAD_DEV(h0, l16, bh + (seg - 4) * 1024); LOAD_DEV(h1, l16, bh + (86 + seg - 4) * 1024);
            glds(vws, bwd + (seg - 4) * 16384, wsl); glds(vws, bwd + (seg - 4) * 16384 + 8192, wsl + 1024);
        }
        asm volatile(
            TILE(160, 176, 232, 244, 240, 4096, 2048, 2560)
            TILE(176, 160, 240, 236, 232, 6144, 4096, 4608)
            TILE(160, 176, 232, 244, 240, 8192, 6144, 6656)
            TILE(176, 160, 240, 236, 232, 10240, 8192, 8704)
            "v_add_u32 %[aK], 0x2000, %[aK]\n\tv_add_u32 %[aV], 0x2000, %[aV]"
            : "+{v[160:175]}"(SA), "+{v[176:191]}"(SB), "+{v[192:207]}"(O), "+{v[224:227]}"(K), "+{v[232:235]}"(VA0), "+{v[236:239]}"(VB0),
              "+{v[240:243]}"(VA1), "+{v[244:247]}"(VB1), "+{v[248:251]}"(PA), "+{v[252:255]}"(PB), [aK] "+v"(aK), [aV] "+v"(aV)
            : "{v[228:231]}"(Q), "{v[208:223]}"(NM), [msk] "s"(msk)
            : "memory");
    }
    STAMP(11);
    asm volatile("s_nop 1" : "+v"(PB));
    O = MFMA(__builtin_bit_cast(bf16x8, VB1), __builtin_bit_cast(bf16x8, PB), O);
    {
      float chk = fabsf(O[8]);
#pragma unroll
      for (int e = 0; e < 8; ++e) chk = fmaxf(chk, fabsf(O[e])) + 0.f * O[e];
      if (__builtin_expect(__any(!(chk < 3.0e38f)), 0)) {
          const f32x16 z = {}; float m = -INFINITY;
#pragma unroll 1
          for (int t = 0; t < 64; ++t) { const f32x16 s = MFMA(lds128(lds + t * 2048 + l16), Q, z);
#pragma unroll
              for (int e = 0; e < 16; ++e) m = fmaxf(m, s[e]); }
          m = -swapmax(m);
#pragma unroll
          for (int e = 0; e < 16; ++e) NM[e] = m;
          O = z;
          const u32x4 one = {0x3f803f80u, 0x3f803f80u, 0x3f803f80u, 0x3f803f80u};
          const unsigned av = 1024 + hh * 256 + (lane & 15) * 16;
#pragma unroll 1
          for (int t = 0; t < 64; ++t) { f32x16 s = MFMA(lds128(lds + t * 2048 + l16), Q, NM);
#pragma unroll
              for (int e = 0; e < 16; ++e) s[e] = __builtin_amdgcn_exp2f(s[e]);
              u32x4 pa, pb;
#pragma unroll
              for (int e = 0; e < 4; ++e) { pa[e] = cvtpk(s[2 * e], s[2 * e + 1]); pb[e] = cvtpk(s[8 + 2 * e], s[9 + 2 * e]); }
              const u32x4 va = __builtin_bit_cast(u32x4, lds128(lds + t * 2048 + av)), vb = __builtin_bit_cast(u32x4, lds128(lds + t * 2048 + av + 512));
              O = MFMA(__builtin_bit_cast(bf16x8, blk ? one : va), __builtin_bit_cast(bf16x8, pa), O);
              O = MFMA(__builtin_bit_cast(bf16x8, blk ? one : vb), __builtin_bit_cast(bf16x8, pb), O); }
      } }
    { const float il = __builtin_amdgcn_rcpf(O[8]);
      const u32x4 oq = {cvtpk(O[0] * il, O[1] * il), cvtpk(O[2] * il, O[3] * il), cvtpk(O[4] * il, O[5] * il), cvtpk(O[6] * il, O[7] * il)};
      __builtin_amdgcn_raw_buffer_store_b128(oq, wsr, WS_OF + ((qb * 8 + w) * 32 + head) * 1024 + l16, 0, 16);
      WAITVM(0);
      if (lane == 0) __hip_atomic_store((gu64*)(ws + WS_F3 + ((qb * 8 + w) * 32 + head) * 8), tag, RLX_AGENT); }
    STAMP(12);
    const int orow = tid >> 3, oc = tid & 7;
    const size_t ooff = (size_t)(rt64 * 64 + orow) * 1024 + ct64 * 128 + oc * 16;
    const u32x4 xres = *(const u32x4*)(gx + ooff);
    const lds_t pw = lds + w * 16384 + r * 256;
    const lds_t pr = lds + orow * 256;
    auto put = [&](const f32x16& v, int rtile, int ctile) {
#pragma unroll
        for (int q4 = 0; q4 < 4; ++q4)
            *(__attribute__((address_space(3))) f32x4*)(pw + rtile * 8192 + (((ctile * 8 + q4 * 2 + hh) ^ (r & 7)) << 4)) = f32x4{v[4 * q4], v[4 * q4 + 1], v[4 * q4 + 2], v[4 * q4 + 3]};
    };
    auto gather = [&](f32x4& lo, f32x4& hi) {
        lo = f32x4{}; hi = f32x4{};
#pragma unroll
        for (int p = 0; p < 8; ++p) { lo += lds128f(pr + p * 16384 + (((2 * oc) ^ (orow & 7)) << 4)); hi += lds128f(pr + p * 16384 + (((2 * oc + 1) ^ (orow & 7)) << 4)); }
    };
    BAR();
    put(d00, 0, 0); put(d01, 0, 1); put(d10, 1, 0); put(d11, 1, 1);
    BAR();
    f32x4 glo, ghi; gather(glo, ghi);
#pragma unroll
    for (int e = 0; e < 4; ++e) { glo[e] *= __builtin_amdgcn_rcpf(1.0f + __builtin_amdgcn_exp2f(glo[e] * -1.4426950408889634f));
                                  ghi[e] *= __builtin_amdgcn_rcpf(1.0f + __builtin_amdgcn_exp2f(ghi[e] * -1.4426950408889634f)); }
    asm volatile("" : "+v"(glo), "+v"(ghi));
    STAMP(13);
    { int l3 = __builtin_amdgcn_mbcnt_hi(~0u, __builtin_amdgcn_mbcnt_lo(~0u, 0u)); asm volatile("" : "+v"(l3));
      wait_tags(ws, WS_F3 + ((2 * rt64 + ((l3 >> 2) & 1)) * 32 + 4 * w + (l3 & 3)) * 8, tag); }
    STAMP(14);
    { const char* bo = ws + WS_OF + (size_t)(2 * rt64 * 32 + 4 * w) * 1024;
      u32x4 o00, o01, o02, o03, o10, o11, o12, o13;
      LOAD_DEV(o00, l16, bo); LOAD_DEV(o10, l16, bo + 32768); LOAD_DEV(o01, l16, bo + 1024); LOAD_DEV(o11, l16, bo + 32768 + 1024);
      LOAD_DEV(o02, l16, bo + 2048); LOAD_DEV(o12, l16, bo + 32768 + 2048); LOAD_DEV(o03, l16, bo + 3072); LOAD_DEV(o13, l16, bo + 32768 + 3072);
      const f32x16 z = {};
      asm volatile("s_waitcnt vmcnt(6)" : "+v"(o00), "+v"(o10)::"memory");
      d00 = MFMA(wo0a, __builtin_bit_cast(bf16x8, o00), z); d01 = MFMA(wo0b, __builtin_bit_cast(bf16x8, o00), z);
      d10 = MFMA(wo0a, __builtin_bit_cast(bf16x8, o10), z); d11 = MFMA(wo0b, __builtin_bit_cast(bf16x8, o10), z);
      asm volatile("s_waitcnt vmcnt(4)" : "+v"(o01), "+v"(o11), "+v"(d00)::"memory");
      d00 = MFMA(wo1a, __builtin_bit_cast(bf16x8, o01), d00); d01 = MFMA(wo1b, __builtin_bit_cast(bf16x8, o01), d01);
      d10 = MFMA(wo1a, __builtin_bit_cast(bf16x8, o11), d10); d11 = MFMA(wo1b, __builtin_bit_cast(bf16x8, o11), d11);
      asm volatile("s_waitcnt vmcnt(2)" : "+v"(o02), "+v"(o12), "+v"(d00)::"memory");
      d00 = MFMA(wo2a, __builtin_bit_cast(bf16x8, o02), d00); d01 = MFMA(wo2b, __builtin_bit_cast(bf16x8, o02), d01);
      d10 = MFMA(wo2a, __builtin_bit_cast(bf16x8, o12), d10); d11 = MFMA(wo2b, __builtin_bit_cast(bf16x8, o12), d11);
      asm volatile("s_waitcnt vmcnt(0)" : "+v"(o03), "+v"(o13), "+v"(d00)::"memory");
      d00 = MFMA(wo3a, __builtin_bit_cast(bf16x8, o03), d00); d01 = MFMA(wo3b, __builtin_bit_cast(bf16x8, o03), d01);
      d10 = MFMA(wo3a, __builtin_bit_cast(bf16x8, o13), d10); d11 = MFMA(wo3b, __builtin_bit_cast(bf16x8, o13), d11); }
    BAR();
    put(d00, 0, 0); put(d01, 0, 1); put(d10, 1, 0); put(d11, 1, 1);
    BAR();
    f32x4 slo, shi; gather(slo, shi);
    u32x4 res;
#pragma unroll
    for (int e = 0; e < 2; ++e) {
        res[e] = cvtpk(__builtin_fmaf(slo[2 * e], glo[2 * e], __uint_as_float(xres[e] << 16)), __builtin_fmaf(slo[2 * e + 1], glo[2 * e + 1], __uint_as_float(xres[e] & 0xffff0000u)));
        res[2 + e] = cvtpk(__builtin_fmaf(shi[2 * e], ghi[2 * e], __uint_as_float(xres[2 + e] << 16)), __builtin_fmaf(shi[2 * e + 1], ghi[2 * e + 1], __uint_as_float(xres[2 + e] & 0xffff0000u)));
    }
    *(u32x4*)(gout + ooff) = res;
    STAMP(15);
    STAMP_FLUSH;
#undef lane
#undef r
#undef hh
#undef blk
#undef tq
#undef tp
#undef tid
}

extern "C" void kernel_launch(void* const* d_in, const int* in_sizes, int n_in, void* d_out, int out_size, void* d_ws, size_t ws_size,
                              hipStream_t stream) {
    (void)hipFuncSetAttribute((const void*)fused_block, hipFuncAttributeMaxDynamicSharedMemorySize, LDS_BYTES);
    fused_block<<<256, 512, LDS_BYTES, stream>>>((const char*)d_in[0], (const char*)d_in[1], (const char*)d_in[2], (const char*)d_in[4],
                                                 (const char*)d_in[5], (const char*)d_in[6], (char*)d_out, (char*)d_ws);
}
```

```cpp
#include <hip/hip_runtime.h>

extern "C" __global__ void __launch_bounds__(512, 2) __attribute__((amdgpu_num_vgpr(8)))
fused_block(const void* gx, const void* gwqkv, const void* gwo, const void* gwg, const void* gwu, const void* gwd, void* gout, void* ws) {
    asm volatile(
        "s_load_dwordx16 s[4:19], s[0:1], 0x0\n"
        "s_getpc_b64 s[56:57]\n"
        ".Lpc_%=:\n"
        "s_lshr_b32 s39, s2, 3\n"
        "s_mul_i32 s39, s39, 1280\n"
        "v_lshl_add_u32 v10, v0, 7, s39\n"
        "v_min_u32 v10, .Lend_%=-.Lpc_%=-4, v10\n"
        "v_cmp_gt_u32 vcc, 10, v0\n"
        "s_mov_b32 m0, 163584\n"
        "s_and_b64 exec, exec, vcc\n"
        "s_cbranch_execz .Lnopf_%=\n"
        "global_load_lds_dword v10, s[56:57]\n"
        ".Lnopf_%=:\n"
        "s_mov_b64 exec, -1\n"
        "v_lshrrev_b32 v1, 6, v0\n"
        "v_and_b32 v2, 63, v0\n"
        "s_and_b32 s23, s2, 7\n"
        "v_readfirstlane_b32 s20, v1\n"
        "s_lshr_b32 s24, s2, 3\n"
        "v_lshrrev_b32 v3, 4, v2\n"
        "v_lshrrev_b32 v4, 3, v2\n"
        "v_xor_b32 v3, v3, v2\n"
        "v_and_b32 v3, 7, v3\n"
        "v_lshlrev_b32 v3, 4, v3\n"
        "v_lshl_or_b32 v24, v4, 10, v3\n"
        "v_xor_b32 v25, 64, v24\n"
        "v_add_u32 v25, 7168, v25\n"
        "s_lshr_b32 s25, s23, 1\n"
        "s_lshl_b32 s25, s25, 2\n"
        "s_lshr_b32 s39, s24, 3\n"
        "s_add_u32 s25, s25, s39\n"
        "s_lshl_b32 s56, s25, 17\n"
        "s_lshl_b32 s57, s20, 14\n"
        "s_add_u32 s40, s56, s57\n"
        "s_lshl_b32 s50, s20, 11\n"
        "s_lshl_b32 s51, s20, 10\n"
        "s_and_b32 s26, s23, 1\n"
        "s_lshl_b32 s26, s26, 3\n"
        "s_and_b32 s39, s24, 7\n"
        "s_add_u32 s26, s26, s39\n"
        "s_and_b32 s21, s20, 3\n"
        "s_lshr_b32 s22, s20, 2\n"
        "s_lshl_b32 s39, s20, 6\n"
        "v_add_u32 v5, s39, v2\n"
        "v_mul_u32_u24 v6, 0xaaab, v5\n"
        "v_lshrrev_b32 v6, 19, v6\n"
        "v_lshlrev_b32 v7, 4, v5\n"
        "s_movk_i32 s57, 2880\n"
        "v_mad_u32_u24 v26, v6, s57, v7\n"
        "s_lshl_b32 s39, s21, 6\n"
        "s_add_u32 s39, s39, 512\n"
        "v_add_u32 v8, s39, v2\n"
        "v_mul_u32_u24 v9, 0xaaab, v8\n"
        "v_lshrrev_b32 v9, 19, v9\n"
        "v_lshlrev_b32 v8, 4, v8\n"
        "v_mad_u32_u24 v27, v9, s57, v8\n"
        "s_mul_i32 s41, s26, 192\n"
        "s_lshr_b32 s39, s24, 3\n"
        "s_lshl_b32 s39, s39, 1\n"
        "s_add_u32 s39, s39, s24\n"
        "s_and_b32 s58, s39, 6\n"
        "s_sub_u32 s43, 7, s58\n"
        "s_mov_b32 s98, s40\n"
        "s_mov_b32 s99, s41\n"
        "s_lshl_b32 s39, s58, 7\n"
        "s_add_u32 s40, s40, s39\n"
        "s_mul_i32 s39, s58, 196608\n"
        "s_add_u32 s41, s41, s39\n"
        "s_mov_b32 s62, 2097152\n"
        "s_mov_b32 s63, 0x20000\n"
        "s_mov_b32 s66, 1572864\n"
        "s_mov_b32 s67, 0x20000\n"
        "s_waitcnt lgkmcnt(0)\n"
        "s_mov_b32 s60, s4\n"
        "s_and_b32 s61, s5, 0xffff\n"
        "s_mov_b32 s64, s6\n"
        "s_and_b32 s65, s7, 0xffff\n"
        "s_add_u32 m0, s50, 0\n"
        "s_nop 0\n"
        "buffer_load_dwordx4 v24, s[60:63], s40 offen lds\n"
        "buffer_load_dwordx4 v25, s[60:63], s40 offen offset:1024 lds\n"
        "s_load_dwordx2 s[30:31], s[18:19], 0x0\n"
        "v_mov_b32 v40, 0x3f803f80\n"
        "v_mov_b32 v41, 0x3f803f80\n"
        "v_and_b32 v3, 31, v2\n"
        "v_lshrrev_b32 v4, 5, v2\n"
        "v_lshrrev_b32 v13, 1, v2\n"
        "v_xor_b32 v13, v13, v4\n"
        "s_add_u32 m0, s51, 16384\n"
        "s_nop 0\n"
        "buffer_load_dwordx4 v26, s[64:67], s41 offen lds\n"
        "v_and_b32 v13, 7, v13\n"
        "v_lshlrev_b32 v13, 4, v13\n"
        "v_lshl_or_b32 v13, v3, 7, v13\n"
        "s_lshl_b32 s39, s21, 12\n"
        "v_add_u32 v13, s39, v13\n"
        "v_bfe_u32 v14, v2, 2, 2\n"
        "v_lshl_add_u32 v14, v4, 3, v14\n"
        "s_cmp_lg_u32 s22, 0\n"
        "s_cbranch_scc1 .Lpw0_%=\n"
        "s_add_u32 m0, s51, 24576\n"
        "s_nop 0\n"
        "buffer_load_dwordx4 v27, s[64:67], s41 offen lds\n"
        ".Lpw0_%=:\n"
        "s_add_u32 s41, s41, 196608\n"
        "s_add_u32 s40, s40, 128\n"
        "s_cmp_eq_u32 s43, 0\n"
        "s_cselect_b32 s41, s99, s41\n"
        "s_cselect_b32 s40, s98, s40\n"
        "s_barrier\n"
        "v_mul_u32_u24 v14, 192, v14\n"
        "v_and_b32 v7, 3, v2\n"
        "v_lshl_add_u32 v14, v7, 3, v14\n"
        "v_bfe_u32 v7, v2, 4, 1\n"
        "s_lshl_b32 s39, s22, 7\n"
        "v_lshl_add_u32 v8, v7, 5, v14\n"
        "v_add_u32 v8, s39, v8\n"
        "s_add_u32 m0, s50, 28672\n"
        "s_nop 0\n"
        "buffer_load_dwordx4 v24, s[60:63], s40 offen lds\n"
        "s_add_u32 s39, s21, 0\n"
        "s_and_b32 s39, s39, 3\n"
        "s_lshl_b32 s59, s39, 5\n"
        "v_xor_b32 v16, s59, v13\n"
        "s_mul_i32 s39, s39, 3072\n"
        "v_add_u32 v20, s39, v8\n"
        "s_add_u32 s39, s21, 1\n"
        "s_and_b32 s39, s39, 3\n"
        "buffer_load_dwordx4 v25, s[60:63], s40 offen offset:1024 lds\n"
        "s_lshl_b32 s59, s39, 5\n"
        "v_xor_b32 v17, s59, v13\n"
        "s_mul_i32 s39, s39, 3072\n"
        "v_add_u32 v21, s39, v8\n"
        "s_add_u32 s39, s21, 2\n"
        "s_and_b32 s39, s39, 3\n"
        "s_lshl_b32 s59, s39, 5\n"
        "s_add_u32 m0, s51, 45056\n"
        "s_nop 0\n"
        "buffer_load_dwordx4 v26, s[64:67], s41 offen lds\n"
        "v_xor_b32 v18, s59, v13\n"
        "s_mul_i32 s39, s39, 3072\n"
        "v_add_u32 v22, s39, v8\n"
        "s_add_u32 s39, s21, 3\n"
        "s_and_b32 s39, s39, 3\n"
        "s_lshl_b32 s59, s39, 5\n"
        "v_xor_b32 v19, s59, v13\n"
        "s_cmp_lg_u32 s22, 0\n"
        "s_cbranch_scc1 .Lpw1_%=\n"
        "s_add_u32 m0, s51, 53248\n"
        "s_nop 0\n"
        "buffer_load_dwordx4 v27, s[64:67], s41 offen lds\n"
        ".Lpw1_%=:\n"
        "s_add_u32 s41, s41, 196608\n"
        "s_add_u32 s40, s40, 128\n"
        "s_cmp_eq_u32 s43, 1\n"
        "s_cselect_b32 s41, s99, s41\n"
        "s_cselect_b32 s40, s98, s40\n"
        "s_mul_i32 s39, s39, 3072\n"
        "v_add_u32 v23, s39, v8\n"
        "s_mul_i32 s36, s26, 3\n"
        "s_lshl_b32 s39, s22, 1\n"
        "s_add_u32 s36, s36, s39\n"
        "s_cmp_lt_u32 s26, 6\n"
        "s_cselect_b32 s28, 0, s22\n"
        "s_cmp_gt_u32 s36, 31\n"
        "s_cselect_b32 s53, 1, 0\n"
        "s_add_u32 s53, s53, s39\n"
        "s_cmp_eq_u32 s53, 1\n"
        "s_cbranch_scc1 .LA1_%=\n"
        "s_cmp_eq_u32 s53, 2\n"
        "s_cbranch_scc1 .LA2_%=\n"
        "s_cmp_eq_u32 s53, 3\n"
        "s_cbranch_scc1 .LA3_%=\n"
        ".LA0_%=:\n"
        "s_waitcnt vmcnt(4) lgkmcnt(0)\n"
        "s_barrier\n"
        "ds_read_b128 v[128:131], v16 offset:0\n"
        "ds_read_b64_tr_b16 v[64:65], v20 offset:16384\n"
        "ds_read_b64_tr_b16 v[66:67], v20 offset:17152\n"
        "ds_read_b64_tr_b16 v[68:69], v20 offset:16448\n"
        "ds_read_b64_tr_b16 v[70:71], v20 offset:17216\n"
        "ds_read_b128 v[132:135], v17 offset:0\n"
        "s_add_u32 s30, s30, 0x7f4a7c15\n"
        "s_addc_u32 s31, s31, 0x9e3779b9\n"
        "s_waitcnt lgkmcnt(3)\n"
        "v_mfma_f32_32x32x16_bf16 v[80:95], v[64:67], v[128:131], 0\n"
        "ds_read_b64_tr_b16 v[112:113], v21 offset:16384\n"
        "ds_read_b64_tr_b16 v[114:115], v21 offset:17152\n"
        "ds_read_b128 v[136:139], v18 offset:0\n"
        "s_add_u32 m0, s50, 57344\n"
        "s_waitcnt lgkmcnt(4)\n"
        "v_mfma_f32_32x32x16_bf16 v[96:111], v[68:71], v[128:131], 0\n"
        "ds_read_b64_tr_b16 v[116:117], v21 offset:16448\n"
        "ds_read_b64_tr_b16 v[118:119], v21 offset:17216\n"
        "buffer_load_dwordx4 v24, s[60:63], s40 offen lds\n"
        "v_mfma_f32_4x4x4_16b_bf16 v[44:47], v[40:41], v[64:65], 0\n"
        "buffer_load_dwordx4 v25, s[60:63], s40 offen offset:1024 lds\n"
        "s_add_u32 m0, s51, 73728\n"
        "s_nop 0\n"
        "buffer_load_dwordx4 v26, s[64:67], s41 offen lds\n"
        "v_mfma_f32_4x4x4_16b_bf16 v[48:51], v[40:41], v[68:69], 0\n"
        "s_mul_i32 s27, s26, 6\n"
        "s_mul_i32 s39, s26, 5\n"
        "s_add_u32 s39, s39, 6\n"
        "v_mfma_f32_4x4x4_16b_bf16 v[44:47], v[40:41], v[66:67], v[44:47]\n"
        "s_cmp_lt_u32 s26, 6\n"
        "s_cselect_b32 s27, s27, s39\n"
        "s_cselect_b32 s56, 12, 10\n"
        "v_mfma_f32_4x4x4_16b_bf16 v[48:51], v[40:41], v[70:71], v[48:51]\n"
        "s_lshl_b32 s42, s27, 5\n"
        "s_mov_b32 s47, s42\n"
        "s_sub_u32 s39, 7, s43\n"
        "s_waitcnt lgkmcnt(3)\n"
        "v_mfma_f32_32x32x16_bf16 v[80:95], v[112:115], v[132:135], v[80:95]\n"
        "ds_read_b64_tr_b16 v[64:65], v22 offset:16384\n"
        "ds_read_b64_tr_b16 v[66:67], v22 offset:17152\n"
        "ds_read_b128 v[140:143], v19 offset:0\n"
        "s_add_u32 m0, s51, 81920\n"
        "s_nop 0\n"
        "buffer_load_dwordx4 v27, s[64:67], s41 offen lds\n"
        "s_add_u32 s41, s41, 196608\n"
        "s_add_u32 s40, s40, 128\n"
        "s_cmp_eq_u32 s43, 2\n"
        "s_cselect_b32 s41, s99, s41\n"
        "s_cselect_b32 s40, s98, s40\n"
        "s_add_u32 m0, s50, 86016\n"
        "s_waitcnt lgkmcnt(3)\n"
        "v_mfma_f32_32x32x16_bf16 v[96:111], v[116:119], v[132:135], v[96:111]\n"
        "ds_read_b64_tr_b16 v[68:69], v22 offset:16448\n"
        "ds_read_b64_tr_b16 v[70:71], v22 offset:17216\n"
        "buffer_load_dwordx4 v24, s[60:63], s40 offen lds\n"
        "buffer_load_dwordx4 v25, s[60:63], s40 offen offset:1024 lds\n"
        "s_mul_i32 s39, s39, 176128\n"
        "s_add_u32 s42, s42, s39\n"
        "s_mov_b32 s68, s10\n"
        "s_and_b32 s69, s11, 0xffff\n"
        "s_mov_b32 s70, 1409024\n"
        "s_mov_b32 s71, 0x20000\n"
        "s_mov_b32 s76, s12\n"
        "s_waitcnt lgkmcnt(3)\n"
        "v_mfma_f32_32x32x16_bf16 v[80:95], v[64:67], v[136:139], v[80:95]\n"
        "ds_read_b64_tr_b16 v[112:113], v23 offset:16384\n"
        "ds_read_b64_tr_b16 v[114:115], v23 offset:17152\n"
        "s_add_u32 m0, s51, 102400\n"
        "s_nop 0\n"
        "buffer_load_dwordx4 v26, s[64:67], s41 offen lds\n"
        "s_add_u32 m0, s51, 110592\n"
        "s_nop 0\n"
        "buffer_load_dwordx4 v27, s[64:67], s41 offen lds\n"
        "s_add_u32 s41, s41, 196608\n"
        "s_add_u32 s40, s40, 128\n"
        "s_cmp_eq_u32 s43, 3\n"
        "s_cselect_b32 s41, s99, s41\n"
        "s_cselect_b32 s40, s98, s40\n"
        "s_waitcnt lgkmcnt(2)\n"
        "v_mfma_f32_32x32x16_bf16 v[96:111], v[68:71], v[136:139], v[96:111]\n"
        "ds_read_b64_tr_b16 v[116:117], v23 offset:16448\n"
        "ds_read_b64_tr_b16 v[118:119], v23 offset:17216\n"
        "s_and_b32 s77, s13, 0xffff\n"
        "s_mov_b32 s78, 1409024\n"
        "s_mov_b32 s79, 0x20000\n"
        "s_cmp_eq_u32 s22, 0\n"
        "s_cselect_b64 s[72:73], s[68:69], s[76:77]\n"
        "s_mov_b64 s[74:75], s[70:71]\n"
        "s_mul_i32 s39, s22, 32\n"
        "s_add_u32 s39, s39, 8192\n"
        "s_waitcnt vmcnt(8) lgkmcnt(0)\n"
        "s_barrier\n"
        "v_mfma_f32_32x32x16_bf16 v[80:95], v[112:115], v[140:143], v[80:95]\n"
        "ds_read_b128 v[144:147], v16 offset:28672\n"
        "ds_read_b64_tr_b16 v[64:65], v20 offset:45056\n"
        "ds_read_b64_tr_b16 v[66:67], v20 offset:45824\n"
        "ds_read_b128 v[148:151], v17 offset:28672\n"
        "s_add_u32 m0, s50, 114688\n"
        "s_nop 0\n"
        "buffer_load_dwordx4 v24, s[60:63], s40 offen lds\n"
        "s_add_u32 s52, s51, s39\n"
        "v_mfma_f32_32x32x16_bf16 v[96:111], v[116:119], v[140:143], v[96:111]\n"
        "ds_read_b64_tr_b16 v[68:69], v20 offset:45120\n"
        "ds_read_b64_tr_b16 v[70:71], v20 offset:45888\n"
        "s_mul_i32 s58, s22, -768\n"
        "s_add_u32 s58, s58, 512\n"
        "s_movk_i32 s57, 2560\n"
        "v_lshlrev_b32 v10, 4, v5\n"
        "v_mad_u32_u24 v32, v6, s57, v10\n"
        "s_waitcnt lgkmcnt(3)\n"
        "v_mfma_f32_32x32x16_bf16 v[80:95], v[64:67], v[144:147], v[80:95]\n"
        "ds_read_b64_tr_b16 v[112:113], v21 offset:45056\n"
        "ds_read_b64_tr_b16 v[114:115], v21 offset:45824\n"
        "ds_read_b128 v[152:155], v18 offset:28672\n"
        "s_waitcnt lgkmcnt(3)\n"
        "v_mfma_f32_32x32x16_bf16 v[96:111], v[68:71], v[144:147], v[96:111]\n"
        "ds_read_b64_tr_b16 v[116:117], v21 offset:45120\n"
        "ds_read_b64_tr_b16 v[118:119], v21 offset:45888\n"
        "v_mfma_f32_4x4x4_16b_bf16 v[44:47], v[40:41], v[64:65], v[44:47]\n"
        "buffer_load_dwordx4 v25, s[60:63], s40 offen offset:1024 lds\n"
        "v_mul_u32_u24 v10, 12, v6\n"
        "v_mfma_f32_4x4x4_16b_bf16 v[48:51], v[40:41], v[68:69], v[48:51]\n"
        "v_sub_u32 v10, v5, v10\n"
        "v_cmp_le_u32 vcc, s56, v10\n"
        "v_mfma_f32_4x4x4_16b_bf16 v[44:47], v[40:41], v[66:67], v[44:47]\n"
        "v_subrev_u32 v10, 32, v32\n"
        "v_cndmask_b32 v32, v32, v10, vcc\n"
        "v_mfma_f32_4x4x4_16b_bf16 v[48:51], v[40:41], v[70:71], v[48:51]\n"
        "v_add_u32 v11, s58, v5\n"
        "v_mul_u32_u24 v12, 0xaaab, v11\n"
        "s_waitcnt lgkmcnt(3)\n"
        "v_mfma_f32_32x32x16_bf16 v[80:95], v[112:115], v[148:151], v[80:95]\n"
        "ds_read_b64_tr_b16 v[64:65], v22 offset:45056\n"
        "ds_read_b64_tr_b16 v[66:67], v22 offset:45824\n"
        "ds_read_b128 v[156:159], v19 offset:28672\n"
        "s_add_u32 m0, s51, 131072\n"
        "s_nop 0\n"
        "buffer_load_dwordx4 v26, s[64:67], s41 offen lds\n"
        "v_lshrrev_b32 v12, 19, v12\n"
        "v_lshlrev_b32 v10, 4, v11\n"
        "s_waitcnt lgkmcnt(3)\n"
        "v_mfma_f32_32x32x16_bf16 v[96:111], v[116:119], v[148:151], v[96:111]\n"
        "ds_read_b64_tr_b16 v[68:69], v22 offset:45120\n"
        "ds_read_b64_tr_b16 v[70:71], v22 offset:45888\n"
        "v_mad_u32_u24 v33, v12, s57, v10\n"
        "v_mul_u32_u24 v10, 12, v12\n"
        "v_sub_u32 v10, v11, v10\n"
        "v_cmp_le_u32 vcc, s56, v10\n"
        "v_subrev_u32 v10, 32, v33\n"
        "s_waitcnt lgkmcnt(3)\n"
        "v_mfma_f32_32x32x16_bf16 v[80:95], v[64:67], v[152:155], v[80:95]\n"
        "ds_read_b64_tr_b16 v[112:113], v23 offset:45056\n"
        "ds_read_b64_tr_b16 v[114:115], v23 offset:45824\n"
        "s_add_u32 m0, s51, 139264\n"
        "s_nop 0\n"
        "buffer_load_dwordx4 v27, s[64:67], s41 offen lds\n"
        "s_add_u32 s41, s41, 196608\n"
        "s_add_u32 s40, s40, 128\n"
        "s_cmp_eq_u32 s43, 4\n"
        "s_cselect_b32 s41, s99, s41\n"
        "s_cselect_b32 s40, s98, s40\n"
        "s_waitcnt lgkmcnt(2)\n"
        "v_mfma_f32_32x32x16_bf16 v[96:111], v[68:71], v[152:155], v[96:111]\n"
        "ds_read_b64_tr_b16 v[116:117], v23 offset:45120\n"
        "ds_read_b64_tr_b16 v[118:119], v23 offset:45888\n"
        "v_cndmask_b32 v33, v33, v10, vcc\n"
        "v_add_u32 v11, 256, v5\n"
        "v_mul_u32_u24 v12, 0xaaab, v11\n"
        "v_lshrrev_b32 v12, 19, v12\n"
        "v_lshlrev_b32 v10, 4, v11\n"
        "v_mad_u32_u24 v34, v12, s57, v10\n"
        "v_mul_u32_u24 v10, 12, v12\n"
        "s_waitcnt vmcnt(8) lgkmcnt(0)\n"
        "s_barrier\n"
        "v_mfma_f32_32x32x16_bf16 v[80:95], v[112:115], v[156:159], v[80:95]\n"
        "v_add_u32 v16, 0xe000, v16\n"
        "v_add_u32 v17, 0xe000, v17\n"
        "v_add_u32 v18, 0xe000, v18\n"
        "v_add_u32 v19, 0xe000, v19\n"
        "v_add_u32 v20, 0xe000, v20\n"
        "v_add_u32 v21, 0xe000, v21\n"
        "v_add_u32 v22, 0xe000, v22\n"
        "v_add_u32 v23, 0xe000, v23\n"
        "ds_read_b128 v[160:163], v16 offset:0\n"
        "ds_read_b64_tr_b16 v[64:65], v20 offset:16384\n"
        "ds_read_b64_tr_b16 v[66:67], v20 offset:17152\n"
        "v_mfma_f32_32x32x16_bf16 v[96:111], v[116:119], v[156:159], v[96:111]\n"
        "ds_read_b64_tr_b16 v[68:69], v20 offset:16448\n"
        "ds_read_b64_tr_b16 v[70:71], v20 offset:17216\n"
        "ds_read_b128 v[164:167], v17 offset:0\n"
        "s_add_u32 m0, s50, 0\n"
        "s_nop 0\n"
        "buffer_load_dwordx4 v24, s[60:63], s40 offen lds\n"
        "v_sub_u32 v10, v11, v10\n"
        "v_cmp_le_u32 vcc, s56, v10\n"
        "v_subrev_u32 v10, 32, v34\n"
        "s_nop 0\n"
        "v_cndmask_b32 v34, v34, v10, vcc\n"
        "s_mul_i32 s39, s22, 96\n"
        "s_add_u32 s39, s39, 57344\n"
        "s_waitcnt lgkmcnt(3)\n"
        "v_mfma_f32_32x32x16_bf16 v[80:95], v[64:67], v[160:163], v[80:95]\n"
        "ds_read_b64_tr_b16 v[112:113], v21 offset:16384\n"
        "ds_read_b64_tr_b16 v[114:115], v21 offset:17152\n"
        "ds_read_b128 v[168:171], v18 offset:0\n"
        "s_waitcnt lgkmcnt(4)\n"
        "v_mfma_f32_32x32x16_bf16 v[96:111], v[68:71], v[160:163], v[96:111]\n"
        "ds_read_b64_tr_b16 v[116:117], v21 offset:16448\n"
        "ds_read_b64_tr_b16 v[118:119], v21 offset:17216\n"
        "v_mfma_f32_4x4x4_16b_bf16 v[44:47], v[40:41], v[64:65], v[44:47]\n"
        "buffer_load_dwordx4 v25, s[60:63], s40 offen offset:1024 lds\n"
        "v_mul_u32_u24 v9, 12320, v7\n"
        "v_mfma_f32_4x4x4_16b_bf16 v[48:51], v[40:41], v[68:69], v[48:51]\n"
        "v_add3_u32 v9, v9, v14, s39\n"
        "s_add_u32 s39, s21, 0\n"
        "v_mfma_f32_4x4x4_16b_bf16 v[44:47], v[40:41], v[66:67], v[44:47]\n"
        "s_and_b32 s39, s39, 3\n"
        "s_mul_i32 s39, s39, 3072\n"
        "v_mfma_f32_4x4x4_16b_bf16 v[48:51], v[40:41], v[70:71], v[48:51]\n"
        "v_add_u32 v36, s39, v9\n"
        "s_add_u32 s39, s21, 1\n"
        "s_waitcnt lgkmcnt(3)\n"
        "v_mfma_f32_32x32x16_bf16 v[80:95], v[112:115], v[164:167], v[80:95]\n"
        "ds_read_b64_tr_b16 v[64:65], v22 offset:16384\n"
        "ds_read_b64_tr_b16 v[66:67], v22 offset:17152\n"
        "ds_read_b128 v[172:175], v19 offset:0\n"
        "s_add_u32 m0, s51, 16384\n"
        "s_nop 0\n"
        "buffer_load_dwordx4 v26, s[64:67], s41 offen lds\n"
        "s_and_b32 s39, s39, 3\n"
        "s_mul_i32 s39, s39, 3072\n"
        "s_waitcnt lgkmcnt(3)\n"
        "v_mfma_f32_32x32x16_bf16 v[96:111], v[116:119], v[164:167], v[96:111]\n"
        "ds_read_b64_tr_b16 v[68:69], v22 offset:16448\n"
        "ds_read_b64_tr_b16 v[70:71], v22 offset:17216\n"
        "v_add_u32 v37, s39, v9\n"
        "s_add_u32 s39, s21, 2\n"
        "s_and_b32 s39, s39, 3\n"
        "s_mul_i32 s39, s39, 3072\n"
        "v_add_u32 v38, s39, v9\n"
        "s_waitcnt lgkmcnt(3)\n"
        "v_mfma_f32_32x32x16_bf16 v[80:95], v[64:67], v[168:171], v[80:95]\n"
        "ds_read_b64_tr_b16 v[112:113], v23 offset:16384\n"
        "ds_read_b64_tr_b16 v[114:115], v23 offset:17152\n"
        "s_add_u32 m0, s51, 24576\n"
        "s_nop 0\n"
        "buffer_load_dwordx4 v27, s[64:67], s41 offen lds\n"
        "s_add_u32 s41, s41, 196608\n"
        "s_add_u32 s40, s40, 128\n"
        "s_cmp_eq_u32 s43, 5\n"
        "s_cselect_b32 s41, s99, s41\n"
        "s_cselect_b32 s40, s98, s40\n"
        "s_waitcnt lgkmcnt(2)\n"
        "v_mfma_f32_32x32x16_bf16 v[96:111], v[68:71], v[168:171], v[96:111]\n"
        "ds_read_b64_tr_b16 v[116:117], v23 offset:16448\n"
        "ds_read_b64_tr_b16 v[118:119], v23 offset:17216\n"
        "s_add_u32 s39, s21, 3\n"
        "s_and_b32 s39, s39, 3\n"
        "s_mul_i32 s39, s39, 3072\n"
        "v_add_u32 v39, s39, v9\n"
        "s_lshl_b32 s29, s25, 2\n"
        "s_add_u32 s29, s29, s21\n"
        "s_waitcnt vmcnt(8) lgkmcnt(0)\n"
        "s_barrier\n"
        "v_mfma_f32_32x32x16_bf16 v[80:95], v[112:115], v[172:175], v[80:95]\n"
        "ds_read_b128 v[176:179], v16 offset:28672\n"
        "ds_read_b64_tr_b16 v[64:65], v20 offset:45056\n"
        "ds_read_b64_tr_b16 v[66:67], v20 offset:45824\n"
        "ds_read_b128 v[180:183], v17 offset:28672\n"
        "s_add_u32 m0, s50, 28672\n"
        "s_nop 0\n"
        "buffer_load_dwordx4 v24, s[60:63], s40 offen lds\n"
        "s_mov_b32 s32, s18\n"
        "s_and_b32 s33, s19, 0xffff\n"
        "v_mfma_f32_32x32x16_bf16 v[96:111], v[116:119], v[172:175], v[96:111]\n"
        "ds_read_b64_tr_b16 v[68:69], v20 offset:45120\n"
        "ds_read_b64_tr_b16 v[70:71], v20 offset:45888\n"
        "s_mov_b32 s34, 14090240\n"
        "s_mov_b32 s35, 0x20000\n"
        "s_lshl_b32 s39, s25, 4\n"
        "s_add_u32 s39, s39, s26\n"
        "s_lshl_b32 s39, s39, 3\n"
        "s_waitcnt lgkmcnt(3)\n"
        "v_mfma_f32_32x32x16_bf16 v[80:95], v[64:67], v[176:179], v[80:95]\n"
        "ds_read_b64_tr_b16 v[112:113], v21 offset:45056\n"
        "ds_read_b64_tr_b16 v[114:115], v21 offset:45824\n"
        "ds_read_b128 v[184:187], v18 offset:28672\n"
        "s_waitcnt lgkmcnt(3)\n"
        "v_mfma_f32_32x32x16_bf16 v[96:111], v[68:71], v[176:179], v[96:111]\n"
        "ds_read_b64_tr_b16 v[116:117], v21 offset:45120\n"
        "ds_read_b64_tr_b16 v[118:119], v21 offset:45888\n"
        "v_mfma_f32_4x4x4_16b_bf16 v[44:47], v[40:41], v[64:65], v[44:47]\n"
        "buffer_load_dwordx4 v25, s[60:63], s40 offen offset:1024 lds\n"
        "s_add_u32 s39, s39, 4096\n"
        "v_mfma_f32_4x4x4_16b_bf16 v[48:51], v[40:41], v[68:69], v[48:51]\n"
        "s_add_u32 s54, s18, s39\n"
        "s_addc_u32 s55, s19, 0\n"
        "s_lshl_b32 s37, s23, 2\n"
        "v_mfma_f32_4x4x4_16b_bf16 v[44:47], v[40:41], v[66:67], v[44:47]\n"
        "s_and_b32 s39, s24, 3\n"
        "s_add_u32 s37, s37, s39\n"
        "v_mfma_f32_4x4x4_16b_bf16 v[48:51], v[40:41], v[70:71], v[48:51]\n"
        "s_lshr_b32 s38, s24, 2\n"
        "s_lshl_b32 s39, s38, 3\n"
        "s_waitcnt lgkmcnt(3)\n"
        "v_mfma_f32_32x32x16_bf16 v[80:95], v[112:115], v[180:183], v[80:95]\n"
        "ds_read_b64_tr_b16 v[64:65], v22 offset:45056\n"
        "ds_read_b64_tr_b16 v[66:67], v22 offset:45824\n"
        "ds_read_b128 v[188:191], v19 offset:28672\n"
        "s_add_u32 m0, s51, 45056\n"
        "s_nop 0\n"
        "buffer_load_dwordx4 v26, s[64:67], s41 offen lds\n"
        "s_lshl_b32 s56, s21, 1\n"
        "s_add_u32 s39, s39, s56\n"
        "s_waitcnt lgkmcnt(3)\n"
        "v_mfma_f32_32x32x16_bf16 v[96:111], v[116:119], v[180:183], v[96:111]\n"
        "ds_read_b64_tr_b16 v[68:69], v22 offset:45120\n"
        "ds_read_b64_tr_b16 v[70:71], v22 offset:45888\n"
        "s_add_u32 s39, s39, s22\n"
        "s_lshl_b32 s39, s39, 5\n"
        "s_add_u32 s39, s39, s37\n"
        "s_lshl_b32 s39, s39, 10\n"
        "s_add_u32 s3, s39, 11993088\n"
        "s_waitcnt lgkmcnt(3)\n"
        "v_mfma_f32_32x32x16_bf16 v[80:95], v[64:67], v[184:187], v[80:95]\n"
        "ds_read_b64_tr_b16 v[112:113], v23 offset:45056\n"
        "ds_read_b64_tr_b16 v[114:115], v23 offset:45824\n"
        "s_add_u32 m0, s51, 53248\n"
        "s_nop 0\n"
        "buffer_load_dwordx4 v27, s[64:67], s41 offen lds\n"
        "s_add_u32 s41, s41, 196608\n"
        "s_add_u32 s40, s40, 128\n"
        "s_cmp_eq_u32 s43, 6\n"
        "s_cselect_b32 s41, s99, s41\n"
        "s_cselect_b32 s40, s98, s40\n"
        "s_waitcnt lgkmcnt(2)\n"
        "v_mfma_f32_32x32x16_bf16 v[96:111], v[68:71], v[184:187], v[96:111]\n"
        "ds_read_b64_tr_b16 v[116:117], v23 offset:45120\n"
        "ds_read_b64_tr_b16 v[118:119], v23 offset:45888\n"
        "s_add_u32 s39, s36, 0\n"
        "s_and_b32 s56, s39, 15\n"
        "s_lshl_b32 s56, s56, 7\n"
        "s_add_u32 s56, s56, s29\n"
        "s_lshl_b32 s57, s56, 10\n"
        "s_add_u32 s57, s57, 65536\n"
        "s_waitcnt vmcnt(8) lgkmcnt(0)\n"
        "s_barrier\n"
        "v_mfma_f32_32x32x16_bf16 v[80:95], v[112:115], v[188:191], v[80:95]\n"
        "v_add_u32 v16, 0xe000, v16\n"
        "v_add_u32 v17, 0xe000, v17\n"
        "v_add_u32 v18, 0xe000, v18\n"
        "v_add_u32 v19, 0xe000, v19\n"
        "v_add_u32 v20, 0xe000, v20\n"
        "v_add_u32 v21, 0xe000, v21\n"
        "v_add_u32 v22, 0xe000, v22\n"
        "v_add_u32 v23, 0xe000, v23\n"
        "ds_read_b128 v[192:195], v16 offset:0\n"
        "ds_read_b64_tr_b16 v[64:65], v20 offset:16384\n"
        "ds_read_b64_tr_b16 v[66:67], v20 offset:17152\n"
        "ds_read_b128 v[196:199], v17 offset:0\n"
        "s_add_u32 m0, s50, 57344\n"
        "s_nop 0\n"
        "buffer_load_dwordx4 v24, s[60:63], s40 offen lds\n"
        "v_mfma_f32_32x32x16_bf16 v[96:111], v[116:119], v[188:191], v[96:111]\n"
        "ds_read_b64_tr_b16 v[68:69], v20 offset:16448\n"
        "ds_read_b64_tr_b16 v[70:71], v20 offset:17216\n"
        "s_lshl_b32 s56, s56, 11\n"
        "s_add_u32 s56, s56, 2162688\n"
        "s_mov_b32 s59, 0x20000\n"
        "s_cmp_gt_u32 s39, 31\n"
        "s_cselect_b32 s58, 1024, 0\n"
        "s_cselect_b32 s59, 512, s59\n"
        "s_add_u32 s56, s56, s58\n"
        "s_cmp_lt_u32 s39, 16\n"
        "s_cselect_b32 s92, s57, s56\n"
        "s_cselect_b32 s59, 0x10000, s59\n"
        "s_cselect_b32 s82, 0x3eb8aa3b, 1.0\n"
        "s_add_u32 s94, s92, s59\n"
        "s_waitcnt lgkmcnt(3)\n"
        "v_mfma_f32_32x32x16_bf16 v[80:95], v[64:67], v[192:195], v[80:95]\n"
        "ds_read_b64_tr_b16 v[112:113], v21 offset:16384\n"
        "ds_read_b64_tr_b16 v[114:115], v21 offset:17152\n"
        "ds_read_b128 v[200:203], v18 offset:0\n"
        "s_waitcnt lgkmcnt(3)\n"
        "v_mfma_f32_32x32x16_bf16 v[96:111], v[68:71], v[192:195], v[96:111]\n"
        "ds_read_b64_tr_b16 v[116:117], v21 offset:16448\n"
        "ds_read_b64_tr_b16 v[118:119], v21 offset:17216\n"
        "v_mfma_f32_4x4x4_16b_bf16 v[44:47], v[40:41], v[64:65], v[44:47]\n"
        "buffer_load_dwordx4 v25, s[60:63], s40 offen offset:1024 lds\n"
        "s_add_u32 s39, s36, 1\n"
        "v_mfma_f32_4x4x4_16b_bf16 v[48:51], v[40:41], v[68:69], v[48:51]\n"
        "s_and_b32 s56, s39, 15\n"
        "s_lshl_b32 s56, s56, 7\n"
        "v_mfma_f32_4x4x4_16b_bf16 v[44:47], v[40:41], v[66:67], v[44:47]\n"
        "s_add_u32 s56, s56, s29\n"
        "s_lshl_b32 s57, s56, 10\n"
        "v_mfma_f32_4x4x4_16b_bf16 v[48:51], v[40:41], v[70:71], v[48:51]\n"
        "s_add_u32 s57, s57, 65536\n"
        "s_lshl_b32 s56, s56, 11\n"
        "s_waitcnt lgkmcnt(3)\n"
        "v_mfma_f32_32x32x16_bf16 v[80:95], v[112:115], v[196:199], v[80:95]\n"
        "ds_read_b64_tr_b16 v[64:65], v22 offset:16384\n"
        "ds_read_b64_tr_b16 v[66:67], v22 offset:17152\n"
        "ds_read_b128 v[204:207], v19 offset:0\n"
        "s_add_u32 m0, s51, 73728\n"
        "s_nop 0\n"
        "buffer_load_dwordx4 v26, s[64:67], s41 offen lds\n"
        "s_add_u32 s56, s56, 2162688\n"
        "s_mov_b32 s59, 0x20000\n"
        "s_cmp_gt_u32 s39, 31\n"
        "s_cselect_b32 s58, 1024, 0\n"
        "s_waitcnt lgkmcnt(3)\n"
        "v_mfma_f32_32x32x16_bf16 v[96:111], v[116:119], v[196:199], v[96:111]\n"
        "ds_read_b64_tr_b16 v[68:69], v22 offset:16448\n"
        "ds_read_b64_tr_b16 v[70:71], v22 offset:17216\n"
        "s_cselect_b32 s59, 512, s59\n"
        "s_add_u32 s56, s56, s58\n"
        "s_cmp_lt_u32 s39, 16\n"
        "s_cselect_b32 s93, s57, s56\n"
        "s_cselect_b32 s59, 0x10000, s59\n"
        "s_cselect_b32 s83, 0x3eb8aa3b, 1.0\n"
        "s_add_u32 s95, s93, s59\n"
        "s_waitcnt lgkmcnt(3)\n"
        "v_mfma_f32_32x32x16_bf16 v[80:95], v[64:67], v[200:203], v[80:95]\n"
        "ds_read_b64_tr_b16 v[112:113], v23 offset:16384\n"
        "ds_read_b64_tr_b16 v[114:115], v23 offset:17152\n"
        "s_add_u32 m0, s51, 81920\n"
        "s_nop 0\n"
        "buffer_load_dwordx4 v27, s[64:67], s41 offen lds\n"
        "s_waitcnt lgkmcnt(2)\n"
        "v_mfma_f32_32x32x16_bf16 v[96:111], v[68:71], v[200:203], v[96:111]\n"
        "ds_read_b64_tr_b16 v[116:117], v23 offset:16448\n"
        "ds_read_b64_tr_b16 v[118:119], v23 offset:17216\n"
        "s_add_u32 s41, s41, 196608\n"
        "s_add_u32 s40, s40, 128\n"
        "s_waitcnt vmcnt(8) lgkmcnt(0)\n"
        "s_barrier\n"
        "v_mfma_f32_32x32x16_bf16 v[80:95], v[112:115], v[204:207], v[80:95]\n"
        "v_add_u32 v16, 0xfffe4000, v16\n"
        "v_add_u32 v17, 0xfffe4000, v17\n"
        "v_add_u32 v18, 0xfffe4000, v18\n"
        "v_add_u32 v19, 0xfffe4000, v19\n"
        "v_add_u32 v20, 0xfffe4000, v20\n"
        "v_add_u32 v21, 0xfffe4000, v21\n"
        "v_add_u32 v22, 0xfffe4000, v22\n"
        "v_add_u32 v23, 0xfffe4000, v23\n"
        "ds_read_b128 v[208:211], v16 offset:0\n"
        "ds_read_b64_tr_b16 v[64:65], v20 offset:16384\n"
        "ds_read_b64_tr_b16 v[66:67], v20 offset:17152\n"
        "v_mfma_f32_32x32x16_bf16 v[96:111], v[116:119], v[204:207], v[96:111]\n"
        "ds_read_b64_tr_b16 v[68:69], v20 offset:16448\n"
        "ds_read_b64_tr_b16 v[70:71], v20 offset:17216\n"
        "ds_read_b128 v[212:215], v17 offset:0\n"
        "s_add_u32 m0, s51, 86016\n"
        "s_nop 0\n"
        "buffer_load_dwordx4 v32, s[68:71], s42 offen lds\n"
        "s_waitcnt lgkmcnt(3)\n"
        "v_mfma_f32_32x32x16_bf16 v[80:95], v[64:67], v[208:211], v[80:95]\n"
        "ds_read_b64_tr_b16 v[112:113], v21 offset:16384\n"
        "ds_read_b64_tr_b16 v[114:115], v21 offset:17152\n"
        "s_waitcnt lgkmcnt(3)\n"
        "v_mfma_f32_32x32x16_bf16 v[96:111], v[68:71], v[208:211], v[96:111]\n"
        "ds_read_b64_tr_b16 v[116:117], v21 offset:16448\n"
        "ds_read_b64_tr_b16 v[118:119], v21 offset:17216\n"
        "v_mfma_f32_4x4x4_16b_bf16 v[44:47], v[40:41], v[64:65], v[44:47]\n"
        "v_mfma_f32_4x4x4_16b_bf16 v[48:51], v[40:41], v[68:69], v[48:51]\n"
        "ds_read_b128 v[216:219], v18 offset:0\n"
        "v_mfma_f32_4x4x4_16b_bf16 v[44:47], v[40:41], v[66:67], v[44:47]\n"
        "s_add_u32 m0, s52, 86016\n"
        "v_mfma_f32_4x4x4_16b_bf16 v[48:51], v[40:41], v[70:71], v[48:51]\n"
        "buffer_load_dwordx4 v33, s[72:75], s42 offen lds\n"
        "s_waitcnt lgkmcnt(3)\n"
        "v_mfma_f32_32x32x16_bf16 v[80:95], v[112:115], v[212:215], v[80:95]\n"
        "ds_read_b64_tr_b16 v[64:65], v22 offset:16384\n"
        "ds_read_b64_tr_b16 v[66:67], v22 offset:17152\n"
        "ds_read_b128 v[220:223], v19 offset:0\n"
        "s_add_u32 m0, s51, 102432\n"
        "s_nop 0\n"
        "buffer_load_dwordx4 v34, s[76:79], s42 offen lds\n"
        "s_waitcnt lgkmcnt(4)\n"
        "v_mfma_f32_32x32x16_bf16 v[96:111], v[116:119], v[212:215], v[96:111]\n"
        "ds_read_b64_tr_b16 v[68:69], v22 offset:16448\n"
        "ds_read_b64_tr_b16 v[70:71], v22 offset:17216\n"
        "s_add_u32 s42, s42, 176128\n"
        "s_cmp_eq_u32 s43, 0\n"
        "s_cselect_b32 s42, s47, s42\n"
        "s_waitcnt lgkmcnt(3)\n"
        "v_mfma_f32_32x32x16_bf16 v[80:95], v[64:67], v[216:219], v[80:95]\n"
        "ds_read_b64_tr_b16 v[112:113], v23 offset:16384\n"
        "ds_read_b64_tr_b16 v[114:115], v23 offset:17152\n"
        "s_waitcnt lgkmcnt(2)\n"
        "v_mfma_f32_32x32x16_bf16 v[96:111], v[68:71], v[216:219], v[96:111]\n"
        "ds_read_b64_tr_b16 v[116:117], v23 offset:16448\n"
        "ds_read_b64_tr_b16 v[118:119], v23 offset:17216\n"
        "s_waitcnt vmcnt(7) lgkmcnt(0)\n"
        "s_barrier\n"
        "v_mfma_f32_32x32x16_bf16 v[80:95], v[112:115], v[220:223], v[80:95]\n"
        "ds_read_b128 v[224:227], v16 offset:28672\n"
        "ds_read_b64_tr_b16 v[64:65], v20 offset:45056\n"
        "ds_read_b64_tr_b16 v[66:67], v20 offset:45824\n"
        "ds_read_b128 v[228:231], v17 offset:28672\n"
        "v_mfma_f32_32x32x16_bf16 v[96:111], v[116:119], v[220:223], v[96:111]\n"
        "ds_read_b64_tr_b16 v[68:69], v20 offset:45120\n"
        "ds_read_b64_tr_b16 v[70:71], v20 offset:45888\n"
        "s_add_u32 m0, s51, 114688\n"
        "s_nop 0\n"
        "buffer_load_dwordx4 v32, s[68:71], s42 offen lds\n"
        "s_waitcnt lgkmcnt(3)\n"
        "v_mfma_f32_32x32x16_bf16 v[80:95], v[64:67], v[224:227], v[80:95]\n"
        "ds_read_b64_tr_b16 v[112:113], v21 offset:45056\n"
        "ds_read_b64_tr_b16 v[114:115], v21 offset:45824\n"
        "s_waitcnt lgkmcnt(2)\n"
        "v_mfma_f32_32x32x16_bf16 v[96:111], v[68:71], v[224:227], v[96:111]\n"
        "ds_read_b64_tr_b16 v[116:117], v21 offset:45120\n"
        "ds_read_b64_tr_b16 v[118:119], v21 offset:45888\n"
        "v_mfma_f32_4x4x4_16b_bf16 v[44:47], v[40:41], v[64:65], v[44:47]\n"
        "v_mfma_f32_4x4x4_16b_bf16 v[48:51], v[40:41], v[68:69], v[48:51]\n"
        "ds_read_b128 v[232:235], v18 offset:28672\n"
        "v_mfma_f32_4x4x4_16b_bf16 v[44:47], v[40:41], v[66:67], v[44:47]\n"
        "s_add_u32 m0, s52, 114688\n"
        "v_mfma_f32_4x4x4_16b_bf16 v[48:51], v[40:41], v[70:71], v[48:51]\n"
        "buffer_load_dwordx4 v33, s[72:75], s42 offen lds\n"
        "s_waitcnt lgkmcnt(3)\n"
        "v_mfma_f32_32x32x16_bf16 v[80:95], v[112:115], v[228:231], v[80:95]\n"
        "ds_read_b64_tr_b16 v[64:65], v22 offset:45056\n"
        "ds_read_b64_tr_b16 v[66:67], v22 offset:45824\n"
        "ds_read_b128 v[236:239], v19 offset:28672\n"
        "s_add_u32 m0, s51, 131104\n"
        "s_nop 0\n"
        "buffer_load_dwordx4 v34, s[76:79], s42 offen lds\n"
        "s_waitcnt lgkmcnt(4)\n"
        "v_mfma_f32_32x32x16_bf16 v[96:111], v[116:119], v[228:231], v[96:111]\n"
        "ds_read_b64_tr_b16 v[68:69], v22 offset:45120\n"
        "ds_read_b64_tr_b16 v[70:71], v22 offset:45888\n"
        "s_add_u32 s42, s42, 176128\n"
        "s_cmp_eq_u32 s43, 1\n"
        "s_cselect_b32 s42, s47, s42\n"
        "s_waitcnt lgkmcnt(3)\n"
        "v_mfma_f32_32x32x16_bf16 v[80:95], v[64:67], v[232:235], v[80:95]\n"
        "ds_read_b64_tr_b16 v[112:113], v23 offset:45056\n"
        "ds_read_b64_tr_b16 v[114:115], v23 offset:45824\n"
        "s_waitcnt lgkmcnt(2)\n"
        "v_mfma_f32_32x32x16_bf16 v[96:111], v[68:71], v[232:235], v[96:111]\n"
        "ds_read_b64_tr_b16 v[116:117], v23 offset:45120\n"
        "ds_read_b64_tr_b16 v[118:119], v23 offset:45888\n"
        "s_waitcnt vmcnt(6) lgkmcnt(0)\n"
        "s_barrier\n"
        "v_mfma_f32_32x32x16_bf16 v[80:95], v[112:115], v[236:239], v[80:95]\n"
        "v_add_u32 v16, 0xe000, v16\n"
        "v_add_u32 v17, 0xe000, v17\n"
        "v_add_u32 v18, 0xe000, v18\n"
        "v_add_u32 v19, 0xe000, v19\n"
        "v_add_u32 v20, 0xe000, v20\n"
        "v_add_u32 v21, 0xe000, v21\n"
        "v_add_u32 v22, 0xe000, v22\n"
        "v_add_u32 v23, 0xe000, v23\n"
        "ds_read_b128 v[240:243], v16 offset:0\n"
        "ds_read_b64_tr_b16 v[64:65], v20 offset:16384\n"
        "ds_read_b64_tr_b16 v[66:67], v20 offset:17152\n"
        "v_mfma_f32_32x32x16_bf16 v[96:111], v[116:119], v[236:239], v[96:111]\n"
        "ds_read_b64_tr_b16 v[68:69], v20 offset:16448\n"
        "ds_read_b64_tr_b16 v[70:71], v20 offset:17216\n"
        "ds_read_b128 v[244:247], v17 offset:0\n"
        "s_add_u32 m0, s51, 0\n"
        "s_nop 0\n"
        "buffer_load_dwordx4 v32, s[68:71], s42 offen lds\n"
        "s_waitcnt lgkmcnt(3)\n"
        "v_mfma_f32_32x32x16_bf16 v[80:95], v[64:67], v[240:243], v[80:95]\n"
        "ds_read_b64_tr_b16 v[112:113], v21 offset:16384\n"
        "ds_read_b64_tr_b16 v[114:115], v21 offset:17152\n"
        "s_waitcnt lgkmcnt(3)\n"
        "v_mfma_f32_32x32x16_bf16 v[96:111], v[68:71], v[240:243], v[96:111]\n"
        "ds_read_b64_tr_b16 v[116:117], v21 offset:16448\n"
        "ds_read_b64_tr_b16 v[118:119], v21 offset:17216\n"
        "v_mfma_f32_4x4x4_16b_bf16 v[44:47], v[40:41], v[64:65], v[44:47]\n"
        "v_mfma_f32_4x4x4_16b_bf16 v[48:51], v[40:41], v[68:69], v[48:51]\n"
        "ds_read_b128 v[248:251], v18 offset:0\n"
        "v_mfma_f32_4x4x4_16b_bf16 v[44:47], v[40:41], v[66:67], v[44:47]\n"
        "s_add_u32 m0, s52, 0\n"
        "v_mfma_f32_4x4x4_16b_bf16 v[48:51], v[40:41], v[70:71], v[48:51]\n"
        "buffer_load_dwordx4 v33, s[72:75], s42 offen lds\n"
        "s_waitcnt lgkmcnt(3)\n"
        "v_mfma_f32_32x32x16_bf16 v[80:95], v[112:115], v[244:247], v[80:95]\n"
        "ds_read_b64_tr_b16 v[64:65], v22 offset:16384\n"
        "ds_read_b64_tr_b16 v[66:67], v22 offset:17152\n"
        "ds_read_b128 v[252:255], v19 offset:0\n"
        "s_add_u32 m0, s51, 16416\n"
        "s_nop 0\n"
        "buffer_load_dwordx4 v34, s[76:79], s42 offen lds\n"
        "s_add_u32 s42, s42, 176128\n"
        "s_cmp_eq_u32 s43, 2\n"
        "s_cselect_b32 s42, s47, s42\n"
        "v_lshlrev_b32 v3, 4, v2\n"
        "s_lshl_b32 s39, s21, 2\n"
        "s_waitcnt lgkmcnt(4)\n"
        "v_mfma_f32_32x32x16_bf16 v[96:111], v[116:119], v[244:247], v[96:111]\n"
        "ds_read_b64_tr_b16 v[68:69], v22 offset:16448\n"
        "ds_read_b64_tr_b16 v[70:71], v22 offset:17216\n"
        "s_add_u32 s39, s39, 156160\n"
        "v_add_u32 v4, s39, v3\n"
        "v_and_b32 v5, 31, v2\n"
        "s_lshl_b32 s56, s21, 5\n"
        "v_add_u32 v5, s56, v5\n"
        "v_lshlrev_b32 v6, 4, v5\n"
        "v_add_u32 v6, 153600, v6\n"
        "v_mov_b32 v43, v3\n"
        "s_waitcnt lgkmcnt(3)\n"
        "v_mfma_f32_32x32x16_bf16 v[80:95], v[64:67], v[248:251], v[80:95]\n"
        "ds_read_b64_tr_b16 v[112:113], v23 offset:16384\n"
        "ds_read_b64_tr_b16 v[114:115], v23 offset:17152\n"
        "ds_write_b32 v4, v44 offset:0\n"
        "ds_write_b32 v4, v48 offset:1024\n"
        "s_waitcnt lgkmcnt(4)\n"
        "v_mfma_f32_32x32x16_bf16 v[96:111], v[68:71], v[248:251], v[96:111]\n"
        "ds_read_b64_tr_b16 v[116:117], v23 offset:16448\n"
        "ds_read_b64_tr_b16 v[118:119], v23 offset:17216\n"
        "v_add_u32 v4, 156160, v3\n"
        "s_waitcnt vmcnt(6) lgkmcnt(0)\n"
        "s_barrier\n"
        "ds_read_b64 v[8:9], v6\n"
        "ds_read_b128 v[12:15], v4 offset:0\n"
        "ds_read_b128 v[52:55], v4 offset:1024\n"
        "v_mfma_f32_32x32x16_bf16 v[80:95], v[112:115], v[252:255], v[80:95]\n"
        "v_mfma_f32_32x32x16_bf16 v[96:111], v[116:119], v[252:255], v[96:111]\n"
        "ds_read_b64_tr_b16 v[64:65], v36 offset:28672\n"
        "ds_read_b64_tr_b16 v[66:67], v36 offset:29440\n"
        "ds_read_b64_tr_b16 v[68:69], v36 offset:28704\n"
        "ds_read_b64_tr_b16 v[70:71], v36 offset:29472\n"
        "ds_read_b64_tr_b16 v[72:73], v36 offset:28736\n"
        "ds_read_b64_tr_b16 v[74:75], v36 offset:29504\n"
        "s_add_u32 m0, s51, 28672\n"
        "s_nop 0\n"
        "buffer_load_dwordx4 v32, s[68:71], s42 offen lds\n"
        "s_add_u32 m0, s52, 28672\n"
        "s_nop 0\n"
        "buffer_load_dwordx4 v33, s[72:75], s42 offen lds\n"
        "s_add_u32 m0, s51, 45088\n"
        "s_nop 0\n"
        "buffer_load_dwordx4 v34, s[76:79], s42 offen lds\n"
        "s_add_u32 s42, s42, 176128\n"
        "s_cmp_eq_u32 s43, 3\n"
        "s_cselect_b32 s42, s47, s42\n"
        "v_mov_b32 v46, 0\n"
        "v_mov_b32 v47, 0\n"
        "v_mov_b32 v50, 0\n"
        "v_mov_b32 v51, 0\n"
        "s_waitcnt lgkmcnt(8)\n"
        "v_cvt_pk_bf16_f32 v44, -v8, -v8\n"
        "v_lshlrev_b32 v7, 16, v44\n"
        "v_sub_f32 v7, -v8, v7\n"
        "v_cvt_pk_bf16_f32 v44, -v8, v7\n"
        "v_mov_b32 v45, v44\n"
        "v_mov_b32 v42, v9\n"
        "s_waitcnt lgkmcnt(6)\n"
        "v_add_f32 v52, v52, v53\n"
        "v_add_f32 v54, v54, v55\n"
        "v_add_f32 v52, v52, v54\n"
        "v_cvt_pk_bf16_f32 v48, v52, v52\n"
        "v_lshlrev_b32 v53, 16, v48\n"
        "v_sub_f32 v52, v52, v53\n"
        "v_cvt_pk_bf16_f32 v49, v52, v52\n"
        "s_nop 1\n"
        "v_mfma_f32_32x32x16_bf16 v[16:31], v[48:51], v[44:47], v[96:111]\n"
        "v_add_f32 v12, v12, v13\n"
        "v_add_f32 v14, v14, v15\n"
        "v_add_f32 v12, v12, v14\n"
        "v_cvt_pk_bf16_f32 v48, v12, v12\n"
        "v_lshlrev_b32 v13, 16, v48\n"
        "v_sub_f32 v12, v12, v13\n"
        "v_cvt_pk_bf16_f32 v49, v12, v12\n"
        "s_nop 1\n"
        "v_mfma_f32_32x32x16_bf16 v[0:15], v[48:51], v[44:47], v[80:95]\n"
        "s_waitcnt lgkmcnt(4)\n"
        "v_mfma_f32_32x32x16_bf16 v[80:95], v[64:67], v[128:131], 0\n"
        "ds_read_b64_tr_b16 v[76:77], v37 offset:28672\n"
        "ds_read_b64_tr_b16 v[78:79], v37 offset:29440\n"
        "v_mul_f32 v49, s83, v42\n"
        "v_mul_f32 v16, v16, v49\n"
        "v_mul_f32 v17, v17, v49\n"
        "v_mul_f32 v18, v18, v49\n"
        "v_mul_f32 v19, v19, v49\n"
        "v_mul_f32 v20, v20, v49\n"
        "s_waitcnt lgkmcnt(4)\n"
        "v_mfma_f32_32x32x16_bf16 v[96:111], v[68:71], v[128:131], 0\n"
        "ds_read_b64_tr_b16 v[56:57], v37 offset:28704\n"
        "ds_read_b64_tr_b16 v[58:59], v37 offset:29472\n"
        "v_mul_f32 v21, v21, v49\n"
        "v_mul_f32 v22, v22, v49\n"
        "v_mul_f32 v23, v23, v49\n"
        "v_mul_f32 v24, v24, v49\n"
        "v_mul_f32 v25, v25, v49\n"
        "v_mul_f32 v26, v26, v49\n"
        "s_waitcnt lgkmcnt(4)\n"
        "v_mfma_f32_32x32x16_bf16 v[112:127], v[72:75], v[128:131], 0\n"
        "ds_read_b64_tr_b16 v[60:61], v37 offset:28736\n"
        "ds_read_b64_tr_b16 v[62:63], v37 offset:29504\n"
        "v_mul_f32 v27, v27, v49\n"
        "v_mul_f32 v28, v28, v49\n"
        "v_mul_f32 v29, v29, v49\n"
        "v_mul_f32 v30, v30, v49\n"
        "v_mul_f32 v31, v31, v49\n"
        "v_cvt_pk_bf16_f32 v16, v16, v17\n"
        "s_waitcnt lgkmcnt(4)\n"
        "v_mfma_f32_32x32x16_bf16 v[80:95], v[76:79], v[132:135], v[80:95]\n"
        "ds_read_b64_tr_b16 v[64:65], v38 offset:28672\n"
        "ds_read_b64_tr_b16 v[66:67], v38 offset:29440\n"
        "v_cvt_pk_bf16_f32 v17, v18, v19\n"
        "v_cvt_pk_bf16_f32 v18, v20, v21\n"
        "v_cvt_pk_bf16_f32 v19, v22, v23\n"
        "v_cvt_pk_bf16_f32 v20, v24, v25\n"
        "v_cvt_pk_bf16_f32 v21, v26, v27\n"
        "v_cvt_pk_bf16_f32 v22, v28, v29\n"
        "s_waitcnt lgkmcnt(4)\n"
        "v_mfma_f32_32x32x16_bf16 v[96:111], v[56:59], v[132:135], v[96:111]\n"
        "ds_read_b64_tr_b16 v[68:69], v38 offset:28704\n"
        "ds_read_b64_tr_b16 v[70:71], v38 offset:29472\n"
        "v_cvt_pk_bf16_f32 v23, v30, v31\n"
        "buffer_store_dwordx4 v[16:19], v43, s[32:35], s93 offen sc1\n"
        "buffer_store_dwordx4 v[20:23], v43, s[32:35], s95 offen sc1\n"
        "v_mul_f32 v48, s82, v42\n"
        "v_mul_f32 v0, v0, v48\n"
        "v_mul_f32 v1, v1, v48\n"
        "s_waitcnt lgkmcnt(4)\n"
        "v_mfma_f32_32x32x16_bf16 v[112:127], v[60:63], v[132:135], v[112:127]\n"
        "ds_read_b64_tr_b16 v[72:73], v38 offset:28736\n"
        "ds_read_b64_tr_b16 v[74:75], v38 offset:29504\n"
        "v_mul_f32 v2, v2, v48\n"
        "v_mul_f32 v3, v3, v48\n"
        "v_mul_f32 v4, v4, v48\n"
        "v_mul_f32 v5, v5, v48\n"
        "v_mul_f32 v6, v6, v48\n"
        "v_mul_f32 v7, v7, v48\n"
        "s_waitcnt lgkmcnt(4)\n"
        "v_mfma_f32_32x32x16_bf16 v[80:95], v[64:67], v[136:139], v[80:95]\n"
        "ds_read_b64_tr_b16 v[76:77], v39 offset:28672\n"
        "ds_read_b64_tr_b16 v[78:79], v39 offset:29440\n"
        "v_mul_f32 v8, v8, v48\n"
        "v_mul_f32 v9, v9, v48\n"
        "v_mul_f32 v10, v10, v48\n"
        "v_mul_f32 v11, v11, v48\n"
        "v_mul_f32 v12, v12, v48\n"
        "v_mul_f32 v13, v13, v48\n"
        "s_waitcnt lgkmcnt(4)\n"
        "v_mfma_f32_32x32x16_bf16 v[96:111], v[68:71], v[136:139], v[96:111]\n"
        "ds_read_b64_tr_b16 v[56:57], v39 offset:28704\n"
        "ds_read_b64_tr_b16 v[58:59], v39 offset:29472\n"
        "v_mul_f32 v14, v14, v48\n"
        "v_mul_f32 v15, v15, v48\n"
        "v_cvt_pk_bf16_f32 v0, v0, v1\n"
        "v_cvt_pk_bf16_f32 v1, v2, v3\n"
        "v_cvt_pk_bf16_f32 v2, v4, v5\n"
        "v_cvt_pk_bf16_f32 v3, v6, v7\n"
        "s_waitcnt lgkmcnt(4)\n"
        "v_mfma_f32_32x32x16_bf16 v[112:127], v[72:75], v[136:139], v[112:127]\n"
        "ds_read_b64_tr_b16 v[60:61], v39 offset:28736\n"
        "ds_read_b64_tr_b16 v[62:63], v39 offset:29504\n"
        "v_cvt_pk_bf16_f32 v4, v8, v9\n"
        "v_cvt_pk_bf16_f32 v5, v10, v11\n"
        "v_cvt_pk_bf16_f32 v6, v12, v13\n"
        "v_cvt_pk_bf16_f32 v7, v14, v15\n"
        "buffer_store_dwordx4 v[0:3], v43, s[32:35], s92 offen sc1\n"
        "buffer_store_dwordx4 v[4:7], v43, s[32:35], s94 offen sc1\n"
        "s_waitcnt lgkmcnt(0)\n"
        "s_branch .LC3_%=\n"
        ".LA1_%=:\n"
        "s_waitcnt vmcnt(4) lgkmcnt(0)\n"
        "s_barrier\n"
        "ds_read_b128 v[128:131], v16 offset:0\n"
        "ds_read_b64_tr_b16 v[64:65], v20 offset:16384\n"
        "ds_read_b64_tr_b16 v[66:67], v20 offset:17152\n"
        "ds_read_b64_tr_b16 v[68:69], v20 offset:16448\n"
        "ds_read_b64_tr_b16 v[70:71], v20 offset:17216\n"
        "ds_read_b128 v[132:135], v17 offset:0\n"
        "s_add_u32 s30, s30, 0x7f4a7c15\n"
        "s_addc_u32 s31, s31, 0x9e3779b9\n"
        "s_waitcnt lgkmcnt(3)\n"
        "v_mfma_f32_32x32x16_bf16 v[80:95], v[128:131], v[64:67], 0\n"
        "ds_read_b64_tr_b16 v[112:113], v21 offset:16384\n"
        "ds_read_b64_tr_b16 v[114:115], v21 offset:17152\n"
        "ds_read_b128 v[136:139], v18 offset:0\n"
        "s_add_u32 m0, s50, 57344\n"
        "s_waitcnt lgkmcnt(4)\n"
        "v_mfma_f32_32x32x16_bf16 v[96:111], v[128:131], v[68:71], 0\n"
        "ds_read_b64_tr_b16 v[116:117], v21 offset:16448\n"
        "ds_read_b64_tr_b16 v[118:119], v21 offset:17216\n"
        "buffer_load_dwordx4 v24, s[60:63], s40 offen lds\n"
        "v_mfma_f32_4x4x4_16b_bf16 v[44:47], v[40:41], v[64:65], 0\n"
        "buffer_load_dwordx4 v25, s[60:63], s40 offen offset:1024 lds\n"
        "s_add_u32 m0, s51, 73728\n"
        "s_nop 0\n"
        "buffer_load_dwordx4 v26, s[64:67], s41 offen lds\n"
        "v_mfma_f32_4x4x4_16b_bf16 v[48:51], v[40:41], v[68:69], 0\n"
        "s_mul_i32 s27, s26, 6\n"
        "s_mul_i32 s39, s26, 5\n"
        "s_add_u32 s39, s39, 6\n"
        "v_mfma_f32_4x4x4_16b_bf16 v[44:47], v[40:41], v[66:67], v[44:47]\n"
        "s_cmp_lt_u32 s26, 6\n"
        "s_cselect_b32 s27, s27, s39\n"
        "s_cselect_b32 s56, 12, 10\n"
        "v_mfma_f32_4x4x4_16b_bf16 v[48:51], v[40:41], v[70:71], v[48:51]\n"
        "s_lshl_b32 s42, s27, 5\n"
        "s_mov_b32 s47, s42\n"
        "s_sub_u32 s39, 7, s43\n"
        "s_waitcnt lgkmcnt(3)\n"
        "v_mfma_f32_32x32x16_bf16 v[80:95], v[132:135], v[112:115], v[80:95]\n"
        "ds_read_b64_tr_b16 v[64:65], v22 offset:16384\n"
        "ds_read_b64_tr_b16 v[66:67], v22 offset:17152\n"
        "ds_read_b128 v[140:143], v19 offset:0\n"
        "s_add_u32 m0, s51, 81920\n"
        "s_nop 0\n"
        "buffer_load_dwordx4 v27, s[64:67], s41 offen lds\n"
        "s_add_u32 s41, s41, 196608\n"
        "s_add_u32 s40, s40, 128\n"
        "s_cmp_eq_u32 s43, 2\n"
        "s_cselect_b32 s41, s99, s41\n"
        "s_cselect_b32 s40, s98, s40\n"
        "s_add_u32 m0, s50, 86016\n"
        "s_waitcnt lgkmcnt(3)\n"
        "v_mfma_f32_32x32x16_bf16 v[96:111], v[132:135], v[116:119], v[96:111]\n"
        "ds_read_b64_tr_b16 v[68:69], v22 offset:16448\n"
        "ds_read_b64_tr_b16 v[70:71], v22 offset:17216\n"
        "buffer_load_dwordx4 v24, s[60:63], s40 offen lds\n"
        "buffer_load_dwordx4 v25, s[60:63], s40 offen offset:1024 lds\n"
        "s_mul_i32 s39, s39, 176128\n"
        "s_add_u32 s42, s42, s39\n"
        "s_mov_b32 s68, s10\n"
        "s_and_b32 s69, s11, 0xffff\n"
        "s_mov_b32 s70, 1409024\n"
        "s_mov_b32 s71, 0x20000\n"
        "s_mov_b32 s76, s12\n"
        "s_waitcnt lgkmcnt(3)\n"
        "v_mfma_f32_32x32x16_bf16 v[80:95], v[136:139], v[64:67], v[80:95]\n"
        "ds_read_b64_tr_b16 v[112:113], v23 offset:16384\n"
        "ds_read_b64_tr_b16 v[114:115], v23 offset:17152\n"
        "s_add_u32 m0, s51, 102400\n"
        "s_nop 0\n"
        "buffer_load_dwordx4 v26, s[64:67], s41 offen lds\n"
        "s_add_u32 m0, s51, 110592\n"
        "s_nop 0\n"
        "buffer_load_dwordx4 v27, s[64:67], s41 offen lds\n"
        "s_add_u32 s41, s41, 196608\n"
        "s_add_u32 s40, s40, 128\n"
        "s_cmp_eq_u32 s43, 3\n"
        "s_cselect_b32 s41, s99, s41\n"
        "s_cselect_b32 s40, s98, s40\n"
        "s_waitcnt lgkmcnt(2)\n"
        "v_mfma_f32_32x32x16_bf16 v[96:111], v[136:139], v[68:71], v[96:111]\n"
        "ds_read_b64_tr_b16 v[116:117], v23 offset:16448\n"
        "ds_read_b64_tr_b16 v[118:119], v23 offset:17216\n"
        "s_and_b32 s77, s13, 0xffff\n"
        "s_mov_b32 s78, 1409024\n"
        "s_mov_b32 s79, 0x20000\n"
        "s_cmp_eq_u32 s22, 0\n"
        "s_cselect_b64 s[72:73], s[68:69], s[76:77]\n"
        "s_mov_b64 s[74:75], s[70:71]\n"
        "s_mul_i32 s39, s22, 32\n"
        "s_add_u32 s39, s39, 8192\n"
        "s_waitcnt vmcnt(8) lgkmcnt(0)\n"
        "s_barrier\n"
        "v_mfma_f32_32x32x16_bf16 v[80:95], v[140:143], v[112:115], v[80:95]\n"
        "ds_read_b128 v[144:147], v16 offset:28672\n"
        "ds_read_b64_tr_b16 v[64:65], v20 offset:45056\n"
        "ds_read_b64_tr_b16 v[66:67], v20 offset:45824\n"
        "ds_read_b128 v[148:151], v17 offset:28672\n"
        "s_add_u32 m0, s50, 114688\n"
        "s_nop 0\n"
        "buffer_load_dwordx4 v24, s[60:63], s40 offen lds\n"
        "s_add_u32 s52, s51, s39\n"
        "v_mfma_f32_32x32x16_bf16 v[96:111], v[140:143], v[116:119], v[96:111]\n"
        "ds_read_b64_tr_b16 v[68:69], v20 offset:45120\n"
        "ds_read_b64_tr_b16 v[70:71], v20 offset:45888\n"
        "s_mul_i32 s58, s22, -768\n"
        "s_add_u32 s58, s58, 512\n"
        "s_movk_i32 s57, 2560\n"
        "v_lshlrev_b32 v10, 4, v5\n"
        "v_mad_u32_u24 v32, v6, s57, v10\n"
        "s_waitcnt lgkmcnt(3)\n"
        "v_mfma_f32_32x32x16_bf16 v[80:95], v[144:147], v[64:67], v[80:95]\n"
        "ds_read_b64_tr_b16 v[112:113], v21 offset:45056\n"
        "ds_read_b64_tr_b16 v[114:115], v21 offset:45824\n"
        "ds_read_b128 v[152:155], v18 offset:28672\n"
        "s_waitcnt lgkmcnt(3)\n"
        "v_mfma_f32_32x32x16_bf16 v[96:111], v[144:147], v[68:71], v[96:111]\n"
        "ds_read_b64_tr_b16 v[116:117], v21 offset:45120\n"
        "ds_read_b64_tr_b16 v[118:119], v21 offset:45888\n"
        "v_mfma_f32_4x4x4_16b_bf16 v[44:47], v[40:41], v[64:65], v[44:47]\n"
        "buffer_load_dwordx4 v25, s[60:63], s40 offen offset:1024 lds\n"
        "v_mul_u32_u24 v10, 12, v6\n"
        "v_mfma_f32_4x4x4_16b_bf16 v[48:51], v[40:41], v[68:69], v[48:51]\n"
        "v_sub_u32 v10, v5, v10\n"
        "v_cmp_le_u32 vcc, s56, v10\n"
        "v_mfma_f32_4x4x4_16b_bf16 v[44:47], v[40:41], v[66:67], v[44:47]\n"
        "v_subrev_u32 v10, 32, v32\n"
        "v_cndmask_b32 v32, v32, v10, vcc\n"
        "v_mfma_f32_4x4x4_16b_bf16 v[48:51], v[40:41], v[70:71], v[48:51]\n"
        "v_add_u32 v11, s58, v5\n"
        "v_mul_u32_u24 v12, 0xaaab, v11\n"
        "s_waitcnt lgkmcnt(3)\n"
        "v_mfma_f32_32x32x16_bf16 v[80:95], v[148:151], v[112:115], v[80:95]\n"
        "ds_read_b64_tr_b16 v[64:65], v22 offset:45056\n"
        "ds_read_b64_tr_b16 v[66:67], v22 offset:45824\n"
        "ds_read_b128 v[156:159], v19 offset:28672\n"
        "s_add_u32 m0, s51, 131072\n"
        "s_nop 0\n"
        "buffer_load_dwordx4 v26, s[64:67], s41 offen lds\n"
        "v_lshrrev_b32 v12, 19, v12\n"
        "v_lshlrev_b32 v10, 4, v11\n"
        "s_waitcnt lgkmcnt(3)\n"
        "v_mfma_f32_32x32x16_bf16 v[96:111], v[148:151], v[116:119], v[96:111]\n"
        "ds_read_b64_tr_b16 v[68:69], v22 offset:45120\n"
        "ds_read_b64_tr_b16 v[70:71], v22 offset:45888\n"
        "v_mad_u32_u24 v33, v12, s57, v10\n"
        "v_mul_u32_u24 v10, 12, v12\n"
        "v_sub_u32 v10, v11, v10\n"
        "v_cmp_le_u32 vcc, s56, v10\n"
        "v_subrev_u32 v10, 32, v33\n"
        "s_waitcnt lgkmcnt(3)\n"
        "v_mfma_f32_32x32x16_bf16 v[80:95], v[152:155], v[64:67], v[80:95]\n"
        "ds_read_b64_tr_b16 v[112:113], v23 offset:45056\n"
        "ds_read_b64_tr_b16 v[114:115], v23 offset:45824\n"
        "s_add_u32 m0, s51, 139264\n"
        "s_nop 0\n"
        "buffer_load_dwordx4 v27, s[64:67], s41 offen lds\n"
        "s_add_u32 s41, s41, 196608\n"
        "s_add_u32 s40, s40, 128\n"
        "s_cmp_eq_u32 s43, 4\n"
        "s_cselect_b32 s41, s99, s41\n"
        "s_cselect_b32 s40, s98, s40\n"
        "s_waitcnt lgkmcnt(2)\n"
        "v_mfma_f32_32x32x16_bf16 v[96:111], v[152:155], v[68:71], v[96:111]\n"
        "ds_read_b64_tr_b16 v[116:117], v23 offset:45120\n"
        "ds_read_b64_tr_b16 v[118:119], v23 offset:45888\n"
        "v_cndmask_b32 v33, v33, v10, vcc\n"
        "v_add_u32 v11, 256, v5\n"
        "v_mul_u32_u24 v12, 0xaaab, v11\n"
        "v_lshrrev_b32 v12, 19, v12\n"
        "v_lshlrev_b32 v10, 4, v11\n"
        "v_mad_u32_u24 v34, v12, s57, v10\n"
        "v_mul_u32_u24 v10, 12, v12\n"
        "s_waitcnt vmcnt(8) lgkmcnt(0)\n"
        "s_barrier\n"
        "v_mfma_f32_32x32x16_bf16 v[80:95], v[156:159], v[112:115], v[80:95]\n"
        "v_add_u32 v16, 0xe000, v16\n"
        "v_add_u32 v17, 0xe000, v17\n"
        "v_add_u32 v18, 0xe000, v18\n"
        "v_add_u32 v19, 0xe000, v19\n"
        "v_add_u32 v20, 0xe000, v20\n"
        "v_add_u32 v21, 0xe000, v21\n"
        "v_add_u32 v22, 0xe000, v22\n"
        "v_add_u32 v23, 0xe000, v23\n"
        "ds_read_b128 v[160:163], v16 offset:0\n"
        "ds_read_b64_tr_b16 v[64:65], v20 offset:16384\n"
        "ds_read_b64_tr_b16 v[66:67], v20 offset:17152\n"
        "v_mfma_f32_32x32x16_bf16 v[96:111], v[156:159], v[116:119], v[96:111]\n"
        "ds_read_b64_tr_b16 v[68:69], v20 offset:16448\n"
        "ds_read_b64_tr_b16 v[70:71], v20 offset:17216\n"
        "ds_read_b128 v[164:167], v17 offset:0\n"
        "s_add_u32 m0, s50, 0\n"
        "s_nop 0\n"
        "buffer_load_dwordx4 v24, s[60:63], s40 offen lds\n"
        "v_sub_u32 v10, v11, v10\n"
        "v_cmp_le_u32 vcc, s56, v10\n"
        "v_subrev_u32 v10, 32, v34\n"
        "s_nop 0\n"
        "v_cndmask_b32 v34, v34, v10, vcc\n"
        "s_mul_i32 s39, s22, 96\n"
        "s_add_u32 s39, s39, 57344\n"
        "s_waitcnt lgkmcnt(3)\n"
        "v_mfma_f32_32x32x16_bf16 v[80:95], v[160:163], v[64:67], v[80:95]\n"
        "ds_read_b64_tr_b16 v[112:113], v21 offset:16384\n"
        "ds_read_b64_tr_b16 v[114:115], v21 offset:17152\n"
        "ds_read_b128 v[168:171], v18 offset:0\n"
        "s_waitcnt lgkmcnt(4)\n"
        "v_mfma_f32_32x32x16_bf16 v[96:111], v[160:163], v[68:71], v[96:111]\n"
        "ds_read_b64_tr_b16 v[116:117], v21 offset:16448\n"
        "ds_read_b64_tr_b16 v[118:119], v21 offset:17216\n"
        "v_mfma_f32_4x4x4_16b_bf16 v[44:47], v[40:41], v[64:65], v[44:47]\n"
        "buffer_load_dwordx4 v25, s[60:63], s40 offen offset:1024 lds\n"
        "v_mul_u32_u24 v9, 12320, v7\n"
        "v_mfma_f32_4x4x4_16b_bf16 v[48:51], v[40:41], v[68:69], v[48:51]\n"
        "v_add3_u32 v9, v9, v14, s39\n"
        "s_add_u32 s39, s21, 0\n"
        "v_mfma_f32_4x4x4_16b_bf16 v[44:47], v[40:41], v[66:67], v[44:47]\n"
        "s_and_b32 s39, s39, 3\n"
        "s_mul_i32 s39, s39, 3072\n"
        "v_mfma_f32_4x4x4_16b_bf16 v[48:51], v[40:41], v[70:71], v[48:51]\n"
        "v_add_u32 v36, s39, v9\n"
        "s_add_u32 s39, s21, 1\n"
        "s_waitcnt lgkmcnt(3)\n"
        "v_mfma_f32_32x32x16_bf16 v[80:95], v[164:167], v[112:115], v[80:95]\n"
        "ds_read_b64_tr_b16 v[64:65], v22 offset:16384\n"
        "ds_read_b64_tr_b16 v[66:67], v22 offset:17152\n"
        "ds_read_b128 v[172:175], v19 offset:0\n"
        "s_add_u32 m0, s51, 16384\n"
        "s_nop 0\n"
        "buffer_load_dwordx4 v26, s[64:67], s41 offen lds\n"
        "s_and_b32 s39, s39, 3\n"
        "s_mul_i32 s39, s39, 3072\n"
        "s_waitcnt lgkmcnt(3)\n"
        "v_mfma_f32_32x32x16_bf16 v[96:111], v[164:167], v[116:119], v[96:111]\n"
        "ds_read_b64_tr_b16 v[68:69], v22 offset:16448\n"
        "ds_read_b64_tr_b16 v[70:71], v22 offset:17216\n"
        "v_add_u32 v37, s39, v9\n"
        "s_add_u32 s39, s21, 2\n"
        "s_and_b32 s39, s39, 3\n"
        "s_mul_i32 s39, s39, 3072\n"
        "v_add_u32 v38, s39, v9\n"
        "s_waitcnt lgkmcnt(3)\n"
        "v_mfma_f32_32x32x16_bf16 v[80:95], v[168:171], v[64:67], v[80:95]\n"
        "ds_read_b64_tr_b16 v[112:113], v23 offset:16384\n"
        "ds_read_b64_tr_b16 v[114:115], v23 offset:17152\n"
        "s_add_u32 m0, s51, 24576\n"
        "s_nop 0\n"
        "buffer_load_dwordx4 v27, s[64:67], s41 offen lds\n"
        "s_add_u32 s41, s41, 196608\n"
        "s_add_u32 s40, s40, 128\n"
        "s_cmp_eq_u32 s43, 5\n"
        "s_cselect_b32 s41, s99, s41\n"
        "s_cselect_b32 s40, s98, s40\n"
        "s_waitcnt lgkmcnt(2)\n"
        "v_mfma_f32_32x32x16_bf16 v[96:111], v[168:171], v[68:71], v[96:111]\n"
        "ds_read_b64_tr_b16 v[116:117], v23 offset:16448\n"
        "ds_read_b64_tr_b16 v[118:119], v23 offset:17216\n"
        "s_add_u32 s39, s21, 3\n"
        "s_and_b32 s39, s39, 3\n"
        "s_mul_i32 s39, s39, 3072\n"
        "v_add_u32 v39, s39, v9\n"
        "s_lshl_b32 s29, s25, 2\n"
        "s_add_u32 s29, s29, s21\n"
        "s_waitcnt vmcnt(8) lgkmcnt(0)\n"
        "s_barrier\n"
        "v_mfma_f32_32x32x16_bf16 v[80:95], v[172:175], v[112:115], v[80:95]\n"
        "ds_read_b128 v[176:179], v16 offset:28672\n"
        "ds_read_b64_tr_b16 v[64:65], v20 offset:45056\n"
        "ds_read_b64_tr_b16 v[66:67], v20 offset:45824\n"
        "ds_read_b128 v[180:183], v17 offset:28672\n"
        "s_add_u32 m0, s50, 28672\n"
        "s_nop 0\n"
        "buffer_load_dwordx4 v24, s[60:63], s40 offen lds\n"
        "s_mov_b32 s32, s18\n"
        "s_and_b32 s33, s19, 0xffff\n"
        "v_mfma_f32_32x32x16_bf16 v[96:111], v[172:175], v[116:119], v[96:111]\n"
        "ds_read_b64_tr_b16 v[68:69], v20 offset:45120\n"
        "ds_read_b64_tr_b16 v[70:71], v20 offset:45888\n"
        "s_mov_b32 s34, 14090240\n"
        "s_mov_b32 s35, 0x20000\n"
        "s_lshl_b32 s39, s25, 4\n"
        "s_add_u32 s39, s39, s26\n"
        "s_lshl_b32 s39, s39, 3\n"
        "s_waitcnt lgkmcnt(3)\n"
        "v_mfma_f32_32x32x16_bf16 v[80:95], v[176:179], v[64:67], v[80:95]\n"
        "ds_read_b64_tr_b16 v[112:113], v21 offset:45056\n"
        "ds_read_b64_tr_b16 v[114:115], v21 offset:45824\n"
        "ds_read_b128 v[184:187], v18 offset:28672\n"
        "s_waitcnt lgkmcnt(3)\n"
        "v_mfma_f32_32x32x16_bf16 v[96:111], v[176:179], v[68:71], v[96:111]\n"
        "ds_read_b64_tr_b16 v[116:117], v21 offset:45120\n"
        "ds_read_b64_tr_b16 v[118:119], v21 offset:45888\n"
        "v_mfma_f32_4x4x4_16b_bf16 v[44:47], v[40:41], v[64:65], v[44:47]\n"
        "buffer_load_dwordx4 v25, s[60:63], s40 offen offset:1024 lds\n"
        "s_add_u32 s39, s39, 4096\n"
        "v_mfma_f32_4x4x4_16b_bf16 v[48:51], v[40:41], v[68:69], v[48:51]\n"
        "s_add_u32 s54, s18, s39\n"
        "s_addc_u32 s55, s19, 0\n"
        "s_lshl_b32 s37, s23, 2\n"
        "v_mfma_f32_4x4x4_16b_bf16 v[44:47], v[40:41], v[66:67], v[44:47]\n"
        "s_and_b32 s39, s24, 3\n"
        "s_add_u32 s37, s37, s39\n"
        "v_mfma_f32_4x4x4_16b_bf16 v[48:51], v[40:41], v[70:71], v[48:51]\n"
        "s_lshr_b32 s38, s24, 2\n"
        "s_lshl_b32 s39, s38, 3\n"
        "s_waitcnt lgkmcnt(3)\n"
        "v_mfma_f32_32x32x16_bf16 v[80:95], v[180:183], v[112:115], v[80:95]\n"
        "ds_read_b64_tr_b16 v[64:65], v22 offset:45056\n"
        "ds_read_b64_tr_b16 v[66:67], v22 offset:45824\n"
        "ds_read_b128 v[188:191], v19 offset:28672\n"
        "s_add_u32 m0, s51, 45056\n"
        "s_nop 0\n"
        "buffer_load_dwordx4 v26, s[64:67], s41 offen lds\n"
        "s_lshl_b32 s56, s21, 1\n"
        "s_add_u32 s39, s39, s56\n"
        "s_waitcnt lgkmcnt(3)\n"
        "v_mfma_f32_32x32x16_bf16 v[96:111], v[180:183], v[116:119], v[96:111]\n"
        "ds_read_b64_tr_b16 v[68:69], v22 offset:45120\n"
        "ds_read_b64_tr_b16 v[70:71], v22 offset:45888\n"
        "s_add_u32 s39, s39, s22\n"
        "s_lshl_b32 s39, s39, 5\n"
        "s_add_u32 s39, s39, s37\n"
        "s_lshl_b32 s39, s39, 10\n"
        "s_add_u32 s3, s39, 11993088\n"
        "s_waitcnt lgkmcnt(3)\n"
        "v_mfma_f32_32x32x16_bf16 v[80:95], v[184:187], v[64:67], v[80:95]\n"
        "ds_read_b64_tr_b16 v[112:113], v23 offset:45056\n"
        "ds_read_b64_tr_b16 v[114:115], v23 offset:45824\n"
        "s_add_u32 m0, s51, 53248\n"
        "s_nop 0\n"
        "buffer_load_dwordx4 v27, s[64:67], s41 offen lds\n"
        "s_add_u32 s41, s41, 196608\n"
        "s_add_u32 s40, s40, 128\n"
        "s_cmp_eq_u32 s43, 6\n"
        "s_cselect_b32 s41, s99, s41\n"
        "s_cselect_b32 s40, s98, s40\n"
        "s_waitcnt lgkmcnt(2)\n"
        "v_mfma_f32_32x32x16_bf16 v[96:111], v[184:187], v[68:71], v[96:111]\n"
        "ds_read_b64_tr_b16 v[116:117], v23 offset:45120\n"
        "ds_read_b64_tr_b16 v[118:119], v23 offset:45888\n"
        "s_add_u32 s39, s36, 0\n"
        "s_and_b32 s56, s39, 15\n"
        "s_lshl_b32 s56, s56, 7\n"
        "s_add_u32 s56, s56, s29\n"
        "s_lshl_b32 s57, s56, 10\n"
        "s_add_u32 s57, s57, 65536\n"
        "s_waitcnt vmcnt(8) lgkmcnt(0)\n"
        "s_barrier\n"
        "v_mfma_f32_32x32x16_bf16 v[80:95], v[188:191], v[112:115], v[80:95]\n"
        "v_add_u32 v16, 0xe000, v16\n"
        "v_add_u32 v17, 0xe000, v17\n"
        "v_add_u32 v18, 0xe000, v18\n"
        "v_add_u32 v19, 0xe000, v19\n"
        "v_add_u32 v20, 0xe000, v20\n"
        "v_add_u32 v21, 0xe000, v21\n"
        "v_add_u32 v22, 0xe000, v22\n"
        "v_add_u32 v23, 0xe000, v23\n"
        "ds_read_b128 v[192:195], v16 offset:0\n"
        "ds_read_b64_tr_b16 v[64:65], v20 offset:16384\n"
        "ds_read_b64_tr_b16 v[66:67], v20 offset:17152\n"
        "ds_read_b128 v[196:199], v17 offset:0\n"
        "s_add_u32 m0, s50, 57344\n"
        "s_nop 0\n"
        "buffer_load_dwordx4 v24, s[60:63], s40 offen lds\n"
        "v_mfma_f32_32x32x16_bf16 v[96:111], v[188:191], v[116:119], v[96:111]\n"
        "ds_read_b64_tr_b16 v[68:69], v20 offset:16448\n"
        "ds_read_b64_tr_b16 v[70:71], v20 offset:17216\n"
        "s_lshl_b32 s56, s56, 11\n"
        "s_add_u32 s56, s56, 2162688\n"
        "s_mov_b32 s59, 0x20000\n"
        "s_cmp_gt_u32 s39, 31\n"
        "s_cselect_b32 s58, 1024, 0\n"
        "s_cselect_b32 s59, 512, s59\n"
        "s_add_u32 s56, s56, s58\n"
        "s_cmp_lt_u32 s39, 16\n"
        "s_cselect_b32 s92, s57, s56\n"
        "s_cselect_b32 s59, 0x10000, s59\n"
        "s_cselect_b32 s82, 0x3eb8aa3b, 1.0\n"
        "s_add_u32 s94, s92, s59\n"
        "s_waitcnt lgkmcnt(3)\n"
        "v_mfma_f32_32x32x16_bf16 v[80:95], v[192:195], v[64:67], v[80:95]\n"
        "ds_read_b64_tr_b16 v[112:113], v21 offset:16384\n"
        "ds_read_b64_tr_b16 v[114:115], v21 offset:17152\n"
        "ds_read_b128 v[200:203], v18 offset:0\n"
        "s_waitcnt lgkmcnt(3)\n"
        "v_mfma_f32_32x32x16_bf16 v[96:111], v[192:195], v[68:71], v[96:111]\n"
        "ds_read_b64_tr_b16 v[116:117], v21 offset:16448\n"
        "ds_read_b64_tr_b16 v[118:119], v21 offset:17216\n"
        "v_mfma_f32_4x4x4_16b_bf16 v[44:47], v[40:41], v[64:65], v[44:47]\n"
        "buffer_load_dwordx4 v25, s[60:63], s40 offen offset:1024 lds\n"
        "s_add_u32 s39, s36, 1\n"
        "v_mfma_f32_4x4x4_16b_bf16 v[48:51], v[40:41], v[68:69], v[48:51]\n"
        "s_and_b32 s56, s39, 15\n"
        "s_lshl_b32 s56, s56, 7\n"
        "v_mfma_f32_4x4x4_16b_bf16 v[44:47], v[40:41], v[66:67], v[44:47]\n"
        "s_add_u32 s56, s56, s29\n"
        "s_lshl_b32 s57, s56, 10\n"
        "v_mfma_f32_4x4x4_16b_bf16 v[48:51], v[40:41], v[70:71], v[48:51]\n"
        "s_add_u32 s57, s57, 65536\n"
        "s_lshl_b32 s56, s56, 11\n"
        "s_waitcnt lgkmcnt(3)\n"
        "v_mfma_f32_32x32x16_bf16 v[80:95], v[196:199], v[112:115], v[80:95]\n"
        "ds_read_b64_tr_b16 v[64:65], v22 offset:16384\n"
        "ds_read_b64_tr_b16 v[66:67], v22 offset:17152\n"
        "ds_read_b128 v[204:207], v19 offset:0\n"
        "s_add_u32 m0, s51, 73728\n"
        "s_nop 0\n"
        "buffer_load_dwordx4 v26, s[64:67], s41 offen lds\n"
        "s_add_u32 s56, s56, 2162688\n"
        "s_mov_b32 s59, 0x20000\n"
        "s_cmp_gt_u32 s39, 31\n"
        "s_cselect_b32 s58, 1024, 0\n"
        "s_waitcnt lgkmcnt(3)\n"
        "v_mfma_f32_32x32x16_bf16 v[96:111], v[196:199], v[116:119], v[96:111]\n"
        "ds_read_b64_tr_b16 v[68:69], v22 offset:16448\n"
        "ds_read_b64_tr_b16 v[70:71], v22 offset:17216\n"
        "s_cselect_b32 s59, 512, s59\n"
        "s_add_u32 s56, s56, s58\n"
        "s_cmp_lt_u32 s39, 16\n"
        "s_cselect_b32 s93, s57, s56\n"
        "s_cselect_b32 s59, 0x10000, s59\n"
        "s_cselect_b32 s83, 0x3eb8aa3b, 1.0\n"
        "s_add_u32 s95, s93, s59\n"
        "s_waitcnt lgkmcnt(3)\n"
        "v_mfma_f32_32x32x16_bf16 v[80:95], v[200:203], v[64:67], v[80:95]\n"
        "ds_read_b64_tr_b16 v[112:113], v23 offset:16384\n"
        "ds_read_b64_tr_b16 v[114:115], v23 offset:17152\n"
        "s_add_u32 m0, s51, 81920\n"
        "s_nop 0\n"
        "buffer_load_dwordx4 v27, s[64:67], s41 offen lds\n"
        "s_waitcnt lgkmcnt(2)\n"
        "v_mfma_f32_32x32x16_bf16 v[96:111], v[200:203], v[68:71], v[96:111]\n"
        "ds_read_b64_tr_b16 v[116:117], v23 offset:16448\n"
        "ds_read_b64_tr_b16 v[118:119], v23 offset:17216\n"
        "s_add_u32 s41, s41, 196608\n"
        "s_add_u32 s40, s40, 128\n"
        "s_waitcnt vmcnt(8) lgkmcnt(0)\n"
        "s_barrier\n"
        "v_mfma_f32_32x32x16_bf16 v[80:95], v[204:207], v[112:115], v[80:95]\n"
        "v_add_u32 v16, 0xfffe4000, v16\n"
        "v_add_u32 v17, 0xfffe4000, v17\n"
        "v_add_u32 v18, 0xfffe4000, v18\n"
        "v_add_u32 v19, 0xfffe4000, v19\n"
        "v_add_u32 v20, 0xfffe4000, v20\n"
        "v_add_u32 v21, 0xfffe4000, v21\n"
        "v_add_u32 v22, 0xfffe4000, v22\n"
        "v_add_u32 v23, 0xfffe4000, v23\n"
        "ds_read_b128 v[208:211], v16 offset:0\n"
        "ds_read_b64_tr_b16 v[64:65], v20 offset:16384\n"
        "ds_read_b64_tr_b16 v[66:67], v20 offset:17152\n"
        "v_mfma_f32_32x32x16_bf16 v[96:111], v[204:207], v[116:119], v[96:111]\n"
        "ds_read_b64_tr_b16 v[68:69], v20 offset:16448\n"
        "ds_read_b64_tr_b16 v[70:71], v20 offset:17216\n"
        "ds_read_b128 v[212:215], v17 offset:0\n"
        "s_add_u32 m0, s51, 86016\n"
        "s_nop 0\n"
        "buffer_load_dwordx4 v32, s[68:71], s42 offen lds\n"
        "s_waitcnt lgkmcnt(3)\n"
        "v_mfma_f32_32x32x16_bf16 v[80:95], v[208:211], v[64:67], v[80:95]\n"
        "ds_read_b64_tr_b16 v[112:113], v21 offset:16384\n"
        "ds_read_b64_tr_b16 v[114:115], v21 offset:17152\n"
        "s_waitcnt lgkmcnt(3)\n"
        "v_mfma_f32_32x32x16_bf16 v[96:111], v[208:211], v[68:71], v[96:111]\n"
        "ds_read_b64_tr_b16 v[116:117], v21 offset:16448\n"
        "ds_read_b64_tr_b16 v[118:119], v21 offset:17216\n"
        "v_mfma_f32_4x4x4_16b_bf16 v[44:47], v[40:41], v[64:65], v[44:47]\n"
        "v_mfma_f32_4x4x4_16b_bf16 v[48:51], v[40:41], v[68:69], v[48:51]\n"
        "ds_read_b128 v[216:219], v18 offset:0\n"
        "v_mfma_f32_4x4x4_16b_bf16 v[44:47], v[40:41], v[66:67], v[44:47]\n"
        "s_add_u32 m0, s52, 86016\n"
        "v_mfma_f32_4x4x4_16b_bf16 v[48:51], v[40:41], v[70:71], v[48:51]\n"
        "buffer_load_dwordx4 v33, s[72:75], s42 offen lds\n"
        "s_waitcnt lgkmcnt(3)\n"
        "v_mfma_f32_32x32x16_bf16 v[80:95], v[212:215], v[112:115], v[80:95]\n"
        "ds_read_b64_tr_b16 v[64:65], v22 offset:16384\n"
        "ds_read_b64_tr_b16 v[66:67], v22 offset:17152\n"
        "ds_read_b128 v[220:223], v19 offset:0\n"
        "s_add_u32 m0, s51, 102432\n"
        "s_nop 0\n"
        "buffer_load_dwordx4 v34, s[76:79], s42 offen lds\n"
        "s_waitcnt lgkmcnt(4)\n"
        "v_mfma_f32_32x32x16_bf16 v[96:111], v[212:215], v[116:119], v[96:111]\n"
        "ds_read_b64_tr_b16 v[68:69], v22 offset:16448\n"
        "ds_read_b64_tr_b16 v[70:71], v22 offset:17216\n"
        "s_add_u32 s42, s42, 176128\n"
        "s_cmp_eq_u32 s43, 0\n"
        "s_cselect_b32 s42, s47, s42\n"
        "s_waitcnt lgkmcnt(3)\n"
        "v_mfma_f32_32x32x16_bf16 v[80:95], v[216:219], v[64:67], v[80:95]\n"
        "ds_read_b64_tr_b16 v[112:113], v23 offset:16384\n"
        "ds_read_b64_tr_b16 v[114:115], v23 offset:17152\n"
        "s_waitcnt lgkmcnt(2)\n"
        "v_mfma_f32_32x32x16_bf16 v[96:111], v[216:219], v[68:71], v[96:111]\n"
        "ds_read_b64_tr_b16 v[116:117], v23 offset:16448\n"
        "ds_read_b64_tr_b16 v[118:119], v23 offset:17216\n"
        "s_waitcnt vmcnt(7) lgkmcnt(0)\n"
        "s_barrier\n"
        "v_mfma_f32_32x32x16_bf16 v[80:95], v[220:223], v[112:115], v[80:95]\n"
        "ds_read_b128 v[224:227], v16 offset:28672\n"
        "ds_read_b64_tr_b16 v[64:65], v20 offset:45056\n"
        "ds_read_b64_tr_b16 v[66:67], v20 offset:45824\n"
        "ds_read_b128 v[228:231], v17 offset:28672\n"
        "v_mfma_f32_32x32x16_bf16 v[96:111], v[220:223], v[116:119], v[96:111]\n"
        "ds_read_b64_tr_b16 v[68:69], v20 offset:45120\n"
        "ds_read_b64_tr_b16 v[70:71], v20 offset:45888\n"
        "s_add_u32 m0, s51, 114688\n"
        "s_nop 0\n"
        "buffer_load_dwordx4 v32, s[68:71], s42 offen lds\n"
        "s_waitcnt lgkmcnt(3)\n"
        "v_mfma_f32_32x32x16_bf16 v[80:95], v[224:227], v[64:67], v[80:95]\n"
        "ds_read_b64_tr_b16 v[112:113], v21 offset:45056\n"
        "ds_read_b64_tr_b16 v[114:115], v21 offset:45824\n"
        "s_waitcnt lgkmcnt(2)\n"
        "v_mfma_f32_32x32x16_bf16 v[96:111], v[224:227], v[68:71], v[96:111]\n"
        "ds_read_b64_tr_b16 v[116:117], v21 offset:45120\n"
        "ds_read_b64_tr_b16 v[118:119], v21 offset:45888\n"
        "v_mfma_f32_4x4x4_16b_bf16 v[44:47], v[40:41], v[64:65], v[44:47]\n"
        "v_mfma_f32_4x4x4_16b_bf16 v[48:51], v[40:41], v[68:69], v[48:51]\n"
        "ds_read_b128 v[232:235], v18 offset:28672\n"
        "v_mfma_f32_4x4x4_16b_bf16 v[44:47], v[40:41], v[66:67], v[44:47]\n"
        "s_add_u32 m0, s52, 114688\n"
        "v_mfma_f32_4x4x4_16b_bf16 v[48:51], v[40:41], v[70:71], v[48:51]\n"
        "buffer_load_dwordx4 v33, s[72:75], s42 offen lds\n"
        "s_waitcnt lgkmcnt(3)\n"
        "v_mfma_f32_32x32x16_bf16 v[80:95], v[228:231], v[112:115], v[80:95]\n"
        "ds_read_b64_tr_b16 v[64:65], v22 offset:45056\n"
        "ds_read_b64_tr_b16 v[66:67], v22 offset:45824\n"
        "ds_read_b128 v[236:239], v19 offset:28672\n"
        "s_add_u32 m0, s51, 131104\n"
        "s_nop 0\n"
        "buffer_load_dwordx4 v34, s[76:79], s42 offen lds\n"
        "s_waitcnt lgkmcnt(4)\n"
        "v_mfma_f32_32x32x16_bf16 v[96:111], v[228:231], v[116:119], v[96:111]\n"
        "ds_read_b64_tr_b16 v[68:69], v22 offset:45120\n"
        "ds_read_b64_tr_b16 v[70:71], v22 offset:45888\n"
        "s_add_u32 s42, s42, 176128\n"
        "s_cmp_eq_u32 s43, 1\n"
        "s_cselect_b32 s42, s47, s42\n"
        "s_waitcnt lgkmcnt(3)\n"
        "v_mfma_f32_32x32x16_bf16 v[80:95], v[232:235], v[64:67], v[80:95]\n"
        "ds_read_b64_tr_b16 v[112:113], v23 offset:45056\n"
        "ds_read_b64_tr_b16 v[114:115], v23 offset:45824\n"
        "s_waitcnt lgkmcnt(2)\n"
        "v_mfma_f32_32x32x16_bf16 v[96:111], v[232:235], v[68:71], v[96:111]\n"
        "ds_read_b64_tr_b16 v[116:117], v23 offset:45120\n"
        "ds_read_b64_tr_b16 v[118:119], v23 offset:45888\n"
        "s_waitcnt vmcnt(6) lgkmcnt(0)\n"
        "s_barrier\n"
        "v_mfma_f32_32x32x16_bf16 v[80:95], v[236:239], v[112:115], v[80:95]\n"
        "v_add_u32 v16, 0xe000, v16\n"
        "v_add_u32 v17, 0xe000, v17\n"
        "v_add_u32 v18, 0xe000, v18\n"
        "v_add_u32 v19, 0xe000, v19\n"
        "v_add_u32 v20, 0xe000, v20\n"
        "v_add_u32 v21, 0xe000, v21\n"
        "v_add_u32 v22, 0xe000, v22\n"
        "v_add_u32 v23, 0xe000, v23\n"
        "ds_read_b128 v[240:243], v16 offset:0\n"
        "ds_read_b64_tr_b16 v[64:65], v20 offset:16384\n"
        "ds_read_b64_tr_b16 v[66:67], v20 offset:17152\n"
        "v_mfma_f32_32x32x16_bf16 v[96:111], v[236:239], v[116:119], v[96:111]\n"
        "ds_read_b64_tr_b16 v[68:69], v20 offset:16448\n"
        "ds_read_b64_tr_b16 v[70:71], v20 offset:17216\n"
        "ds_read_b128 v[244:247], v17 offset:0\n"
        "s_add_u32 m0, s51, 0\n"
        "s_nop 0\n"
        "buffer_load_dwordx4 v32, s[68:71], s42 offen lds\n"
        "s_waitcnt lgkmcnt(3)\n"
        "v_mfma_f32_32x32x16_bf16 v[80:95], v[240:243], v[64:67], v[80:95]\n"
        "ds_read_b64_tr_b16 v[112:113], v21 offset:16384\n"
        "ds_read_b64_tr_b16 v[114:115], v21 offset:17152\n"
        "s_waitcnt lgkmcnt(3)\n"
        "v_mfma_f32_32x32x16_bf16 v[96:111], v[240:243], v[68:71], v[96:111]\n"
        "ds_read_b64_tr_b16 v[116:117], v21 offset:16448\n"
        "ds_read_b64_tr_b16 v[118:119], v21 offset:17216\n"
        "v_mfma_f32_4x4x4_16b_bf16 v[44:47], v[40:41], v[64:65], v[44:47]\n"
        "v_mfma_f32_4x4x4_16b_bf16 v[48:51], v[40:41], v[68:69], v[48:51]\n"
        "ds_read_b128 v[248:251], v18 offset:0\n"
        "v_mfma_f32_4x4x4_16b_bf16 v[44:47], v[40:41], v[66:67], v[44:47]\n"
        "s_add_u32 m0, s52, 0\n"
        "v_mfma_f32_4x4x4_16b_bf16 v[48:51], v[40:41], v[70:71], v[48:51]\n"
        "buffer_load_dwordx4 v33, s[72:75], s42 offen lds\n"
        "s_waitcnt lgkmcnt(3)\n"
        "v_mfma_f32_32x32x16_bf16 v[80:95], v[244:247], v[112:115], v[80:95]\n"
        "ds_read_b64_tr_b16 v[64:65], v22 offset:16384\n"
        "ds_read_b64_tr_b16 v[66:67], v22 offset:17152\n"
        "ds_read_b128 v[252:255], v19 offset:0\n"
        "s_add_u32 m0, s51, 16416\n"
        "s_nop 0\n"
        "buffer_load_dwordx4 v34, s[76:79], s42 offen lds\n"
        "s_add_u32 s42, s42, 176128\n"
        "s_cmp_eq_u32 s43, 2\n"
        "s_cselect_b32 s42, s47, s42\n"
        "v_lshlrev_b32 v3, 4, v2\n"
        "s_lshl_b32 s39, s21, 2\n"
        "s_add_u32 s39, s39, 156160\n"
        "v_add_u32 v4, s39, v3\n"
        "v_and_b32 v5, 31, v2\n"
        "s_lshl_b32 s56, s21, 5\n"
        "v_add_u32 v5, s56, v5\n"
        "s_waitcnt lgkmcnt(4)\n"
        "v_mfma_f32_32x32x16_bf16 v[96:111], v[244:247], v[116:119], v[96:111]\n"
        "ds_read_b64_tr_b16 v[68:69], v22 offset:16448\n"
        "ds_read_b64_tr_b16 v[70:71], v22 offset:17216\n"
        "v_lshlrev_b32 v6, 4, v5\n"
        "v_add_u32 v6, 153600, v6\n"
        "v_bfe_u32 v7, v2, 4, 1\n"
        "v_lshrrev_b32 v8, 5, v2\n"
        "v_and_b32 v9, 15, v2\n"
        "v_lshlrev_b32 v9, 4, v9\n"
        "v_lshl_or_b32 v9, v8, 8, v9\n"
        "v_lshl_or_b32 v43, v7, 17, v9\n"
        "s_lshl_b32 s39, s21, 7\n"
        "s_add_u32 s39, s39, 155648\n"
        "v_lshlrev_b32 v42, 4, v8\n"
        "v_add_u32 v42, s39, v42\n"
        "s_waitcnt lgkmcnt(3)\n"
        "v_mfma_f32_32x32x16_bf16 v[80:95], v[248:251], v[64:67], v[80:95]\n"
        "ds_read_b64_tr_b16 v[112:113], v23 offset:16384\n"
        "ds_read_b64_tr_b16 v[114:115], v23 offset:17152\n"
        "ds_write_b32 v4, v44 offset:0\n"
        "ds_write_b32 v4, v48 offset:1024\n"
        "s_waitcnt lgkmcnt(4)\n"
        "v_mfma_f32_32x32x16_bf16 v[96:111], v[248:251], v[68:71], v[96:111]\n"
        "ds_read_b64_tr_b16 v[116:117], v23 offset:16448\n"
        "ds_read_b64_tr_b16 v[118:119], v23 offset:17216\n"
        "v_add_u32 v4, 156160, v3\n"
        "s_waitcnt vmcnt(6) lgkmcnt(0)\n"
        "s_barrier\n"
        "ds_read_b64 v[8:9], v6\n"
        "ds_read_b128 v[12:15], v4 offset:0\n"
        "ds_read_b128 v[52:55], v4 offset:1024\n"
        "v_mfma_f32_32x32x16_bf16 v[80:95], v[252:255], v[112:115], v[80:95]\n"
        "v_mfma_f32_32x32x16_bf16 v[96:111], v[252:255], v[116:119], v[96:111]\n"
        "ds_read_b64_tr_b16 v[64:65], v36 offset:28672\n"
        "ds_read_b64_tr_b16 v[66:67], v36 offset:29440\n"
        "ds_read_b64_tr_b16 v[68:69], v36 offset:28704\n"
        "ds_read_b64_tr_b16 v[70:71], v36 offset:29472\n"
        "ds_read_b64_tr_b16 v[72:73], v36 offset:28736\n"
        "ds_read_b64_tr_b16 v[74:75], v36 offset:29504\n"
        "s_add_u32 m0, s51, 28672\n"
        "s_nop 0\n"
        "buffer_load_dwordx4 v32, s[68:71], s42 offen lds\n"
        "s_add_u32 m0, s52, 28672\n"
        "s_nop 0\n"
        "buffer_load_dwordx4 v33, s[72:75], s42 offen lds\n"
        "s_add_u32 m0, s51, 45088\n"
        "s_nop 0\n"
        "buffer_load_dwordx4 v34, s[76:79], s42 offen lds\n"
        "s_add_u32 s42, s42, 176128\n"
        "s_cmp_eq_u32 s43, 3\n"
        "s_cselect_b32 s42, s47, s42\n"
        "v_mov_b32 v46, 0\n"
        "v_mov_b32 v47, 0\n"
        "v_mov_b32 v50, 0\n"
        "v_mov_b32 v51, 0\n"
        "s_waitcnt lgkmcnt(8)\n"
        "v_cvt_pk_bf16_f32 v44, -v8, -v8\n"
        "v_lshlrev_b32 v7, 16, v44\n"
        "v_sub_f32 v7, -v8, v7\n"
        "v_cvt_pk_bf16_f32 v44, -v8, v7\n"
        "v_mov_b32 v45, v44\n"
        "s_waitcnt lgkmcnt(6)\n"
        "v_add_f32 v52, v52, v53\n"
        "v_add_f32 v54, v54, v55\n"
        "v_add_f32 v52, v52, v54\n"
        "v_cvt_pk_bf16_f32 v48, v52, v52\n"
        "v_lshlrev_b32 v53, 16, v48\n"
        "v_sub_f32 v52, v52, v53\n"
        "v_cvt_pk_bf16_f32 v49, v52, v52\n"
        "s_nop 1\n"
        "v_mfma_f32_32x32x16_bf16 v[16:31], v[44:47], v[48:51], v[96:111]\n"
        "v_add_f32 v12, v12, v13\n"
        "v_add_f32 v14, v14, v15\n"
        "v_add_f32 v12, v12, v14\n"
        "v_cvt_pk_bf16_f32 v48, v12, v12\n"
        "v_lshlrev_b32 v13, 16, v48\n"
        "v_sub_f32 v12, v12, v13\n"
        "v_cvt_pk_bf16_f32 v49, v12, v12\n"
        "s_nop 1\n"
        "v_mfma_f32_32x32x16_bf16 v[0:15], v[44:47], v[48:51], v[80:95]\n"
        "s_waitcnt lgkmcnt(4)\n"
        "v_mfma_f32_32x32x16_bf16 v[80:95], v[64:67], v[128:131], 0\n"
        "ds_read_b64_tr_b16 v[76:77], v37 offset:28672\n"
        "ds_read_b64_tr_b16 v[78:79], v37 offset:29440\n"
        "ds_read_b128 v[48:51], v42 offset:0\n"
        "ds_read_b128 v[52:55], v42 offset:32\n"
        "s_waitcnt lgkmcnt(1)\n"
        "v_mul_f32 v16, v16, v48\n"
        "v_mul_f32 v17, v17, v49\n"
        "v_mul_f32 v18, v18, v50\n"
        "v_mul_f32 v19, v19, v51\n"
        "ds_read_b128 v[48:51], v42 offset:64\n"
        "v_mfma_f32_32x32x16_bf16 v[96:111], v[68:71], v[128:131], 0\n"
        "ds_read_b64_tr_b16 v[56:57], v37 offset:28704\n"
        "ds_read_b64_tr_b16 v[58:59], v37 offset:29472\n"
        "s_waitcnt lgkmcnt(3)\n"
        "v_mul_f32 v20, v20, v52\n"
        "v_mul_f32 v21, v21, v53\n"
        "v_mul_f32 v22, v22, v54\n"
        "v_mul_f32 v23, v23, v55\n"
        "ds_read_b128 v[52:55], v42 offset:96\n"
        "s_waitcnt lgkmcnt(3)\n"
        "v_mul_f32 v24, v24, v48\n"
        "v_mul_f32 v25, v25, v49\n"
        "v_mfma_f32_32x32x16_bf16 v[112:127], v[72:75], v[128:131], 0\n"
        "ds_read_b64_tr_b16 v[60:61], v37 offset:28736\n"
        "ds_read_b64_tr_b16 v[62:63], v37 offset:29504\n"
        "v_mul_f32 v26, v26, v50\n"
        "v_mul_f32 v27, v27, v51\n"
        "s_waitcnt lgkmcnt(2)\n"
        "v_mul_f32 v28, v28, v52\n"
        "v_mul_f32 v29, v29, v53\n"
        "v_mul_f32 v30, v30, v54\n"
        "v_mul_f32 v31, v31, v55\n"
        "v_mfma_f32_32x32x16_bf16 v[80:95], v[76:79], v[132:135], v[80:95]\n"
        "ds_read_b64_tr_b16 v[64:65], v38 offset:28672\n"
        "ds_read_b64_tr_b16 v[66:67], v38 offset:29440\n"
        "v_cvt_pk_bf16_f32 v16, v16, v17\n"
        "v_cvt_pk_bf16_f32 v17, v18, v19\n"
        "v_cvt_pk_bf16_f32 v18, v20, v21\n"
        "v_cvt_pk_bf16_f32 v19, v22, v23\n"
        "v_cvt_pk_bf16_f32 v20, v24, v25\n"
        "v_cvt_pk_bf16_f32 v21, v26, v27\n"
        "v_cvt_pk_bf16_f32 v22, v28, v29\n"
        "v_mfma_f32_32x32x16_bf16 v[96:111], v[56:59], v[132:135], v[96:111]\n"
        "ds_read_b64_tr_b16 v[68:69], v38 offset:28704\n"
        "ds_read_b64_tr_b16 v[70:71], v38 offset:29472\n"
        "v_cvt_pk_bf16_f32 v23, v30, v31\n"
        "buffer_store_dwordx4 v[16:19], v43, s[32:35], s93 offen sc1\n"
        "buffer_store_dwordx4 v[20:23], v43, s[32:35], s95 offen sc1\n"
        "ds_read_b128 v[48:51], v42 offset:0\n"
        "ds_read_b128 v[52:55], v42 offset:32\n"
        "s_waitcnt lgkmcnt(1)\n"
        "v_mul_f32 v0, v0, v48\n"
        "v_mul_f32 v1, v1, v49\n"
        "v_mfma_f32_32x32x16_bf16 v[112:127], v[60:63], v[132:135], v[112:127]\n"
        "ds_read_b64_tr_b16 v[72:73], v38 offset:28736\n"
        "ds_read_b64_tr_b16 v[74:75], v38 offset:29504\n"
        "v_mul_f32 v2, v2, v50\n"
        "v_mul_f32 v3, v3, v51\n"
        "ds_read_b128 v[48:51], v42 offset:64\n"
        "s_waitcnt lgkmcnt(3)\n"
        "v_mul_f32 v4, v4, v52\n"
        "v_mul_f32 v5, v5, v53\n"
        "v_mul_f32 v6, v6, v54\n"
        "v_mfma_f32_32x32x16_bf16 v[80:95], v[64:67], v[136:139], v[80:95]\n"
        "ds_read_b64_tr_b16 v[76:77], v39 offset:28672\n"
        "ds_read_b64_tr_b16 v[78:79], v39 offset:29440\n"
        "v_mul_f32 v7, v7, v55\n"
        "ds_read_b128 v[52:55], v42 offset:96\n"
        "s_waitcnt lgkmcnt(3)\n"
        "v_mul_f32 v8, v8, v48\n"
        "v_mul_f32 v9, v9, v49\n"
        "v_mul_f32 v10, v10, v50\n"
        "v_mul_f32 v11, v11, v51\n"
        "s_waitcnt lgkmcnt(0)\n"
        "v_mul_f32 v12, v12, v52\n"
        "v_mfma_f32_32x32x16_bf16 v[96:111], v[68:71], v[136:139], v[96:111]\n"
        "ds_read_b64_tr_b16 v[56:57], v39 offset:28704\n"
        "ds_read_b64_tr_b16 v[58:59], v39 offset:29472\n"
        "v_mul_f32 v13, v13, v53\n"
        "v_mul_f32 v14, v14, v54\n"
        "v_mul_f32 v15, v15, v55\n"
        "v_cvt_pk_bf16_f32 v0, v0, v1\n"
        "v_cvt_pk_bf16_f32 v1, v2, v3\n"
        "v_cvt_pk_bf16_f32 v2, v4, v5\n"
        "v_cvt_pk_bf16_f32 v3, v6, v7\n"
        "v_mfma_f32_32x32x16_bf16 v[112:127], v[72:75], v[136:139], v[112:127]\n"
        "ds_read_b64_tr_b16 v[60:61], v39 offset:28736\n"
        "ds_read_b64_tr_b16 v[62:63], v39 offset:29504\n"
        "v_cvt_pk_bf16_f32 v4, v8, v9\n"
        "v_cvt_pk_bf16_f32 v5, v10, v11\n"
        "v_cvt_pk_bf16_f32 v6, v12, v13\n"
        "v_cvt_pk_bf16_f32 v7, v14, v15\n"
        "buffer_store_dwordx4 v[0:3], v43, s[32:35], s92 offen sc1\n"
        "buffer_store_dwordx4 v[4:7], v43, s[32:35], s94 offen sc1\n"
        "s_waitcnt lgkmcnt(0)\n"
        "s_branch .LC3_%=\n"
        ".LA2_%=:\n"
        "s_waitcnt vmcnt(3) lgkmcnt(0)\n"
        "s_barrier\n"
        "ds_read_b128 v[128:131], v16 offset:0\n"
        "ds_read_b64_tr_b16 v[64:65], v20 offset:16384\n"
        "ds_read_b64_tr_b16 v[66:67], v20 offset:17152\n"
        "ds_read_b128 v[132:135], v17 offset:0\n"
        "s_add_u32 s30, s30, 0x7f4a7c15\n"
        "s_addc_u32 s31, s31, 0x9e3779b9\n"
        "s_waitcnt lgkmcnt(1)\n"
        "v_mfma_f32_32x32x16_bf16 v[80:95], v[64:67], v[128:131], 0\n"
        "ds_read_b64_tr_b16 v[112:113], v21 offset:16384\n"
        "ds_read_b64_tr_b16 v[114:115], v21 offset:17152\n"
        "ds_read_b128 v[136:139], v18 offset:0\n"
        "v_mfma_f32_4x4x4_16b_bf16 v[44:47], v[40:41], v[64:65], 0\n"
        "s_add_u32 m0, s50, 57344\n"
        "s_nop 0\n"
        "buffer_load_dwordx4 v24, s[60:63], s40 offen lds\n"
        "v_mfma_f32_4x4x4_16b_bf16 v[96:99], v[40:41], v[128:129], 0\n"
        "buffer_load_dwordx4 v25, s[60:63], s40 offen offset:1024 lds\n"
        "s_mul_i32 s27, s26, 6\n"
        "v_mfma_f32_4x4x4_16b_bf16 v[100:103], v[128:129], v[128:129], 0\n"
        "s_mul_i32 s39, s26, 5\n"
        "s_add_u32 s39, s39, 6\n"
        "v_mfma_f32_4x4x4_16b_bf16 v[104:107], v[40:41], v[130:131], 0\n"
        "s_cmp_lt_u32 s26, 6\n"
        "s_cselect_b32 s27, s27, s39\n"
        "v_mfma_f32_4x4x4_16b_bf16 v[108:111], v[130:131], v[130:131], 0\n"
        "s_cselect_b32 s56, 12, 10\n"
        "s_lshl_b32 s42, s27, 5\n"
        "v_mfma_f32_4x4x4_16b_bf16 v[44:47], v[40:41], v[66:67], v[44:47]\n"
        "s_mov_b32 s47, s42\n"
        "s_sub_u32 s39, 7, s43\n"
        "s_waitcnt lgkmcnt(1)\n"
        "v_mfma_f32_32x32x16_bf16 v[80:95], v[112:115], v[132:135], v[80:95]\n"
        "ds_read_b64_tr_b16 v[64:65], v22 offset:16384\n"
        "ds_read_b64_tr_b16 v[66:67], v22 offset:17152\n"
        "ds_read_b128 v[140:143], v19 offset:0\n"
        "v_mfma_f32_4x4x4_16b_bf16 v[96:99], v[40:41], v[132:133], v[96:99]\n"
        "s_add_u32 m0, s51, 73728\n"
        "s_nop 0\n"
        "buffer_load_dwordx4 v26, s[64:67], s41 offen lds\n"
        "s_add_u32 s41, s41, 196608\n"
        "v_mfma_f32_4x4x4_16b_bf16 v[100:103], v[132:133], v[132:133], v[100:103]\n"
        "s_add_u32 s40, s40, 128\n"
        "s_cmp_eq_u32 s43, 2\n"
        "s_cselect_b32 s41, s99, s41\n"
        "v_mfma_f32_4x4x4_16b_bf16 v[104:107], v[40:41], v[134:135], v[104:107]\n"
        "s_cselect_b32 s40, s98, s40\n"
        "s_add_u32 m0, s50, 86016\n"
        "v_mfma_f32_4x4x4_16b_bf16 v[108:111], v[134:135], v[134:135], v[108:111]\n"
        "buffer_load_dwordx4 v24, s[60:63], s40 offen lds\n"
        "s_mul_i32 s39, s39, 176128\n"
        "s_add_u32 s42, s42, s39\n"
        "s_waitcnt lgkmcnt(3)\n"
        "v_mfma_f32_4x4x4_16b_bf16 v[96:99], v[40:41], v[136:137], v[96:99]\n"
        "s_mov_b32 s68, s10\n"
        "s_and_b32 s69, s11, 0xffff\n"
        "s_mov_b32 s70, 1409024\n"
        "v_mfma_f32_4x4x4_16b_bf16 v[100:103], v[136:137], v[136:137], v[100:103]\n"
        "s_mov_b32 s71, 0x20000\n"
        "s_mov_b32 s76, s12\n"
        "s_waitcnt lgkmcnt(1)\n"
        "v_mfma_f32_32x32x16_bf16 v[80:95], v[64:67], v[136:139], v[80:95]\n"
        "ds_read_b64_tr_b16 v[112:113], v23 offset:16384\n"
        "ds_read_b64_tr_b16 v[114:115], v23 offset:17152\n"
        "buffer_load_dwordx4 v25, s[60:63], s40 offen offset:1024 lds\n"
        "v_mfma_f32_4x4x4_16b_bf16 v[104:107], v[40:41], v[138:139], v[104:107]\n"
        "s_add_u32 m0, s51, 102400\n"
        "s_nop 0\n"
        "buffer_load_dwordx4 v26, s[64:67], s41 offen lds\n"
        "s_add_u32 s41, s41, 196608\n"
        "v_mfma_f32_4x4x4_16b_bf16 v[108:111], v[138:139], v[138:139], v[108:111]\n"
        "s_add_u32 s40, s40, 128\n"
        "s_cmp_eq_u32 s43, 3\n"
        "s_waitcnt lgkmcnt(2)\n"
        "v_mfma_f32_4x4x4_16b_bf16 v[96:99], v[40:41], v[140:141], v[96:99]\n"
        "s_cselect_b32 s41, s99, s41\n"
        "s_cselect_b32 s40, s98, s40\n"
        "s_and_b32 s77, s13, 0xffff\n"
        "v_mfma_f32_4x4x4_16b_bf16 v[100:103], v[140:141], v[140:141], v[100:103]\n"
        "s_mov_b32 s78, 1409024\n"
        "s_mov_b32 s79, 0x20000\n"
        "v_mfma_f32_4x4x4_16b_bf16 v[104:107], v[40:41], v[142:143], v[104:107]\n"
        "s_cmp_eq_u32 s22, 0\n"
        "s_cselect_b64 s[72:73], s[68:69], s[76:77]\n"
        "s_mov_b64 s[74:75], s[70:71]\n"
        "v_mfma_f32_4x4x4_16b_bf16 v[108:111], v[142:143], v[142:143], v[108:111]\n"
        "s_mul_i32 s39, s22, 32\n"
        "s_add_u32 s39, s39, 8192\n"
        "s_waitcnt vmcnt(6) lgkmcnt(0)\n"
        "s_barrier\n"
        "v_mfma_f32_32x32x16_bf16 v[80:95], v[112:115], v[140:143], v[80:95]\n"
        "ds_read_b128 v[144:147], v16 offset:28672\n"
        "ds_read_b64_tr_b16 v[64:65], v20 offset:45056\n"
        "ds_read_b64_tr_b16 v[66:67], v20 offset:45824\n"
        "ds_read_b128 v[148:151], v17 offset:28672\n"
        "s_add_u32 m0, s50, 114688\n"
        "s_nop 0\n"
        "buffer_load_dwordx4 v24, s[60:63], s40 offen lds\n"
        "s_add_u32 s52, s51, s39\n"
        "s_mul_i32 s58, s22, -768\n"
        "s_add_u32 s58, s58, 512\n"
        "s_movk_i32 s57, 2560\n"
        "v_lshlrev_b32 v10, 4, v5\n"
        "v_mad_u32_u24 v32, v6, s57, v10\n"
        "s_waitcnt lgkmcnt(1)\n"
        "v_mfma_f32_32x32x16_bf16 v[80:95], v[64:67], v[144:147], v[80:95]\n"
        "ds_read_b64_tr_b16 v[112:113], v21 offset:45056\n"
        "ds_read_b64_tr_b16 v[114:115], v21 offset:45824\n"
        "v_mfma_f32_4x4x4_16b_bf16 v[44:47], v[40:41], v[64:65], v[44:47]\n"
        "ds_read_b128 v[152:155], v18 offset:28672\n"
        "buffer_load_dwordx4 v25, s[60:63], s40 offen offset:1024 lds\n"
        "v_mfma_f32_4x4x4_16b_bf16 v[96:99], v[40:41], v[144:145], v[96:99]\n"
        "v_mul_u32_u24 v10, 12, v6\n"
        "v_mfma_f32_4x4x4_16b_bf16 v[100:103], v[144:145], v[144:145], v[100:103]\n"
        "v_sub_u32 v10, v5, v10\n"
        "v_cmp_le_u32 vcc, s56, v10\n"
        "v_mfma_f32_4x4x4_16b_bf16 v[104:107], v[40:41], v[146:147], v[104:107]\n"
        "v_subrev_u32 v10, 32, v32\n"
        "v_mfma_f32_4x4x4_16b_bf16 v[108:111], v[146:147], v[146:147], v[108:111]\n"
        "v_cndmask_b32 v32, v32, v10, vcc\n"
        "v_add_u32 v11, s58, v5\n"
        "v_mfma_f32_4x4x4_16b_bf16 v[44:47], v[40:41], v[66:67], v[44:47]\n"
        "v_mul_u32_u24 v12, 0xaaab, v11\n"
        "s_waitcnt lgkmcnt(1)\n"
        "v_mfma_f32_32x32x16_bf16 v[80:95], v[112:115], v[148:151], v[80:95]\n"
        "ds_read_b64_tr_b16 v[64:65], v22 offset:45056\n"
        "ds_read_b64_tr_b16 v[66:67], v22 offset:45824\n"
        "ds_read_b128 v[156:159], v19 offset:28672\n"
        "v_mfma_f32_4x4x4_16b_bf16 v[96:99], v[40:41], v[148:149], v[96:99]\n"
        "s_add_u32 m0, s51, 131072\n"
        "s_nop 0\n"
        "buffer_load_dwordx4 v26, s[64:67], s41 offen lds\n"
        "v_mfma_f32_4x4x4_16b_bf16 v[100:103], v[148:149], v[148:149], v[100:103]\n"
        "s_add_u32 s41, s41, 196608\n"
        "s_add_u32 s40, s40, 128\n"
        "s_cmp_eq_u32 s43, 4\n"
        "v_mfma_f32_4x4x4_16b_bf16 v[104:107], v[40:41], v[150:151], v[104:107]\n"
        "s_cselect_b32 s41, s99, s41\n"
        "s_cselect_b32 s40, s98, s40\n"
        "v_mfma_f32_4x4x4_16b_bf16 v[108:111], v[150:151], v[150:151], v[108:111]\n"
        "v_lshrrev_b32 v12, 19, v12\n"
        "v_lshlrev_b32 v10, 4, v11\n"
        "v_mad_u32_u24 v33, v12, s57, v10\n"
        "s_waitcnt lgkmcnt(3)\n"
        "v_mfma_f32_4x4x4_16b_bf16 v[96:99], v[40:41], v[152:153], v[96:99]\n"
        "v_mul_u32_u24 v10, 12, v12\n"
        "v_sub_u32 v10, v11, v10\n"
        "v_mfma_f32_4x4x4_16b_bf16 v[100:103], v[152:153], v[152:153], v[100:103]\n"
        "v_cmp_le_u32 vcc, s56, v10\n"
        "v_subrev_u32 v10, 32, v33\n"
        "s_waitcnt lgkmcnt(1)\n"
        "v_mfma_f32_32x32x16_bf16 v[80:95], v[64:67], v[152:155], v[80:95]\n"
        "ds_read_b64_tr_b16 v[112:113], v23 offset:45056\n"
        "ds_read_b64_tr_b16 v[114:115], v23 offset:45824\n"
        "v_mfma_f32_4x4x4_16b_bf16 v[104:107], v[40:41], v[154:155], v[104:107]\n"
        "v_cndmask_b32 v33, v33, v10, vcc\n"
        "v_mfma_f32_4x4x4_16b_bf16 v[108:111], v[154:155], v[154:155], v[108:111]\n"
        "v_add_u32 v11, 256, v5\n"
        "s_waitcnt lgkmcnt(2)\n"
        "v_mfma_f32_4x4x4_16b_bf16 v[96:99], v[40:41], v[156:157], v[96:99]\n"
        "v_mul_u32_u24 v12, 0xaaab, v11\n"
        "v_lshrrev_b32 v12, 19, v12\n"
        "v_mfma_f32_4x4x4_16b_bf16 v[100:103], v[156:157], v[156:157], v[100:103]\n"
        "v_lshlrev_b32 v10, 4, v11\n"
        "v_mfma_f32_4x4x4_16b_bf16 v[104:107], v[40:41], v[158:159], v[104:107]\n"
        "v_mad_u32_u24 v34, v12, s57, v10\n"
        "v_mfma_f32_4x4x4_16b_bf16 v[108:111], v[158:159], v[158:159], v[108:111]\n"
        "v_mul_u32_u24 v10, 12, v12\n"
        "s_waitcnt vmcnt(6) lgkmcnt(0)\n"
        "s_barrier\n"
        "v_mfma_f32_32x32x16_bf16 v[80:95], v[112:115], v[156:159], v[80:95]\n"
        "v_add_u32 v16, 0xe000, v16\n"
        "v_add_u32 v17, 0xe000, v17\n"
        "v_add_u32 v18, 0xe000, v18\n"
        "v_add_u32 v19, 0xe000, v19\n"
        "v_add_u32 v20, 0xe000, v20\n"
        "v_add_u32 v21, 0xe000, v21\n"
        "v_add_u32 v22, 0xe000, v22\n"
        "v_add_u32 v23, 0xe000, v23\n"
        "ds_read_b128 v[160:163], v16 offset:0\n"
        "ds_read_b64_tr_b16 v[64:65], v20 offset:16384\n"
        "ds_read_b64_tr_b16 v[66:67], v20 offset:17152\n"
        "ds_read_b128 v[164:167], v17 offset:0\n"
        "s_add_u32 m0, s50, 0\n"
        "s_nop 0\n"
        "buffer_load_dwordx4 v24, s[60:63], s40 offen lds\n"
        "v_sub_u32 v10, v11, v10\n"
        "v_cmp_le_u32 vcc, s56, v10\n"
        "v_subrev_u32 v10, 32, v34\n"
        "s_nop 0\n"
        "v_cndmask_b32 v34, v34, v10, vcc\n"
        "s_mul_i32 s39, s22, 96\n"
        "s_add_u32 s39, s39, 57344\n"
        "s_waitcnt lgkmcnt(1)\n"
        "v_mfma_f32_32x32x16_bf16 v[80:95], v[64:67], v[160:163], v[80:95]\n"
        "ds_read_b64_tr_b16 v[112:113], v21 offset:16384\n"
        "ds_read_b64_tr_b16 v[114:115], v21 offset:17152\n"
        "v_mfma_f32_4x4x4_16b_bf16 v[44:47], v[40:41], v[64:65], v[44:47]\n"
        "ds_read_b128 v[168:171], v18 offset:0\n"
        "buffer_load_dwordx4 v25, s[60:63], s40 offen offset:1024 lds\n"
        "v_mfma_f32_4x4x4_16b_bf16 v[96:99], v[40:41], v[160:161], v[96:99]\n"
        "v_mul_u32_u24 v9, 12320, v7\n"
        "v_mfma_f32_4x4x4_16b_bf16 v[100:103], v[160:161], v[160:161], v[100:103]\n"
        "v_add3_u32 v9, v9, v14, s39\n"
        "s_add_u32 s39, s21, 0\n"
        "v_mfma_f32_4x4x4_16b_bf16 v[104:107], v[40:41], v[162:163], v[104:107]\n"
        "s_and_b32 s39, s39, 3\n"
        "v_mfma_f32_4x4x4_16b_bf16 v[108:111], v[162:163], v[162:163], v[108:111]\n"
        "s_mul_i32 s39, s39, 3072\n"
        "v_add_u32 v36, s39, v9\n"
        "v_mfma_f32_4x4x4_16b_bf16 v[44:47], v[40:41], v[66:67], v[44:47]\n"
        "s_add_u32 s39, s21, 1\n"
        "s_waitcnt lgkmcnt(1)\n"
        "v_mfma_f32_32x32x16_bf16 v[80:95], v[112:115], v[164:167], v[80:95]\n"
        "ds_read_b64_tr_b16 v[64:65], v22 offset:16384\n"
        "ds_read_b64_tr_b16 v[66:67], v22 offset:17152\n"
        "ds_read_b128 v[172:175], v19 offset:0\n"
        "v_mfma_f32_4x4x4_16b_bf16 v[96:99], v[40:41], v[164:165], v[96:99]\n"
        "s_add_u32 m0, s51, 16384\n"
        "s_nop 0\n"
        "buffer_load_dwordx4 v26, s[64:67], s41 offen lds\n"
        "v_mfma_f32_4x4x4_16b_bf16 v[100:103], v[164:165], v[164:165], v[100:103]\n"
        "s_add_u32 s41, s41, 196608\n"
        "s_add_u32 s40, s40, 128\n"
        "s_cmp_eq_u32 s43, 5\n"
        "v_mfma_f32_4x4x4_16b_bf16 v[104:107], v[40:41], v[166:167], v[104:107]\n"
        "s_cselect_b32 s41, s99, s41\n"
        "s_cselect_b32 s40, s98, s40\n"
        "v_mfma_f32_4x4x4_16b_bf16 v[108:111], v[166:167], v[166:167], v[108:111]\n"
        "s_and_b32 s39, s39, 3\n"
        "s_mul_i32 s39, s39, 3072\n"
        "v_add_u32 v37, s39, v9\n"
        "s_waitcnt lgkmcnt(3)\n"
        "v_mfma_f32_4x4x4_16b_bf16 v[96:99], v[40:41], v[168:169], v[96:99]\n"
        "s_add_u32 s39, s21, 2\n"
        "s_and_b32 s39, s39, 3\n"
        "v_mfma_f32_4x4x4_16b_bf16 v[100:103], v[168:169], v[168:169], v[100:103]\n"
        "s_mul_i32 s39, s39, 3072\n"
        "v_add_u32 v38, s39, v9\n"
        "s_waitcnt lgkmcnt(1)\n"
        "v_mfma_f32_32x32x16_bf16 v[80:95], v[64:67], v[168:171], v[80:95]\n"
        "ds_read_b64_tr_b16 v[112:113], v23 offset:16384\n"
        "ds_read_b64_tr_b16 v[114:115], v23 offset:17152\n"
        "v_mfma_f32_4x4x4_16b_bf16 v[104:107], v[40:41], v[170:171], v[104:107]\n"
        "s_add_u32 s39, s21, 3\n"
        "v_mfma_f32_4x4x4_16b_bf16 v[108:111], v[170:171], v[170:171], v[108:111]\n"
        "s_and_b32 s39, s39, 3\n"
        "s_waitcnt lgkmcnt(2)\n"
        "v_mfma_f32_4x4x4_16b_bf16 v[96:99], v[40:41], v[172:173], v[96:99]\n"
        "s_mul_i32 s39, s39, 3072\n"
        "v_mfma_f32_4x4x4_16b_bf16 v[100:103], v[172:173], v[172:173], v[100:103]\n"
        "v_add_u32 v39, s39, v9\n"
        "v_mfma_f32_4x4x4_16b_bf16 v[104:107], v[40:41], v[174:175], v[104:107]\n"
        "s_lshl_b32 s29, s25, 2\n"
        "v_mfma_f32_4x4x4_16b_bf16 v[108:111], v[174:175], v[174:175], v[108:111]\n"
        "s_add_u32 s29, s29, s21\n"
        "s_waitcnt vmcnt(6) lgkmcnt(0)\n"
        "s_barrier\n"
        "v_mfma_f32_32x32x16_bf16 v[80:95], v[112:115], v[172:175], v[80:95]\n"
        "ds_read_b128 v[176:179], v16 offset:28672\n"
        "ds_read_b64_tr_b16 v[64:65], v20 offset:45056\n"
        "ds_read_b64_tr_b16 v[66:67], v20 offset:45824\n"
        "ds_read_b128 v[180:183], v17 offset:28672\n"
        "s_add_u32 m0, s50, 28672\n"
        "s_nop 0\n"
        "buffer_load_dwordx4 v24, s[60:63], s40 offen lds\n"
        "s_mov_b32 s32, s18\n"
        "s_and_b32 s33, s19, 0xffff\n"
        "s_mov_b32 s34, 14090240\n"
        "s_mov_b32 s35, 0x20000\n"
        "s_lshl_b32 s39, s25, 4\n"
        "s_add_u32 s39, s39, s26\n"
        "s_lshl_b32 s39, s39, 3\n"
        "s_waitcnt lgkmcnt(1)\n"
        "v_mfma_f32_32x32x16_bf16 v[80:95], v[64:67], v[176:179], v[80:95]\n"
        "ds_read_b64_tr_b16 v[112:113], v21 offset:45056\n"
        "ds_read_b64_tr_b16 v[114:115], v21 offset:45824\n"
        "v_mfma_f32_4x4x4_16b_bf16 v[44:47], v[40:41], v[64:65], v[44:47]\n"
        "ds_read_b128 v[184:187], v18 offset:28672\n"
        "buffer_load_dwordx4 v25, s[60:63], s40 offen offset:1024 lds\n"
        "v_mfma_f32_4x4x4_16b_bf16 v[96:99], v[40:41], v[176:177], v[96:99]\n"
        "s_add_u32 s39, s39, 4096\n"
        "s_add_u32 s54, s18, s39\n"
        "v_mfma_f32_4x4x4_16b_bf16 v[100:103], v[176:177], v[176:177], v[100:103]\n"
        "s_addc_u32 s55, s19, 0\n"
        "v_mfma_f32_4x4x4_16b_bf16 v[104:107], v[40:41], v[178:179], v[104:107]\n"
        "s_lshl_b32 s37, s23, 2\n"
        "s_and_b32 s39, s24, 3\n"
        "v_mfma_f32_4x4x4_16b_bf16 v[108:111], v[178:179], v[178:179], v[108:111]\n"
        "s_add_u32 s37, s37, s39\n"
        "s_lshr_b32 s38, s24, 2\n"
        "v_mfma_f32_4x4x4_16b_bf16 v[44:47], v[40:41], v[66:67], v[44:47]\n"
        "s_lshl_b32 s39, s38, 3\n"
        "s_waitcnt lgkmcnt(1)\n"
        "v_mfma_f32_32x32x16_bf16 v[80:95], v[112:115], v[180:183], v[80:95]\n"
        "ds_read_b64_tr_b16 v[64:65], v22 offset:45056\n"
        "ds_read_b64_tr_b16 v[66:67], v22 offset:45824\n"
        "ds_read_b128 v[188:191], v19 offset:28672\n"
        "v_mfma_f32_4x4x4_16b_bf16 v[96:99], v[40:41], v[180:181], v[96:99]\n"
        "s_add_u32 m0, s51, 45056\n"
        "s_nop 0\n"
        "buffer_load_dwordx4 v26, s[64:67], s41 offen lds\n"
        "v_mfma_f32_4x4x4_16b_bf16 v[100:103], v[180:181], v[180:181], v[100:103]\n"
        "s_add_u32 s41, s41, 196608\n"
        "s_add_u32 s40, s40, 128\n"
        "s_cmp_eq_u32 s43, 6\n"
        "v_mfma_f32_4x4x4_16b_bf16 v[104:107], v[40:41], v[182:183], v[104:107]\n"
        "s_cselect_b32 s41, s99, s41\n"
        "s_cselect_b32 s40, s98, s40\n"
        "v_mfma_f32_4x4x4_16b_bf16 v[108:111], v[182:183], v[182:183], v[108:111]\n"
        "s_lshl_b32 s56, s21, 1\n"
        "s_add_u32 s39, s39, s56\n"
        "s_add_u32 s39, s39, s22\n"
        "s_waitcnt lgkmcnt(3)\n"
        "v_mfma_f32_4x4x4_16b_bf16 v[96:99], v[40:41], v[184:185], v[96:99]\n"
        "s_lshl_b32 s39, s39, 5\n"
        "s_add_u32 s39, s39, s37\n"
        "v_mfma_f32_4x4x4_16b_bf16 v[100:103], v[184:185], v[184:185], v[100:103]\n"
        "s_lshl_b32 s39, s39, 10\n"
        "s_add_u32 s3, s39, 11993088\n"
        "s_waitcnt lgkmcnt(1)\n"
        "v_mfma_f32_32x32x16_bf16 v[80:95], v[64:67], v[184:187], v[80:95]\n"
        "ds_read_b64_tr_b16 v[112:113], v23 offset:45056\n"
        "ds_read_b64_tr_b16 v[114:115], v23 offset:45824\n"
        "v_mfma_f32_4x4x4_16b_bf16 v[104:107], v[40:41], v[186:187], v[104:107]\n"
        "s_add_u32 s39, s36, 0\n"
        "v_mfma_f32_4x4x4_16b_bf16 v[108:111], v[186:187], v[186:187], v[108:111]\n"
        "s_and_b32 s56, s39, 15\n"
        "s_waitcnt lgkmcnt(2)\n"
        "v_mfma_f32_4x4x4_16b_bf16 v[96:99], v[40:41], v[188:189], v[96:99]\n"
        "s_lshl_b32 s56, s56, 7\n"
        "v_mfma_f32_4x4x4_16b_bf16 v[100:103], v[188:189], v[188:189], v[100:103]\n"
        "s_add_u32 s56, s56, s29\n"
        "v_mfma_f32_4x4x4_16b_bf16 v[104:107], v[40:41], v[190:191], v[104:107]\n"
        "s_lshl_b32 s57, s56, 10\n"
        "v_mfma_f32_4x4x4_16b_bf16 v[108:111], v[190:191], v[190:191], v[108:111]\n"
        "s_add_u32 s57, s57, 65536\n"
        "s_waitcnt vmcnt(6) lgkmcnt(0)\n"
        "s_barrier\n"
        "v_mfma_f32_32x32x16_bf16 v[80:95], v[112:115], v[188:191], v[80:95]\n"
        "v_add_u32 v16, 0xe000, v16\n"
        "v_add_u32 v17, 0xe000, v17\n"
        "v_add_u32 v18, 0xe000, v18\n"
        "v_add_u32 v19, 0xe000, v19\n"
        "v_add_u32 v20, 0xe000, v20\n"
        "v_add_u32 v21, 0xe000, v21\n"
        "v_add_u32 v22, 0xe000, v22\n"
        "v_add_u32 v23, 0xe000, v23\n"
        "ds_read_b128 v[192:195], v16 offset:0\n"
        "ds_read_b64_tr_b16 v[64:65], v20 offset:16384\n"
        "ds_read_b64_tr_b16 v[66:67], v20 offset:17152\n"
        "ds_read_b128 v[196:199], v17 offset:0\n"
        "s_add_u32 m0, s50, 57344\n"
        "s_nop 0\n"
        "buffer_load_dwordx4 v24, s[60:63], s40 offen lds\n"
        "s_lshl_b32 s56, s56, 11\n"
        "s_add_u32 s56, s56, 2162688\n"
        "s_mov_b32 s59, 0x20000\n"
        "s_cmp_gt_u32 s39, 31\n"
        "s_cselect_b32 s58, 1024, 0\n"
        "s_cselect_b32 s59, 512, s59\n"
        "s_add_u32 s56, s56, s58\n"
        "s_cmp_lt_u32 s39, 16\n"
        "s_cselect_b32 s92, s57, s56\n"
        "s_cselect_b32 s59, 0x10000, s59\n"
        "s_cselect_b32 s82, 0x3eb8aa3b, 1.0\n"
        "s_add_u32 s94, s92, s59\n"
        "s_waitcnt lgkmcnt(1)\n"
        "v_mfma_f32_32x32x16_bf16 v[80:95], v[64:67], v[192:195], v[80:95]\n"
        "ds_read_b64_tr_b16 v[112:113], v21 offset:16384\n"
        "ds_read_b64_tr_b16 v[114:115], v21 offset:17152\n"
        "v_mfma_f32_4x4x4_16b_bf16 v[44:47], v[40:41], v[64:65], v[44:47]\n"
        "ds_read_b128 v[200:203], v18 offset:0\n"
        "buffer_load_dwordx4 v25, s[60:63], s40 offen offset:1024 lds\n"
        "v_mfma_f32_4x4x4_16b_bf16 v[96:99], v[40:41], v[192:193], v[96:99]\n"
        "s_add_u32 s39, s36, 1\n"
        "v_mfma_f32_4x4x4_16b_bf16 v[100:103], v[192:193], v[192:193], v[100:103]\n"
        "s_and_b32 s56, s39, 15\n"
        "s_lshl_b32 s56, s56, 7\n"
        "v_mfma_f32_4x4x4_16b_bf16 v[104:107], v[40:41], v[194:195], v[104:107]\n"
        "s_add_u32 s56, s56, s29\n"
        "v_mfma_f32_4x4x4_16b_bf16 v[108:111], v[194:195], v[194:195], v[108:111]\n"
        "s_lshl_b32 s57, s56, 10\n"
        "s_add_u32 s57, s57, 65536\n"
        "v_mfma_f32_4x4x4_16b_bf16 v[44:47], v[40:41], v[66:67], v[44:47]\n"
        "s_lshl_b32 s56, s56, 11\n"
        "s_waitcnt lgkmcnt(1)\n"
        "v_mfma_f32_32x32x16_bf16 v[80:95], v[112:115], v[196:199], v[80:95]\n"
        "ds_read_b64_tr_b16 v[64:65], v22 offset:16384\n"
        "ds_read_b64_tr_b16 v[66:67], v22 offset:17152\n"
        "ds_read_b128 v[204:207], v19 offset:0\n"
        "v_mfma_f32_4x4x4_16b_bf16 v[96:99], v[40:41], v[196:197], v[96:99]\n"
        "s_add_u32 m0, s51, 73728\n"
        "s_nop 0\n"
        "buffer_load_dwordx4 v26, s[64:67], s41 offen lds\n"
        "s_add_u32 s41, s41, 196608\n"
        "v_mfma_f32_4x4x4_16b_bf16 v[100:103], v[196:197], v[196:197], v[100:103]\n"
        "s_add_u32 s40, s40, 128\n"
        "s_add_u32 s56, s56, 2162688\n"
        "v_mfma_f32_4x4x4_16b_bf16 v[104:107], v[40:41], v[198:199], v[104:107]\n"
        "s_mov_b32 s59, 0x20000\n"
        "s_cmp_gt_u32 s39, 31\n"
        "s_cselect_b32 s58, 1024, 0\n"
        "v_mfma_f32_4x4x4_16b_bf16 v[108:111], v[198:199], v[198:199], v[108:111]\n"
        "s_cselect_b32 s59, 512, s59\n"
        "s_add_u32 s56, s56, s58\n"
        "s_waitcnt lgkmcnt(3)\n"
        "v_mfma_f32_4x4x4_16b_bf16 v[96:99], v[40:41], v[200:201], v[96:99]\n"
        "s_cmp_lt_u32 s39, 16\n"
        "s_cselect_b32 s93, s57, s56\n"
        "s_cselect_b32 s59, 0x10000, s59\n"
        "v_mfma_f32_4x4x4_16b_bf16 v[100:103], v[200:201], v[200:201], v[100:103]\n"
        "s_cselect_b32 s83, 0x3eb8aa3b, 1.0\n"
        "s_add_u32 s95, s93, s59\n"
        "s_waitcnt lgkmcnt(1)\n"
        "v_mfma_f32_32x32x16_bf16 v[80:95], v[64:67], v[200:203], v[80:95]\n"
        "ds_read_b64_tr_b16 v[112:113], v23 offset:16384\n"
        "ds_read_b64_tr_b16 v[114:115], v23 offset:17152\n"
        "v_mfma_f32_4x4x4_16b_bf16 v[104:107], v[40:41], v[202:203], v[104:107]\n"
        "v_mfma_f32_4x4x4_16b_bf16 v[108:111], v[202:203], v[202:203], v[108:111]\n"
        "s_waitcnt lgkmcnt(2)\n"
        "v_mfma_f32_4x4x4_16b_bf16 v[96:99], v[40:41], v[204:205], v[96:99]\n"
        "v_mfma_f32_4x4x4_16b_bf16 v[100:103], v[204:205], v[204:205], v[100:103]\n"
        "v_mfma_f32_4x4x4_16b_bf16 v[104:107], v[40:41], v[206:207], v[104:107]\n"
        "v_mfma_f32_4x4x4_16b_bf16 v[108:111], v[206:207], v[206:207], v[108:111]\n"
        "s_waitcnt vmcnt(6) lgkmcnt(0)\n"
        "s_barrier\n"
        "v_mfma_f32_32x32x16_bf16 v[80:95], v[112:115], v[204:207], v[80:95]\n"
        "v_add_u32 v16, 0xfffe4000, v16\n"
        "v_add_u32 v17, 0xfffe4000, v17\n"
        "v_add_u32 v18, 0xfffe4000, v18\n"
        "v_add_u32 v19, 0xfffe4000, v19\n"
        "v_add_u32 v20, 0xfffe4000, v20\n"
        "v_add_u32 v21, 0xfffe4000, v21\n"
        "v_add_u32 v22, 0xfffe4000, v22\n"
        "v_add_u32 v23, 0xfffe4000, v23\n"
        "ds_read_b128 v[208:211], v16 offset:0\n"
        "ds_read_b64_tr_b16 v[64:65], v20 offset:16384\n"
        "ds_read_b64_tr_b16 v[66:67], v20 offset:17152\n"
        "ds_read_b128 v[212:215], v17 offset:0\n"
        "s_add_u32 m0, s51, 86016\n"
        "s_nop 0\n"
        "buffer_load_dwordx4 v32, s[68:71], s42 offen lds\n"
        "s_waitcnt lgkmcnt(1)\n"
        "v_mfma_f32_32x32x16_bf16 v[80:95], v[64:67], v[208:211], v[80:95]\n"
        "ds_read_b64_tr_b16 v[112:113], v21 offset:16384\n"
        "ds_read_b64_tr_b16 v[114:115], v21 offset:17152\n"
        "v_mfma_f32_4x4x4_16b_bf16 v[44:47], v[40:41], v[64:65], v[44:47]\n"
        "v_mfma_f32_4x4x4_16b_bf16 v[96:99], v[40:41], v[208:209], v[96:99]\n"
        "ds_read_b128 v[216:219], v18 offset:0\n"
        "v_mfma_f32_4x4x4_16b_bf16 v[100:103], v[208:209], v[208:209], v[100:103]\n"
        "v_mfma_f32_4x4x4_16b_bf16 v[104:107], v[40:41], v[210:211], v[104:107]\n"
        "s_add_u32 m0, s52, 86016\n"
        "v_mfma_f32_4x4x4_16b_bf16 v[108:111], v[210:211], v[210:211], v[108:111]\n"
        "buffer_load_dwordx4 v33, s[72:75], s42 offen lds\n"
        "v_mfma_f32_4x4x4_16b_bf16 v[44:47], v[40:41], v[66:67], v[44:47]\n"
        "s_waitcnt lgkmcnt(1)\n"
        "v_mfma_f32_32x32x16_bf16 v[80:95], v[112:115], v[212:215], v[80:95]\n"
        "ds_read_b64_tr_b16 v[64:65], v22 offset:16384\n"
        "ds_read_b64_tr_b16 v[66:67], v22 offset:17152\n"
        "v_mfma_f32_4x4x4_16b_bf16 v[96:99], v[40:41], v[212:213], v[96:99]\n"
        "ds_read_b128 v[220:223], v19 offset:0\n"
        "v_mfma_f32_4x4x4_16b_bf16 v[100:103], v[212:213], v[212:213], v[100:103]\n"
        "s_add_u32 m0, s51, 102432\n"
        "v_mfma_f32_4x4x4_16b_bf16 v[104:107], v[40:41], v[214:215], v[104:107]\n"
        "buffer_load_dwordx4 v34, s[76:79], s42 offen lds\n"
        "v_mfma_f32_4x4x4_16b_bf16 v[108:111], v[214:215], v[214:215], v[108:111]\n"
        "s_add_u32 s42, s42, 176128\n"
        "s_waitcnt lgkmcnt(3)\n"
        "v_mfma_f32_4x4x4_16b_bf16 v[96:99], v[40:41], v[216:217], v[96:99]\n"
        "s_cmp_eq_u32 s43, 0\n"
        "v_mfma_f32_4x4x4_16b_bf16 v[100:103], v[216:217], v[216:217], v[100:103]\n"
        "s_cselect_b32 s42, s47, s42\n"
        "s_waitcnt lgkmcnt(1)\n"
        "v_mfma_f32_32x32x16_bf16 v[80:95], v[64:67], v[216:219], v[80:95]\n"
        "ds_read_b64_tr_b16 v[112:113], v23 offset:16384\n"
        "ds_read_b64_tr_b16 v[114:115], v23 offset:17152\n"
        "v_mfma_f32_4x4x4_16b_bf16 v[104:107], v[40:41], v[218:219], v[104:107]\n"
        "v_mfma_f32_4x4x4_16b_bf16 v[108:111], v[218:219], v[218:219], v[108:111]\n"
        "s_waitcnt lgkmcnt(2)\n"
        "v_mfma_f32_4x4x4_16b_bf16 v[96:99], v[40:41], v[220:221], v[96:99]\n"
        "v_mfma_f32_4x4x4_16b_bf16 v[100:103], v[220:221], v[220:221], v[100:103]\n"
        "v_mfma_f32_4x4x4_16b_bf16 v[104:107], v[40:41], v[222:223], v[104:107]\n"
        "v_mfma_f32_4x4x4_16b_bf16 v[108:111], v[222:223], v[222:223], v[108:111]\n"
        "s_waitcnt vmcnt(6) lgkmcnt(0)\n"
        "s_barrier\n"
        "v_mfma_f32_32x32x16_bf16 v[80:95], v[112:115], v[220:223], v[80:95]\n"
        "ds_read_b128 v[224:227], v16 offset:28672\n"
        "ds_read_b64_tr_b16 v[64:65], v20 offset:45056\n"
        "ds_read_b64_tr_b16 v[66:67], v20 offset:45824\n"
        "ds_read_b128 v[228:231], v17 offset:28672\n"
        "s_add_u32 m0, s51, 114688\n"
        "s_nop 0\n"
        "buffer_load_dwordx4 v32, s[68:71], s42 offen lds\n"
        "s_waitcnt lgkmcnt(1)\n"
        "v_mfma_f32_32x32x16_bf16 v[80:95], v[64:67], v[224:227], v[80:95]\n"
        "ds_read_b64_tr_b16 v[112:113], v21 offset:45056\n"
        "ds_read_b64_tr_b16 v[114:115], v21 offset:45824\n"
        "v_mfma_f32_4x4x4_16b_bf16 v[44:47], v[40:41], v[64:65], v[44:47]\n"
        "v_mfma_f32_4x4x4_16b_bf16 v[96:99], v[40:41], v[224:225], v[96:99]\n"
        "ds_read_b128 v[232:235], v18 offset:28672\n"
        "v_mfma_f32_4x4x4_16b_bf16 v[100:103], v[224:225], v[224:225], v[100:103]\n"
        "v_mfma_f32_4x4x4_16b_bf16 v[104:107], v[40:41], v[226:227], v[104:107]\n"
        "s_add_u32 m0, s52, 114688\n"
        "v_mfma_f32_4x4x4_16b_bf16 v[108:111], v[226:227], v[226:227], v[108:111]\n"
        "buffer_load_dwordx4 v33, s[72:75], s42 offen lds\n"
        "v_mfma_f32_4x4x4_16b_bf16 v[44:47], v[40:41], v[66:67], v[44:47]\n"
        "s_waitcnt lgkmcnt(1)\n"
        "v_mfma_f32_32x32x16_bf16 v[80:95], v[112:115], v[228:231], v[80:95]\n"
        "ds_read_b64_tr_b16 v[64:65], v22 offset:45056\n"
        "ds_read_b64_tr_b16 v[66:67], v22 offset:45824\n"
        "v_mfma_f32_4x4x4_16b_bf16 v[96:99], v[40:41], v[228:229], v[96:99]\n"
        "ds_read_b128 v[236:239], v19 offset:28672\n"
        "v_mfma_f32_4x4x4_16b_bf16 v[100:103], v[228:229], v[228:229], v[100:103]\n"
        "s_add_u32 m0, s51, 131104\n"
        "v_mfma_f32_4x4x4_16b_bf16 v[104:107], v[40:41], v[230:231], v[104:107]\n"
        "buffer_load_dwordx4 v34, s[76:79], s42 offen lds\n"
        "v_mfma_f32_4x4x4_16b_bf16 v[108:111], v[230:231], v[230:231], v[108:111]\n"
        "s_add_u32 s42, s42, 176128\n"
        "s_waitcnt lgkmcnt(3)\n"
        "v_mfma_f32_4x4x4_16b_bf16 v[96:99], v[40:41], v[232:233], v[96:99]\n"
        "s_cmp_eq_u32 s43, 1\n"
        "v_mfma_f32_4x4x4_16b_bf16 v[100:103], v[232:233], v[232:233], v[100:103]\n"
        "s_cselect_b32 s42, s47, s42\n"
        "s_waitcnt lgkmcnt(1)\n"
        "v_mfma_f32_32x32x16_bf16 v[80:95], v[64:67], v[232:235], v[80:95]\n"
        "ds_read_b64_tr_b16 v[112:113], v23 offset:45056\n"
        "ds_read_b64_tr_b16 v[114:115], v23 offset:45824\n"
        "v_mfma_f32_4x4x4_16b_bf16 v[104:107], v[40:41], v[234:235], v[104:107]\n"
        "v_mfma_f32_4x4x4_16b_bf16 v[108:111], v[234:235], v[234:235], v[108:111]\n"
        "s_waitcnt lgkmcnt(2)\n"
        "v_mfma_f32_4x4x4_16b_bf16 v[96:99], v[40:41], v[236:237], v[96:99]\n"
        "v_mfma_f32_4x4x4_16b_bf16 v[100:103], v[236:237], v[236:237], v[100:103]\n"
        "v_mfma_f32_4x4x4_16b_bf16 v[104:107], v[40:41], v[238:239], v[104:107]\n"
        "v_mfma_f32_4x4x4_16b_bf16 v[108:111], v[238:239], v[238:239], v[108:111]\n"
        "s_waitcnt vmcnt(6) lgkmcnt(0)\n"
        "s_barrier\n"
        "v_mfma_f32_32x32x16_bf16 v[80:95], v[112:115], v[236:239], v[80:95]\n"
        "v_add_u32 v16, 0xe000, v16\n"
        "v_add_u32 v17, 0xe000, v17\n"
        "v_add_u32 v18, 0xe000, v18\n"
        "v_add_u32 v19, 0xe000, v19\n"
        "v_add_u32 v20, 0xe000, v20\n"
        "v_add_u32 v21, 0xe000, v21\n"
        "v_add_u32 v22, 0xe000, v22\n"
        "v_add_u32 v23, 0xe000, v23\n"
        "ds_read_b128 v[240:243], v16 offset:0\n"
        "ds_read_b64_tr_b16 v[64:65], v20 offset:16384\n"
        "ds_read_b64_tr_b16 v[66:67], v20 offset:17152\n"
        "ds_read_b128 v[244:247], v17 offset:0\n"
        "ds_read_b128 v[248:251], v18 offset:0\n"
        "ds_read_b128 v[252:255], v19 offset:0\n"
        "s_add_u32 m0, s51, 0\n"
        "s_nop 0\n"
        "buffer_load_dwordx4 v32, s[68:71], s42 offen lds\n"
        "s_waitcnt lgkmcnt(3)\n"
        "v_mfma_f32_32x32x16_bf16 v[80:95], v[64:67], v[240:243], v[80:95]\n"
        "ds_read_b64_tr_b16 v[112:113], v21 offset:16384\n"
        "ds_read_b64_tr_b16 v[114:115], v21 offset:17152\n"
        "v_mfma_f32_4x4x4_16b_bf16 v[44:47], v[40:41], v[64:65], v[44:47]\n"
        "v_mfma_f32_4x4x4_16b_bf16 v[96:99], v[40:41], v[240:241], v[96:99]\n"
        "v_mfma_f32_4x4x4_16b_bf16 v[100:103], v[240:241], v[240:241], v[100:103]\n"
        "v_mfma_f32_4x4x4_16b_bf16 v[104:107], v[40:41], v[242:243], v[104:107]\n"
        "v_mfma_f32_4x4x4_16b_bf16 v[108:111], v[242:243], v[242:243], v[108:111]\n"
        "s_add_u32 m0, s52, 0\n"
        "s_waitcnt lgkmcnt(4)\n"
        "v_mfma_f32_4x4x4_16b_bf16 v[96:99], v[40:41], v[244:245], v[96:99]\n"
        "v_mfma_f32_4x4x4_16b_bf16 v[100:103], v[244:245], v[244:245], v[100:103]\n"
        "v_mfma_f32_4x4x4_16b_bf16 v[104:107], v[40:41], v[246:247], v[104:107]\n"
        "buffer_load_dwordx4 v33, s[72:75], s42 offen lds\n"
        "v_mfma_f32_4x4x4_16b_bf16 v[108:111], v[246:247], v[246:247], v[108:111]\n"
        "v_mfma_f32_4x4x4_16b_bf16 v[44:47], v[40:41], v[66:67], v[44:47]\n"
        "s_waitcnt lgkmcnt(0)\n"
        "v_mfma_f32_32x32x16_bf16 v[80:95], v[112:115], v[244:247], v[80:95]\n"
        "ds_read_b64_tr_b16 v[64:65], v22 offset:16384\n"
        "ds_read_b64_tr_b16 v[66:67], v22 offset:17152\n"
        "v_mfma_f32_4x4x4_16b_bf16 v[96:99], v[40:41], v[248:249], v[96:99]\n"
        "s_add_u32 m0, s51, 16416\n"
        "s_nop 0\n"
        "buffer_load_dwordx4 v34, s[76:79], s42 offen lds\n"
        "v_mfma_f32_4x4x4_16b_bf16 v[100:103], v[248:249], v[248:249], v[100:103]\n"
        "s_add_u32 s42, s42, 176128\n"
        "s_cmp_eq_u32 s43, 2\n"
        "v_mfma_f32_4x4x4_16b_bf16 v[104:107], v[40:41], v[250:251], v[104:107]\n"
        "s_cselect_b32 s42, s47, s42\n"
        "v_lshlrev_b32 v3, 4, v2\n"
        "v_mfma_f32_4x4x4_16b_bf16 v[108:111], v[250:251], v[250:251], v[108:111]\n"
        "s_lshl_b32 s39, s21, 2\n"
        "s_add_u32 s39, s39, 158208\n"
        "v_mfma_f32_4x4x4_16b_bf16 v[96:99], v[40:41], v[252:253], v[96:99]\n"
        "v_add_u32 v4, s39, v3\n"
        "v_and_b32 v5, 31, v2\n"
        "v_mfma_f32_4x4x4_16b_bf16 v[100:103], v[252:253], v[252:253], v[100:103]\n"
        "s_lshl_b32 s56, s21, 5\n"
        "v_add_u32 v5, s56, v5\n"
        "v_mfma_f32_4x4x4_16b_bf16 v[104:107], v[40:41], v[254:255], v[104:107]\n"
        "v_lshlrev_b32 v6, 4, v5\n"
        "v_add_u32 v6, 153600, v6\n"
        "v_mfma_f32_4x4x4_16b_bf16 v[108:111], v[254:255], v[254:255], v[108:111]\n"
        "v_mov_b32 v43, v3\n"
        "s_waitcnt lgkmcnt(0)\n"
        "v_mfma_f32_32x32x16_bf16 v[80:95], v[64:67], v[248:251], v[80:95]\n"
        "ds_read_b64_tr_b16 v[112:113], v23 offset:16384\n"
        "ds_read_b64_tr_b16 v[114:115], v23 offset:17152\n"
        "ds_write_b32 v4, v44 offset:0\n"
        "v_add_u32 v4, 158208, v3\n"
        "v_add_f32 v11, v96, v104\n"
        "v_add_f32 v7, v100, v108\n"
        "v_add_f32 v8, v101, v109\n"
        "v_add_f32 v9, v102, v110\n"
        "v_add_f32 v10, v103, v111\n"
        "s_mov_b32 s56, 0x22222222\n"
        "s_mov_b32 s57, s56\n"
        "s_lshl_b64 s[58:59], s[56:57], 1\n"
        "v_cndmask_b32 v7, v7, v8, s[56:57]\n"
        "s_lshl_b64 s[56:57], s[56:57], 2\n"
        "v_cndmask_b32 v7, v7, v9, s[58:59]\n"
        "v_cndmask_b32 v7, v7, v10, s[56:57]\n"
        "v_mov_b32 v8, v11\n"
        "v_mov_b32 v9, v7\n"
        "s_nop 0\n"
        "v_permlane32_swap_b32 v11, v8\n"
        "v_permlane32_swap_b32 v7, v9\n"
        "v_add_f32 v11, v11, v8\n"
        "v_add_f32 v7, v7, v9\n"
        "v_mul_f32 v8, 0x3b000000, v11\n"
        "v_mul_f32 v7, 0x3b000000, v7\n"
        "v_fma_f32 v7, -v8, v8, v7\n"
        "v_max_f32 v7, 0, v7\n"
        "v_add_f32 v10, 0x3727c5ac, v7\n"
        "v_rsq_f32 v9, v10\n"
        "v_lshlrev_b32 v7, 2, v5\n"
        "v_add_u32 v7, 155648, v7\n"
        "ds_write_b96 v6, v[8:10]\n"
        "ds_write_b32 v7, v9\n"
        "s_waitcnt lgkmcnt(0)\n"
        "s_nop 4\n"
        "s_cmp_lg_u32 s28, 0\n"
        "s_cbranch_scc1 .LB2_%=\n"
        "s_waitcnt vmcnt(6) lgkmcnt(0)\n"
        "s_barrier\n"
        "ds_read_b64 v[8:9], v6\n"
        "ds_read_b128 v[12:15], v4 offset:0\n"
        "v_mfma_f32_32x32x16_bf16 v[80:95], v[112:115], v[252:255], v[80:95]\n"
        "ds_read_b64_tr_b16 v[64:65], v36 offset:28672\n"
        "ds_read_b64_tr_b16 v[66:67], v36 offset:29440\n"
        "ds_read_b64_tr_b16 v[68:69], v36 offset:28704\n"
        "ds_read_b64_tr_b16 v[70:71], v36 offset:29472\n"
        "ds_read_b64_tr_b16 v[72:73], v36 offset:28736\n"
        "ds_read_b64_tr_b16 v[74:75], v36 offset:29504\n"
        "s_add_u32 m0, s51, 28672\n"
        "s_nop 0\n"
        "buffer_load_dwordx4 v32, s[68:71], s42 offen lds\n"
        "s_add_u32 m0, s52, 28672\n"
        "s_nop 0\n"
        "buffer_load_dwordx4 v33, s[72:75], s42 offen lds\n"
        "s_add_u32 m0, s51, 45088\n"
        "s_nop 0\n"
        "buffer_load_dwordx4 v34, s[76:79], s42 offen lds\n"
        "s_add_u32 s42, s42, 176128\n"
        "s_cmp_eq_u32 s43, 3\n"
        "s_cselect_b32 s42, s47, s42\n"
        "v_mov_b32 v46, 0\n"
        "v_mov_b32 v47, 0\n"
        "v_mov_b32 v50, 0\n"
        "v_mov_b32 v51, 0\n"
        "s_waitcnt lgkmcnt(7)\n"
        "v_cvt_pk_bf16_f32 v44, -v8, -v8\n"
        "v_lshlrev_b32 v7, 16, v44\n"
        "v_sub_f32 v7, -v8, v7\n"
        "v_cvt_pk_bf16_f32 v44, -v8, v7\n"
        "v_mov_b32 v45, v44\n"
        "v_mov_b32 v42, v9\n"
        "s_waitcnt lgkmcnt(6)\n"
        "v_add_f32 v12, v12, v13\n"
        "v_add_f32 v14, v14, v15\n"
        "v_add_f32 v12, v12, v14\n"
        "v_cvt_pk_bf16_f32 v48, v12, v12\n"
        "v_lshlrev_b32 v13, 16, v48\n"
        "v_sub_f32 v12, v12, v13\n"
        "v_cvt_pk_bf16_f32 v49, v12, v12\n"
        "s_nop 1\n"
        "v_mfma_f32_32x32x16_bf16 v[0:15], v[48:51], v[44:47], v[80:95]\n"
        "s_waitcnt lgkmcnt(4)\n"
        "v_mfma_f32_32x32x16_bf16 v[80:95], v[64:67], v[128:131], 0\n"
        "ds_read_b64_tr_b16 v[76:77], v37 offset:28672\n"
        "ds_read_b64_tr_b16 v[78:79], v37 offset:29440\n"
        "v_mul_f32 v48, s82, v42\n"
        "s_nop 6\n"
        "v_mul_f32 v0, v0, v48\n"
        "v_mul_f32 v1, v1, v48\n"
        "s_waitcnt lgkmcnt(4)\n"
        "v_mfma_f32_32x32x16_bf16 v[96:111], v[68:71], v[128:131], 0\n"
        "ds_read_b64_tr_b16 v[56:57], v37 offset:28704\n"
        "ds_read_b64_tr_b16 v[58:59], v37 offset:29472\n"
        "v_mul_f32 v2, v2, v48\n"
        "v_mul_f32 v3, v3, v48\n"
        "v_mul_f32 v4, v4, v48\n"
        "s_waitcnt lgkmcnt(4)\n"
        "v_mfma_f32_32x32x16_bf16 v[112:127], v[72:75], v[128:131], 0\n"
        "ds_read_b64_tr_b16 v[60:61], v37 offset:28736\n"
        "ds_read_b64_tr_b16 v[62:63], v37 offset:29504\n"
        "v_mul_f32 v5, v5, v48\n"
        "v_mul_f32 v6, v6, v48\n"
        "v_mul_f32 v7, v7, v48\n"
        "s_waitcnt lgkmcnt(4)\n"
        "v_mfma_f32_32x32x16_bf16 v[80:95], v[76:79], v[132:135], v[80:95]\n"
        "ds_read_b64_tr_b16 v[64:65], v38 offset:28672\n"
        "ds_read_b64_tr_b16 v[66:67], v38 offset:29440\n"
        "v_mul_f32 v8, v8, v48\n"
        "v_mul_f32 v9, v9, v48\n"
        "v_mul_f32 v10, v10, v48\n"
        "s_waitcnt lgkmcnt(4)\n"
        "v_mfma_f32_32x32x16_bf16 v[96:111], v[56:59], v[132:135], v[96:111]\n"
        "ds_read_b64_tr_b16 v[68:69], v38 offset:28704\n"
        "ds_read_b64_tr_b16 v[70:71], v38 offset:29472\n"
        "v_mul_f32 v11, v11, v48\n"
        "v_mul_f32 v12, v12, v48\n"
        "v_mul_f32 v13, v13, v48\n"
        "s_waitcnt lgkmcnt(4)\n"
        "v_mfma_f32_32x32x16_bf16 v[112:127], v[60:63], v[132:135], v[112:127]\n"
        "ds_read_b64_tr_b16 v[72:73], v38 offset:28736\n"
        "ds_read_b64_tr_b16 v[74:75], v38 offset:29504\n"
        "v_mul_f32 v14, v14, v48\n"
        "v_mul_f32 v15, v15, v48\n"
        "v_cvt_pk_bf16_f32 v0, v0, v1\n"
        "s_waitcnt lgkmcnt(4)\n"
        "v_mfma_f32_32x32x16_bf16 v[80:95], v[64:67], v[136:139], v[80:95]\n"
        "ds_read_b64_tr_b16 v[76:77], v39 offset:28672\n"
        "ds_read_b64_tr_b16 v[78:79], v39 offset:29440\n"
        "v_cvt_pk_bf16_f32 v1, v2, v3\n"
        "v_cvt_pk_bf16_f32 v2, v4, v5\n"
        "v_cvt_pk_bf16_f32 v3, v6, v7\n"
        "s_waitcnt lgkmcnt(4)\n"
        "v_mfma_f32_32x32x16_bf16 v[96:111], v[68:71], v[136:139], v[96:111]\n"
        "ds_read_b64_tr_b16 v[56:57], v39 offset:28704\n"
        "ds_read_b64_tr_b16 v[58:59], v39 offset:29472\n"
        "v_cvt_pk_bf16_f32 v4, v8, v9\n"
        "v_cvt_pk_bf16_f32 v5, v10, v11\n"
        "v_cvt_pk_bf16_f32 v6, v12, v13\n"
        "s_waitcnt lgkmcnt(4)\n"
        "v_mfma_f32_32x32x16_bf16 v[112:127], v[72:75], v[136:139], v[112:127]\n"
        "ds_read_b64_tr_b16 v[60:61], v39 offset:28736\n"
        "ds_read_b64_tr_b16 v[62:63], v39 offset:29504\n"
        "v_cvt_pk_bf16_f32 v7, v14, v15\n"
        "buffer_store_dwordx4 v[0:3], v43, s[32:35], s92 offen sc1\n"
        "buffer_store_dwordx4 v[4:7], v43, s[32:35], s94 offen sc1\n"
        "s_waitcnt lgkmcnt(0)\n"
        "s_branch .LC3_%=\n"
        ".LB2_%=:\n"
        "s_waitcnt vmcnt(6) lgkmcnt(0)\n"
        "s_barrier\n"
        "ds_read_b64 v[8:9], v6\n"
        "ds_read_b128 v[12:15], v4 offset:0\n"
        "v_mfma_f32_32x32x16_bf16 v[80:95], v[112:115], v[252:255], v[80:95]\n"
        "ds_read_b64_tr_b16 v[64:65], v36 offset:28672\n"
        "ds_read_b64_tr_b16 v[66:67], v36 offset:29440\n"
        "ds_read_b64_tr_b16 v[68:69], v36 offset:28704\n"
        "ds_read_b64_tr_b16 v[70:71], v36 offset:29472\n"
        "s_add_u32 m0, s51, 28672\n"
        "s_nop 0\n"
        "buffer_load_dwordx4 v32, s[68:71], s42 offen lds\n"
        "s_add_u32 m0, s52, 28672\n"
        "s_nop 0\n"
        "buffer_load_dwordx4 v33, s[72:75], s42 offen lds\n"
        "s_add_u32 m0, s51, 45088\n"
        "s_nop 0\n"
        "buffer_load_dwordx4 v34, s[76:79], s42 offen lds\n"
        "s_add_u32 s42, s42, 176128\n"
        "s_cmp_eq_u32 s43, 3\n"
        "s_cselect_b32 s42, s47, s42\n"
        "v_mov_b32 v46, 0\n"
        "v_mov_b32 v47, 0\n"
        "v_mov_b32 v50, 0\n"
        "v_mov_b32 v51, 0\n"
        "s_waitcnt lgkmcnt(5)\n"
        "v_cvt_pk_bf16_f32 v44, -v8, -v8\n"
        "v_lshlrev_b32 v7, 16, v44\n"
        "v_sub_f32 v7, -v8, v7\n"
        "v_cvt_pk_bf16_f32 v44, -v8, v7\n"
        "v_mov_b32 v45, v44\n"
        "v_mov_b32 v42, v9\n"
        "s_waitcnt lgkmcnt(4)\n"
        "v_add_f32 v12, v12, v13\n"
        "v_add_f32 v14, v14, v15\n"
        "v_add_f32 v12, v12, v14\n"
        "v_cvt_pk_bf16_f32 v48, v12, v12\n"
        "v_lshlrev_b32 v13, 16, v48\n"
        "v_sub_f32 v12, v12, v13\n"
        "v_cvt_pk_bf16_f32 v49, v12, v12\n"
        "s_nop 1\n"
        "v_mfma_f32_32x32x16_bf16 v[0:15], v[48:51], v[44:47], v[80:95]\n"
        "s_waitcnt lgkmcnt(2)\n"
        "v_mfma_f32_32x32x16_bf16 v[80:95], v[64:67], v[128:131], 0\n"
        "ds_read_b64_tr_b16 v[76:77], v37 offset:28672\n"
        "ds_read_b64_tr_b16 v[78:79], v37 offset:29440\n"
        "v_mul_f32 v48, s82, v42\n"
        "s_nop 6\n"
        "v_mul_f32 v0, v0, v48\n"
        "v_mul_f32 v1, v1, v48\n"
        "v_mul_f32 v2, v2, v48\n"
        "v_mul_f32 v3, v3, v48\n"
        "s_waitcnt lgkmcnt(2)\n"
        "v_mfma_f32_32x32x16_bf16 v[96:111], v[68:71], v[128:131], 0\n"
        "ds_read_b64_tr_b16 v[56:57], v37 offset:28704\n"
        "ds_read_b64_tr_b16 v[58:59], v37 offset:29472\n"
        "v_mul_f32 v4, v4, v48\n"
        "v_mul_f32 v5, v5, v48\n"
        "v_mul_f32 v6, v6, v48\n"
        "v_mul_f32 v7, v7, v48\n"
        "s_waitcnt lgkmcnt(2)\n"
        "v_mfma_f32_32x32x16_bf16 v[80:95], v[76:79], v[132:135], v[80:95]\n"
        "ds_read_b64_tr_b16 v[64:65], v38 offset:28672\n"
        "ds_read_b64_tr_b16 v[66:67], v38 offset:29440\n"
        "v_mul_f32 v8, v8, v48\n"
        "v_mul_f32 v9, v9, v48\n"
        "v_mul_f32 v10, v10, v48\n"
        "v_mul_f32 v11, v11, v48\n"
        "v_mul_f32 v12, v12, v48\n"
        "s_waitcnt lgkmcnt(2)\n"
        "v_mfma_f32_32x32x16_bf16 v[96:111], v[56:59], v[132:135], v[96:111]\n"
        "ds_read_b64_tr_b16 v[68:69], v38 offset:28704\n"
        "ds_read_b64_tr_b16 v[70:71], v38 offset:29472\n"
        "v_mul_f32 v13, v13, v48\n"
        "v_mul_f32 v14, v14, v48\n"
        "v_mul_f32 v15, v15, v48\n"
        "v_cvt_pk_bf16_f32 v0, v0, v1\n"
        "s_waitcnt lgkmcnt(2)\n"
        "v_mfma_f32_32x32x16_bf16 v[80:95], v[64:67], v[136:139], v[80:95]\n"
        "ds_read_b64_tr_b16 v[76:77], v39 offset:28672\n"
        "ds_read_b64_tr_b16 v[78:79], v39 offset:29440\n"
        "v_cvt_pk_bf16_f32 v1, v2, v3\n"
        "v_cvt_pk_bf16_f32 v2, v4, v5\n"
        "v_cvt_pk_bf16_f32 v3, v6, v7\n"
        "v_cvt_pk_bf16_f32 v4, v8, v9\n"
        "v_cvt_pk_bf16_f32 v5, v10, v11\n"
        "s_waitcnt lgkmcnt(2)\n"
        "v_mfma_f32_32x32x16_bf16 v[96:111], v[68:71], v[136:139], v[96:111]\n"
        "ds_read_b64_tr_b16 v[56:57], v39 offset:28704\n"
        "ds_read_b64_tr_b16 v[58:59], v39 offset:29472\n"
        "v_cvt_pk_bf16_f32 v6, v12, v13\n"
        "v_cvt_pk_bf16_f32 v7, v14, v15\n"
        "buffer_store_dwordx4 v[0:3], v43, s[32:35], s92 offen sc1\n"
        "buffer_store_dwordx4 v[4:7], v43, s[32:35], s94 offen sc1\n"
        "s_waitcnt lgkmcnt(0)\n"
        "s_branch .LC2_%=\n"
        ".LA3_%=:\n"
        "s_waitcnt vmcnt(3) lgkmcnt(0)\n"
        "s_barrier\n"
        "ds_read_b128 v[128:131], v16 offset:0\n"
        "ds_read_b64_tr_b16 v[64:65], v20 offset:16384\n"
        "ds_read_b64_tr_b16 v[66:67], v20 offset:17152\n"
        "ds_read_b128 v[132:135], v17 offset:0\n"
        "s_add_u32 s30, s30, 0x7f4a7c15\n"
        "s_addc_u32 s31, s31, 0x9e3779b9\n"
        "s_waitcnt lgkmcnt(1)\n"
        "v_mfma_f32_32x32x16_bf16 v[80:95], v[128:131], v[64:67], 0\n"
        "ds_read_b64_tr_b16 v[112:113], v21 offset:16384\n"
        "ds_read_b64_tr_b16 v[114:115], v21 offset:17152\n"
        "ds_read_b128 v[136:139], v18 offset:0\n"
        "v_mfma_f32_4x4x4_16b_bf16 v[44:47], v[40:41], v[64:65], 0\n"
        "s_add_u32 m0, s50, 57344\n"
        "s_nop 0\n"
        "buffer_load_dwordx4 v24, s[60:63], s40 offen lds\n"
        "v_mfma_f32_4x4x4_16b_bf16 v[96:99], v[40:41], v[128:129], 0\n"
        "buffer_load_dwordx4 v25, s[60:63], s40 offen offset:1024 lds\n"
        "s_mul_i32 s27, s26, 6\n"
        "v_mfma_f32_4x4x4_16b_bf16 v[100:103], v[128:129], v[128:129], 0\n"
        "s_mul_i32 s39, s26, 5\n"
        "s_add_u32 s39, s39, 6\n"
        "v_mfma_f32_4x4x4_16b_bf16 v[104:107], v[40:41], v[130:131], 0\n"
        "s_cmp_lt_u32 s26, 6\n"
        "s_cselect_b32 s27, s27, s39\n"
        "v_mfma_f32_4x4x4_16b_bf16 v[108:111], v[130:131], v[130:131], 0\n"
        "s_cselect_b32 s56, 12, 10\n"
        "s_lshl_b32 s42, s27, 5\n"
        "v_mfma_f32_4x4x4_16b_bf16 v[44:47], v[40:41], v[66:67], v[44:47]\n"
        "s_mov_b32 s47, s42\n"
        "s_sub_u32 s39, 7, s43\n"
        "s_waitcnt lgkmcnt(1)\n"
        "v_mfma_f32_32x32x16_bf16 v[80:95], v[132:135], v[112:115], v[80:95]\n"
        "ds_read_b64_tr_b16 v[64:65], v22 offset:16384\n"
        "ds_read_b64_tr_b16 v[66:67], v22 offset:17152\n"
        "ds_read_b128 v[140:143], v19 offset:0\n"
        "v_mfma_f32_4x4x4_16b_bf16 v[96:99], v[40:41], v[132:133], v[96:99]\n"
        "s_add_u32 m0, s51, 73728\n"
        "s_nop 0\n"
        "buffer_load_dwordx4 v26, s[64:67], s41 offen lds\n"
        "s_add_u32 s41, s41, 196608\n"
        "v_mfma_f32_4x4x4_16b_bf16 v[100:103], v[132:133], v[132:133], v[100:103]\n"
        "s_add_u32 s40, s40, 128\n"
        "s_cmp_eq_u32 s43, 2\n"
        "s_cselect_b32 s41, s99, s41\n"
        "v_mfma_f32_4x4x4_16b_bf16 v[104:107], v[40:41], v[134:135], v[104:107]\n"
        "s_cselect_b32 s40, s98, s40\n"
        "s_add_u32 m0, s50, 86016\n"
        "v_mfma_f32_4x4x4_16b_bf16 v[108:111], v[134:135], v[134:135], v[108:111]\n"
        "buffer_load_dwordx4 v24, s[60:63], s40 offen lds\n"
        "s_mul_i32 s39, s39, 176128\n"
        "s_add_u32 s42, s42, s39\n"
        "s_waitcnt lgkmcnt(3)\n"
        "v_mfma_f32_4x4x4_16b_bf16 v[96:99], v[40:41], v[136:137], v[96:99]\n"
        "s_mov_b32 s68, s10\n"
        "s_and_b32 s69, s11, 0xffff\n"
        "s_mov_b32 s70, 1409024\n"
        "v_mfma_f32_4x4x4_16b_bf16 v[100:103], v[136:137], v[136:137], v[100:103]\n"
        "s_mov_b32 s71, 0x20000\n"
        "s_mov_b32 s76, s12\n"
        "s_waitcnt lgkmcnt(1)\n"
        "v_mfma_f32_32x32x16_bf16 v[80:95], v[136:139], v[64:67], v[80:95]\n"
        "ds_read_b64_tr_b16 v[112:113], v23 offset:16384\n"
        "ds_read_b64_tr_b16 v[114:115], v23 offset:17152\n"
        "buffer_load_dwordx4 v25, s[60:63], s40 offen offset:1024 lds\n"
        "v_mfma_f32_4x4x4_16b_bf16 v[104:107], v[40:41], v[138:139], v[104:107]\n"
        "s_add_u32 m0, s51, 102400\n"
        "s_nop 0\n"
        "buffer_load_dwordx4 v26, s[64:67], s41 offen lds\n"
        "s_add_u32 s41, s41, 196608\n"
        "v_mfma_f32_4x4x4_16b_bf16 v[108:111], v[138:139], v[138:139], v[108:111]\n"
        "s_add_u32 s40, s40, 128\n"
        "s_cmp_eq_u32 s43, 3\n"
        "s_waitcnt lgkmcnt(2)\n"
        "v_mfma_f32_4x4x4_16b_bf16 v[96:99], v[40:41], v[140:141], v[96:99]\n"
        "s_cselect_b32 s41, s99, s41\n"
        "s_cselect_b32 s40, s98, s40\n"
        "s_and_b32 s77, s13, 0xffff\n"
        "v_mfma_f32_4x4x4_16b_bf16 v[100:103], v[140:141], v[140:141], v[100:103]\n"
        "s_mov_b32 s78, 1409024\n"
        "s_mov_b32 s79, 0x20000\n"
        "v_mfma_f32_4x4x4_16b_bf16 v[104:107], v[40:41], v[142:143], v[104:107]\n"
        "s_cmp_eq_u32 s22, 0\n"
        "s_cselect_b64 s[72:73], s[68:69], s[76:77]\n"
        "s_mov_b64 s[74:75], s[70:71]\n"
        "v_mfma_f32_4x4x4_16b_bf16 v[108:111], v[142:143], v[142:143], v[108:111]\n"
        "s_mul_i32 s39, s22, 32\n"
        "s_add_u32 s39, s39, 8192\n"
        "s_waitcnt vmcnt(6) lgkmcnt(0)\n"
        "s_barrier\n"
        "v_mfma_f32_32x32x16_bf16 v[80:95], v[140:143], v[112:115], v[80:95]\n"
        "ds_read_b128 v[144:147], v16 offset:28672\n"
        "ds_read_b64_tr_b16 v[64:65], v20 offset:45056\n"
        "ds_read_b64_tr_b16 v[66:67], v20 offset:45824\n"
        "ds_read_b128 v[148:151], v17 offset:28672\n"
        "s_add_u32 m0, s50, 114688\n"
        "s_nop 0\n"
        "buffer_load_dwordx4 v24, s[60:63], s40 offen lds\n"
        "s_add_u32 s52, s51, s39\n"
        "s_mul_i32 s58, s22, -768\n"
        "s_add_u32 s58, s58, 512\n"
        "s_movk_i32 s57, 2560\n"
        "v_lshlrev_b32 v10, 4, v5\n"
        "v_mad_u32_u24 v32, v6, s57, v10\n"
        "s_waitcnt lgkmcnt(1)\n"
        "v_mfma_f32_32x32x16_bf16 v[80:95], v[144:147], v[64:67], v[80:95]\n"
        "ds_read_b64_tr_b16 v[112:113], v21 offset:45056\n"
        "ds_read_b64_tr_b16 v[114:115], v21 offset:45824\n"
        "v_mfma_f32_4x4x4_16b_bf16 v[44:47], v[40:41], v[64:65], v[44:47]\n"
        "ds_read_b128 v[152:155], v18 offset:28672\n"
        "buffer_load_dwordx4 v25, s[60:63], s40 offen offset:1024 lds\n"
        "v_mfma_f32_4x4x4_16b_bf16 v[96:99], v[40:41], v[144:145], v[96:99]\n"
        "v_mul_u32_u24 v10, 12, v6\n"
        "v_mfma_f32_4x4x4_16b_bf16 v[100:103], v[144:145], v[144:145], v[100:103]\n"
        "v_sub_u32 v10, v5, v10\n"
        "v_cmp_le_u32 vcc, s56, v10\n"
        "v_mfma_f32_4x4x4_16b_bf16 v[104:107], v[40:41], v[146:147], v[104:107]\n"
        "v_subrev_u32 v10, 32, v32\n"
        "v_mfma_f32_4x4x4_16b_bf16 v[108:111], v[146:147], v[146:147], v[108:111]\n"
        "v_cndmask_b32 v32, v32, v10, vcc\n"
        "v_add_u32 v11, s58, v5\n"
        "v_mfma_f32_4x4x4_16b_bf16 v[44:47], v[40:41], v[66:67], v[44:47]\n"
        "v_mul_u32_u24 v12, 0xaaab, v11\n"
        "s_waitcnt lgkmcnt(1)\n"
        "v_mfma_f32_32x32x16_bf16 v[80:95], v[148:151], v[112:115], v[80:95]\n"
        "ds_read_b64_tr_b16 v[64:65], v22 offset:45056\n"
        "ds_read_b64_tr_b16 v[66:67], v22 offset:45824\n"
        "ds_read_b128 v[156:159], v19 offset:28672\n"
        "v_mfma_f32_4x4x4_16b_bf16 v[96:99], v[40:41], v[148:149], v[96:99]\n"
        "s_add_u32 m0, s51, 131072\n"
        "s_nop 0\n"
        "buffer_load_dwordx4 v26, s[64:67], s41 offen lds\n"
        "v_mfma_f32_4x4x4_16b_bf16 v[100:103], v[148:149], v[148:149], v[100:103]\n"
        "s_add_u32 s41, s41, 196608\n"
        "s_add_u32 s40, s40, 128\n"
        "s_cmp_eq_u32 s43, 4\n"
        "v_mfma_f32_4x4x4_16b_bf16 v[104:107], v[40:41], v[150:151], v[104:107]\n"
        "s_cselect_b32 s41, s99, s41\n"
        "s_cselect_b32 s40, s98, s40\n"
        "v_mfma_f32_4x4x4_16b_bf16 v[108:111], v[150:151], v[150:151], v[108:111]\n"
        "v_lshrrev_b32 v12, 19, v12\n"
        "v_lshlrev_b32 v10, 4, v11\n"
        "v_mad_u32_u24 v33, v12, s57, v10\n"
        "s_waitcnt lgkmcnt(3)\n"
        "v_mfma_f32_4x4x4_16b_bf16 v[96:99], v[40:41], v[152:153], v[96:99]\n"
        "v_mul_u32_u24 v10, 12, v12\n"
        "v_sub_u32 v10, v11, v10\n"
        "v_mfma_f32_4x4x4_16b_bf16 v[100:103], v[152:153], v[152:153], v[100:103]\n"
        "v_cmp_le_u32 vcc, s56, v10\n"
        "v_subrev_u32 v10, 32, v33\n"
        "s_waitcnt lgkmcnt(1)\n"
        "v_mfma_f32_32x32x16_bf16 v[80:95], v[152:155], v[64:67], v[80:95]\n"
        "ds_read_b64_tr_b16 v[112:113], v23 offset:45056\n"
        "ds_read_b64_tr_b16 v[114:115], v23 offset:45824\n"
        "v_mfma_f32_4x4x4_16b_bf16 v[104:107], v[40:41], v[154:155], v[104:107]\n"
        "v_cndmask_b32 v33, v33, v10, vcc\n"
        "v_mfma_f32_4x4x4_16b_bf16 v[108:111], v[154:155], v[154:155], v[108:111]\n"
        "v_add_u32 v11, 256, v5\n"
        "s_waitcnt lgkmcnt(2)\n"
        "v_mfma_f32_4x4x4_16b_bf16 v[96:99], v[40:41], v[156:157], v[96:99]\n"
        "v_mul_u32_u24 v12, 0xaaab, v11\n"
        "v_lshrrev_b32 v12, 19, v12\n"
        "v_mfma_f32_4x4x4_16b_bf16 v[100:103], v[156:157], v[156:157], v[100:103]\n"
        "v_lshlrev_b32 v10, 4, v11\n"
        "v_mfma_f32_4x4x4_16b_bf16 v[104:107], v[40:41], v[158:159], v[104:107]\n"
        "v_mad_u32_u24 v34, v12, s57, v10\n"
        "v_mfma_f32_4x4x4_16b_bf16 v[108:111], v[158:159], v[158:159], v[108:111]\n"
        "v_mul_u32_u24 v10, 12, v12\n"
        "s_waitcnt vmcnt(6) lgkmcnt(0)\n"
        "s_barrier\n"
        "v_mfma_f32_32x32x16_bf16 v[80:95], v[156:159], v[112:115], v[80:95]\n"
        "v_add_u32 v16, 0xe000, v16\n"
        "v_add_u32 v17, 0xe000, v17\n"
        "v_add_u32 v18, 0xe000, v18\n"
        "v_add_u32 v19, 0xe000, v19\n"
        "v_add_u32 v20, 0xe000, v20\n"
        "v_add_u32 v21, 0xe000, v21\n"
        "v_add_u32 v22, 0xe000, v22\n"
        "v_add_u32 v23, 0xe000, v23\n"
        "ds_read_b128 v[160:163], v16 offset:0\n"
        "ds_read_b64_tr_b16 v[64:65], v20 offset:16384\n"
        "ds_read_b64_tr_b16 v[66:67], v20 offset:17152\n"
        "ds_read_b128 v[164:167], v17 offset:0\n"
        "s_add_u32 m0, s50, 0\n"
        "s_nop 0\n"
        "buffer_load_dwordx4 v24, s[60:63], s40 offen lds\n"
        "v_sub_u32 v10, v11, v10\n"
        "v_cmp_le_u32 vcc, s56, v10\n"
        "v_subrev_u32 v10, 32, v34\n"
        "s_nop 0\n"
        "v_cndmask_b32 v34, v34, v10, vcc\n"
        "s_mul_i32 s39, s22, 96\n"
        "s_add_u32 s39, s39, 57344\n"
        "s_waitcnt lgkmcnt(1)\n"
        "v_mfma_f32_32x32x16_bf16 v[80:95], v[160:163], v[64:67], v[80:95]\n"
        "ds_read_b64_tr_b16 v[112:113], v21 offset:16384\n"
        "ds_read_b64_tr_b16 v[114:115], v21 offset:17152\n"
        "v_mfma_f32_4x4x4_16b_bf16 v[44:47], v[40:41], v[64:65], v[44:47]\n"
        "ds_read_b128 v[168:171], v18 offset:0\n"
        "buffer_load_dwordx4 v25, s[60:63], s40 offen offset:1024 lds\n"
        "v_mfma_f32_4x4x4_16b_bf16 v[96:99], v[40:41], v[160:161], v[96:99]\n"
        "v_mul_u32_u24 v9, 12320, v7\n"
        "v_mfma_f32_4x4x4_16b_bf16 v[100:103], v[160:161], v[160:161], v[100:103]\n"
        "v_add3_u32 v9, v9, v14, s39\n"
        "s_add_u32 s39, s21, 0\n"
        "v_mfma_f32_4x4x4_16b_bf16 v[104:107], v[40:41], v[162:163], v[104:107]\n"
        "s_and_b32 s39, s39, 3\n"
        "v_mfma_f32_4x4x4_16b_bf16 v[108:111], v[162:163], v[162:163], v[108:111]\n"
        "s_mul_i32 s39, s39, 3072\n"
        "v_add_u32 v36, s39, v9\n"
        "v_mfma_f32_4x4x4_16b_bf16 v[44:47], v[40:41], v[66:67], v[44:47]\n"
        "s_add_u32 s39, s21, 1\n"
        "s_waitcnt lgkmcnt(1)\n"
        "v_mfma_f32_32x32x16_bf16 v[80:95], v[164:167], v[112:115], v[80:95]\n"
        "ds_read_b64_tr_b16 v[64:65], v22 offset:16384\n"
        "ds_read_b64_tr_b16 v[66:67], v22 offset:17152\n"
        "ds_read_b128 v[172:175], v19 offset:0\n"
        "v_mfma_f32_4x4x4_16b_bf16 v[96:99], v[40:41], v[164:165], v[96:99]\n"
        "s_add_u32 m0, s51, 16384\n"
        "s_nop 0\n"
        "buffer_load_dwordx4 v26, s[64:67], s41 offen lds\n"
        "v_mfma_f32_4x4x4_16b_bf16 v[100:103], v[164:165], v[164:165], v[100:103]\n"
        "s_add_u32 s41, s41, 196608\n"
        "s_add_u32 s40, s40, 128\n"
        "s_cmp_eq_u32 s43, 5\n"
        "v_mfma_f32_4x4x4_16b_bf16 v[104:107], v[40:41], v[166:167], v[104:107]\n"
        "s_cselect_b32 s41, s99, s41\n"
        "s_cselect_b32 s40, s98, s40\n"
        "v_mfma_f32_4x4x4_16b_bf16 v[108:111], v[166:167], v[166:167], v[108:111]\n"
        "s_and_b32 s39, s39, 3\n"
        "s_mul_i32 s39, s39, 3072\n"
        "v_add_u32 v37, s39, v9\n"
        "s_waitcnt lgkmcnt(3)\n"
        "v_mfma_f32_4x4x4_16b_bf16 v[96:99], v[40:41], v[168:169], v[96:99]\n"
        "s_add_u32 s39, s21, 2\n"
        "s_and_b32 s39, s39, 3\n"
        "v_mfma_f32_4x4x4_16b_bf16 v[100:103], v[168:169], v[168:169], v[100:103]\n"
        "s_mul_i32 s39, s39, 3072\n"
        "v_add_u32 v38, s39, v9\n"
        "s_waitcnt lgkmcnt(1)\n"
        "v_mfma_f32_32x32x16_bf16 v[80:95], v[168:171], v[64:67], v[80:95]\n"
        "ds_read_b64_tr_b16 v[112:113], v23 offset:16384\n"
        "ds_read_b64_tr_b16 v[114:115], v23 offset:17152\n"
        "v_mfma_f32_4x4x4_16b_bf16 v[104:107], v[40:41], v[170:171], v[104:107]\n"
        "s_add_u32 s39, s21, 3\n"
        "v_mfma_f32_4x4x4_16b_bf16 v[108:111], v[170:171], v[170:171], v[108:111]\n"
        "s_and_b32 s39, s39, 3\n"
        "s_waitcnt lgkmcnt(2)\n"
        "v_mfma_f32_4x4x4_16b_bf16 v[96:99], v[40:41], v[172:173], v[96:99]\n"
        "s_mul_i32 s39, s39, 3072\n"
        "v_mfma_f32_4x4x4_16b_bf16 v[100:103], v[172:173], v[172:173], v[100:103]\n"
        "v_add_u32 v39, s39, v9\n"
        "v_mfma_f32_4x4x4_16b_bf16 v[104:107], v[40:41], v[174:175], v[104:107]\n"
        "s_lshl_b32 s29, s25, 2\n"
        "v_mfma_f32_4x4x4_16b_bf16 v[108:111], v[174:175], v[174:175], v[108:111]\n"
        "s_add_u32 s29, s29, s21\n"
        "s_waitcnt vmcnt(6) lgkmcnt(0)\n"
        "s_barrier\n"
        "v_mfma_f32_32x32x16_bf16 v[80:95], v[172:175], v[112:115], v[80:95]\n"
        "ds_read_b128 v[176:179], v16 offset:28672\n"
        "ds_read_b64_tr_b16 v[64:65], v20 offset:45056\n"
        "ds_read_b64_tr_b16 v[66:67], v20 offset:45824\n"
        "ds_read_b128 v[180:183], v17 offset:28672\n"
        "s_add_u32 m0, s50, 28672\n"
        "s_nop 0\n"
        "buffer_load_dwordx4 v24, s[60:63], s40 offen lds\n"
        "s_mov_b32 s32, s18\n"
        "s_and_b32 s33, s19, 0xffff\n"
        "s_mov_b32 s34, 14090240\n"
        "s_mov_b32 s35, 0x20000\n"
        "s_lshl_b32 s39, s25, 4\n"
        "s_add_u32 s39, s39, s26\n"
        "s_lshl_b32 s39, s39, 3\n"
        "s_waitcnt lgkmcnt(1)\n"
        "v_mfma_f32_32x32x16_bf16 v[80:95], v[176:179], v[64:67], v[80:95]\n"
        "ds_read_b64_tr_b16 v[112:113], v21 offset:45056\n"
        "ds_read_b64_tr_b16 v[114:115], v21 offset:45824\n"
        "v_mfma_f32_4x4x4_16b_bf16 v[44:47], v[40:41], v[64:65], v[44:47]\n"
        "ds_read_b128 v[184:187], v18 offset:28672\n"
        "buffer_load_dwordx4 v25, s[60:63], s40 offen offset:1024 lds\n"
        "v_mfma_f32_4x4x4_16b_bf16 v[96:99], v[40:41], v[176:177], v[96:99]\n"
        "s_add_u32 s39, s39, 4096\n"
        "s_add_u32 s54, s18, s39\n"
        "v_mfma_f32_4x4x4_16b_bf16 v[100:103], v[176:177], v[176:177], v[100:103]\n"
        "s_addc_u32 s55, s19, 0\n"
        "v_mfma_f32_4x4x4_16b_bf16 v[104:107], v[40:41], v[178:179], v[104:107]\n"
        "s_lshl_b32 s37, s23, 2\n"
        "s_and_b32 s39, s24, 3\n"
        "v_mfma_f32_4x4x4_16b_bf16 v[108:111], v[178:179], v[178:179], v[108:111]\n"
        "s_add_u32 s37, s37, s39\n"
        "s_lshr_b32 s38, s24, 2\n"
        "v_mfma_f32_4x4x4_16b_bf16 v[44:47], v[40:41], v[66:67], v[44:47]\n"
        "s_lshl_b32 s39, s38, 3\n"
        "s_waitcnt lgkmcnt(1)\n"
        "v_mfma_f32_32x32x16_bf16 v[80:95], v[180:183], v[112:115], v[80:95]\n"
        "ds_read_b64_tr_b16 v[64:65], v22 offset:45056\n"
        "ds_read_b64_tr_b16 v[66:67], v22 offset:45824\n"
        "ds_read_b128 v[188:191], v19 offset:28672\n"
        "v_mfma_f32_4x4x4_16b_bf16 v[96:99], v[40:41], v[180:181], v[96:99]\n"
        "s_add_u32 m0, s51, 45056\n"
        "s_nop 0\n"
        "buffer_load_dwordx4 v26, s[64:67], s41 offen lds\n"
        "v_mfma_f32_4x4x4_16b_bf16 v[100:103], v[180:181], v[180:181], v[100:103]\n"
        "s_add_u32 s41, s41, 196608\n"
        "s_add_u32 s40, s40, 128\n"
        "s_cmp_eq_u32 s43, 6\n"
        "v_mfma_f32_4x4x4_16b_bf16 v[104:107], v[40:41], v[182:183], v[104:107]\n"
        "s_cselect_b32 s41, s99, s41\n"
        "s_cselect_b32 s40, s98, s40\n"
        "v_mfma_f32_4x4x4_16b_bf16 v[108:111], v[182:183], v[182:183], v[108:111]\n"
        "s_lshl_b32 s56, s21, 1\n"
        "s_add_u32 s39, s39, s56\n"
        "s_add_u32 s39, s39, s22\n"
        "s_waitcnt lgkmcnt(3)\n"
        "v_mfma_f32_4x4x4_16b_bf16 v[96:99], v[40:41], v[184:185], v[96:99]\n"
        "s_lshl_b32 s39, s39, 5\n"
        "s_add_u32 s39, s39, s37\n"
        "v_mfma_f32_4x4x4_16b_bf16 v[100:103], v[184:185], v[184:185], v[100:103]\n"
        "s_lshl_b32 s39, s39, 10\n"
        "s_add_u32 s3, s39, 11993088\n"
        "s_waitcnt lgkmcnt(1)\n"
        "v_mfma_f32_32x32x16_bf16 v[80:95], v[184:187], v[64:67], v[80:95]\n"
        "ds_read_b64_tr_b16 v[112:113], v23 offset:45056\n"
        "ds_read_b64_tr_b16 v[114:115], v23 offset:45824\n"
        "v_mfma_f32_4x4x4_16b_bf16 v[104:107], v[40:41], v[186:187], v[104:107]\n"
        "s_add_u32 s39, s36, 0\n"
        "v_mfma_f32_4x4x4_16b_bf16 v[108:111], v[186:187], v[186:187], v[108:111]\n"
        "s_and_b32 s56, s39, 15\n"
        "s_waitcnt lgkmcnt(2)\n"
        "v_mfma_f32_4x4x4_16b_bf16 v[96:99], v[40:41], v[188:189], v[96:99]\n"
        "s_lshl_b32 s56, s56, 7\n"
        "v_mfma_f32_4x4x4_16b_bf16 v[100:103], v[188:189], v[188:189], v[100:103]\n"
        "s_add_u32 s56, s56, s29\n"
        "v_mfma_f32_4x4x4_16b_bf16 v[104:107], v[40:41], v[190:191], v[104:107]\n"
        "s_lshl_b32 s57, s56, 10\n"
        "v_mfma_f32_4x4x4_16b_bf16 v[108:111], v[190:191], v[190:191], v[108:111]\n"
        "s_add_u32 s57, s57, 65536\n"
        "s_waitcnt vmcnt(6) lgkmcnt(0)\n"
        "s_barrier\n"
        "v_mfma_f32_32x32x16_bf16 v[80:95], v[188:191], v[112:115], v[80:95]\n"
        "v_add_u32 v16, 0xe000, v16\n"
        "v_add_u32 v17, 0xe000, v17\n"
        "v_add_u32 v18, 0xe000, v18\n"
        "v_add_u32 v19, 0xe000, v19\n"
        "v_add_u32 v20, 0xe000, v20\n"
        "v_add_u32 v21, 0xe000, v21\n"
        "v_add_u32 v22, 0xe000, v22\n"
        "v_add_u32 v23, 0xe000, v23\n"
        "ds_read_b128 v[192:195], v16 offset:0\n"
        "ds_read_b64_tr_b16 v[64:65], v20 offset:16384\n"
        "ds_read_b64_tr_b16 v[66:67], v20 offset:17152\n"
        "ds_read_b128 v[196:199], v17 offset:0\n"
        "s_add_u32 m0, s50, 57344\n"
        "s_nop 0\n"
        "buffer_load_dwordx4 v24, s[60:63], s40 offen lds\n"
        "s_lshl_b32 s56, s56, 11\n"
        "s_add_u32 s56, s56, 2162688\n"
        "s_mov_b32 s59, 0x20000\n"
        "s_cmp_gt_u32 s39, 31\n"
        "s_cselect_b32 s58, 1024, 0\n"
        "s_cselect_b32 s59, 512, s59\n"
        "s_add_u32 s56, s56, s58\n"
        "s_cmp_lt_u32 s39, 16\n"
        "s_cselect_b32 s92, s57, s56\n"
        "s_cselect_b32 s59, 0x10000, s59\n"
        "s_cselect_b32 s82, 0x3eb8aa3b, 1.0\n"
        "s_add_u32 s94, s92, s59\n"
        "s_waitcnt lgkmcnt(1)\n"
        "v_mfma_f32_32x32x16_bf16 v[80:95], v[192:195], v[64:67], v[80:95]\n"
        "ds_read_b64_tr_b16 v[112:113], v21 offset:16384\n"
        "ds_read_b64_tr_b16 v[114:115], v21 offset:17152\n"
        "v_mfma_f32_4x4x4_16b_bf16 v[44:47], v[40:41], v[64:65], v[44:47]\n"
        "ds_read_b128 v[200:203], v18 offset:0\n"
        "buffer_load_dwordx4 v25, s[60:63], s40 offen offset:1024 lds\n"
        "v_mfma_f32_4x4x4_16b_bf16 v[96:99], v[40:41], v[192:193], v[96:99]\n"
        "s_add_u32 s39, s36, 1\n"
        "v_mfma_f32_4x4x4_16b_bf16 v[100:103], v[192:193], v[192:193], v[100:103]\n"
        "s_and_b32 s56, s39, 15\n"
        "s_lshl_b32 s56, s56, 7\n"
        "v_mfma_f32_4x4x4_16b_bf16 v[104:107], v[40:41], v[194:195], v[104:107]\n"
        "s_add_u32 s56, s56, s29\n"
        "v_mfma_f32_4x4x4_16b_bf16 v[108:111], v[194:195], v[194:195], v[108:111]\n"
        "s_lshl_b32 s57, s56, 10\n"
        "s_add_u32 s57, s57, 65536\n"
        "v_mfma_f32_4x4x4_16b_bf16 v[44:47], v[40:41], v[66:67], v[44:47]\n"
        "s_lshl_b32 s56, s56, 11\n"
        "s_waitcnt lgkmcnt(1)\n"
        "v_mfma_f32_32x32x16_bf16 v[80:95], v[196:199], v[112:115], v[80:95]\n"
        "ds_read_b64_tr_b16 v[64:65], v22 offset:16384\n"
        "ds_read_b64_tr_b16 v[66:67], v22 offset:17152\n"
        "ds_read_b128 v[204:207], v19 offset:0\n"
        "v_mfma_f32_4x4x4_16b_bf16 v[96:99], v[40:41], v[196:197], v[96:99]\n"
        "s_add_u32 m0, s51, 73728\n"
        "s_nop 0\n"
        "buffer_load_dwordx4 v26, s[64:67], s41 offen lds\n"
        "s_add_u32 s41, s41, 196608\n"
        "v_mfma_f32_4x4x4_16b_bf16 v[100:103], v[196:197], v[196:197], v[100:103]\n"
        "s_add_u32 s40, s40, 128\n"
        "s_add_u32 s56, s56, 2162688\n"
        "v_mfma_f32_4x4x4_16b_bf16 v[104:107], v[40:41], v[198:199], v[104:107]\n"
        "s_mov_b32 s59, 0x20000\n"
        "s_cmp_gt_u32 s39, 31\n"
        "s_cselect_b32 s58, 1024, 0\n"
        "v_mfma_f32_4x4x4_16b_bf16 v[108:111], v[198:199], v[198:199], v[108:111]\n"
        "s_cselect_b32 s59, 512, s59\n"
        "s_add_u32 s56, s56, s58\n"
        "s_waitcnt lgkmcnt(3)\n"
        "v_mfma_f32_4x4x4_16b_bf16 v[96:99], v[40:41], v[200:201], v[96:99]\n"
        "s_cmp_lt_u32 s39, 16\n"
        "s_cselect_b32 s93, s57, s56\n"
        "s_cselect_b32 s59, 0x10000, s59\n"
        "v_mfma_f32_4x4x4_16b_bf16 v[100:103], v[200:201], v[200:201], v[100:103]\n"
        "s_cselect_b32 s83, 0x3eb8aa3b, 1.0\n"
        "s_add_u32 s95, s93, s59\n"
        "s_waitcnt lgkmcnt(1)\n"
        "v_mfma_f32_32x32x16_bf16 v[80:95], v[200:203], v[64:67], v[80:95]\n"
        "ds_read_b64_tr_b16 v[112:113], v23 offset:16384\n"
        "ds_read_b64_tr_b16 v[114:115], v23 offset:17152\n"
        "v_mfma_f32_4x4x4_16b_bf16 v[104:107], v[40:41], v[202:203], v[104:107]\n"
        "v_mfma_f32_4x4x4_16b_bf16 v[108:111], v[202:203], v[202:203], v[108:111]\n"
        "s_waitcnt lgkmcnt(2)\n"
        "v_mfma_f32_4x4x4_16b_bf16 v[96:99], v[40:41], v[204:205], v[96:99]\n"
        "v_mfma_f32_4x4x4_16b_bf16 v[100:103], v[204:205], v[204:205], v[100:103]\n"
        "v_mfma_f32_4x4x4_16b_bf16 v[104:107], v[40:41], v[206:207], v[104:107]\n"
        "v_mfma_f32_4x4x4_16b_bf16 v[108:111], v[206:207], v[206:207], v[108:111]\n"
        "s_waitcnt vmcnt(6) lgkmcnt(0)\n"
        "s_barrier\n"
        "v_mfma_f32_32x32x16_bf16 v[80:95], v[204:207], v[112:115], v[80:95]\n"
        "v_add_u32 v16, 0xfffe4000, v16\n"
        "v_add_u32 v17, 0xfffe4000, v17\n"
        "v_add_u32 v18, 0xfffe4000, v18\n"
        "v_add_u32 v19, 0xfffe4000, v19\n"
        "v_add_u32 v20, 0xfffe4000, v20\n"
        "v_add_u32 v21, 0xfffe4000, v21\n"
        "v_add_u32 v22, 0xfffe4000, v22\n"
        "v_add_u32 v23, 0xfffe4000, v23\n"
        "ds_read_b128 v[208:211], v16 offset:0\n"
        "ds_read_b64_tr_b16 v[64:65], v20 offset:16384\n"
        "ds_read_b64_tr_b16 v[66:67], v20 offset:17152\n"
        "ds_read_b128 v[212:215], v17 offset:0\n"
        "s_add_u32 m0, s51, 86016\n"
        "s_nop 0\n"
        "buffer_load_dwordx4 v32, s[68:71], s42 offen lds\n"
        "s_waitcnt lgkmcnt(1)\n"
        "v_mfma_f32_32x32x16_bf16 v[80:95], v[208:211], v[64:67], v[80:95]\n"
        "ds_read_b64_tr_b16 v[112:113], v21 offset:16384\n"
        "ds_read_b64_tr_b16 v[114:115], v21 offset:17152\n"
        "v_mfma_f32_4x4x4_16b_bf16 v[44:47], v[40:41], v[64:65], v[44:47]\n"
        "v_mfma_f32_4x4x4_16b_bf16 v[96:99], v[40:41], v[208:209], v[96:99]\n"
        "ds_read_b128 v[216:219], v18 offset:0\n"
        "v_mfma_f32_4x4x4_16b_bf16 v[100:103], v[208:209], v[208:209], v[100:103]\n"
        "v_mfma_f32_4x4x4_16b_bf16 v[104:107], v[40:41], v[210:211], v[104:107]\n"
        "s_add_u32 m0, s52, 86016\n"
        "v_mfma_f32_4x4x4_16b_bf16 v[108:111], v[210:211], v[210:211], v[108:111]\n"
        "buffer_load_dwordx4 v33, s[72:75], s42 offen lds\n"
        "v_mfma_f32_4x4x4_16b_bf16 v[44:47], v[40:41], v[66:67], v[44:47]\n"
        "s_waitcnt lgkmcnt(1)\n"
        "v_mfma_f32_32x32x16_bf16 v[80:95], v[212:215], v[112:115], v[80:95]\n"
        "ds_read_b64_tr_b16 v[64:65], v22 offset:16384\n"
        "ds_read_b64_tr_b16 v[66:67], v22 offset:17152\n"
        "v_mfma_f32_4x4x4_16b_bf16 v[96:99], v[40:41], v[212:213], v[96:99]\n"
        "ds_read_b128 v[220:223], v19 offset:0\n"
        "v_mfma_f32_4x4x4_16b_bf16 v[100:103], v[212:213], v[212:213], v[100:103]\n"
        "s_add_u32 m0, s51, 102432\n"
        "v_mfma_f32_4x4x4_16b_bf16 v[104:107], v[40:41], v[214:215], v[104:107]\n"
        "buffer_load_dwordx4 v34, s[76:79], s42 offen lds\n"
        "v_mfma_f32_4x4x4_16b_bf16 v[108:111], v[214:215], v[214:215], v[108:111]\n"
        "s_add_u32 s42, s42, 176128\n"
        "s_waitcnt lgkmcnt(3)\n"
        "v_mfma_f32_4x4x4_16b_bf16 v[96:99], v[40:41], v[216:217], v[96:99]\n"
        "s_cmp_eq_u32 s43, 0\n"
        "v_mfma_f32_4x4x4_16b_bf16 v[100:103], v[216:217], v[216:217], v[100:103]\n"
        "s_cselect_b32 s42, s47, s42\n"
        "s_waitcnt lgkmcnt(1)\n"
        "v_mfma_f32_32x32x16_bf16 v[80:95], v[216:219], v[64:67], v[80:95]\n"
        "ds_read_b64_tr_b16 v[112:113], v23 offset:16384\n"
        "ds_read_b64_tr_b16 v[114:115], v23 offset:17152\n"
        "v_mfma_f32_4x4x4_16b_bf16 v[104:107], v[40:41], v[218:219], v[104:107]\n"
        "v_mfma_f32_4x4x4_16b_bf16 v[108:111], v[218:219], v[218:219], v[108:111]\n"
        "s_waitcnt lgkmcnt(2)\n"
        "v_mfma_f32_4x4x4_16b_bf16 v[96:99], v[40:41], v[220:221], v[96:99]\n"
        "v_mfma_f32_4x4x4_16b_bf16 v[100:103], v[220:221], v[220:221], v[100:103]\n"
        "v_mfma_f32_4x4x4_16b_bf16 v[104:107], v[40:41], v[222:223], v[104:107]\n"
        "v_mfma_f32_4x4x4_16b_bf16 v[108:111], v[222:223], v[222:223], v[108:111]\n"
        "s_waitcnt vmcnt(6) lgkmcnt(0)\n"
        "s_barrier\n"
        "v_mfma_f32_32x32x16_bf16 v[80:95], v[220:223], v[112:115], v[80:95]\n"
        "ds_read_b128 v[224:227], v16 offset:28672\n"
        "ds_read_b64_tr_b16 v[64:65], v20 offset:45056\n"
        "ds_read_b64_tr_b16 v[66:67], v20 offset:45824\n"
        "ds_read_b128 v[228:231], v17 offset:28672\n"
        "s_add_u32 m0, s51, 114688\n"
        "s_nop 0\n"
        "buffer_load_dwordx4 v32, s[68:71], s42 offen lds\n"
        "s_waitcnt lgkmcnt(1)\n"
        "v_mfma_f32_32x32x16_bf16 v[80:95], v[224:227], v[64:67], v[80:95]\n"
        "ds_read_b64_tr_b16 v[112:113], v21 offset:45056\n"
        "ds_read_b64_tr_b16 v[114:115], v21 offset:45824\n"
        "v_mfma_f32_4x4x4_16b_bf16 v[44:47], v[40:41], v[64:65], v[44:47]\n"
        "v_mfma_f32_4x4x4_16b_bf16 v[96:99], v[40:41], v[224:225], v[96:99]\n"
        "ds_read_b128 v[232:235], v18 offset:28672\n"
        "v_mfma_f32_4x4x4_16b_bf16 v[100:103], v[224:225], v[224:225], v[100:103]\n"
        "v_mfma_f32_4x4x4_16b_bf16 v[104:107], v[40:41], v[226:227], v[104:107]\n"
        "s_add_u32 m0, s52, 114688\n"
        "v_mfma_f32_4x4x4_16b_bf16 v[108:111], v[226:227], v[226:227], v[108:111]\n"
        "buffer_load_dwordx4 v33, s[72:75], s42 offen lds\n"
        "v_mfma_f32_4x4x4_16b_bf16 v[44:47], v[40:41], v[66:67], v[44:47]\n"
        "s_waitcnt lgkmcnt(1)\n"
        "v_mfma_f32_32x32x16_bf16 v[80:95], v[228:231], v[112:115], v[80:95]\n"
        "ds_read_b64_tr_b16 v[64:65], v22 offset:45056\n"
        "ds_read_b64_tr_b16 v[66:67], v22 offset:45824\n"
        "v_mfma_f32_4x4x4_16b_bf16 v[96:99], v[40:41], v[228:229], v[96:99]\n"
        "ds_read_b128 v[236:239], v19 offset:28672\n"
        "v_mfma_f32_4x4x4_16b_bf16 v[100:103], v[228:229], v[228:229], v[100:103]\n"
        "s_add_u32 m0, s51, 131104\n"
        "v_mfma_f32_4x4x4_16b_bf16 v[104:107], v[40:41], v[230:231], v[104:107]\n"
        "buffer_load_dwordx4 v34, s[76:79], s42 offen lds\n"
        "v_mfma_f32_4x4x4_16b_bf16 v[108:111], v[230:231], v[230:231], v[108:111]\n"
        "s_add_u32 s42, s42, 176128\n"
        "s_waitcnt lgkmcnt(3)\n"
        "v_mfma_f32_4x4x4_16b_bf16 v[96:99], v[40:41], v[232:233], v[96:99]\n"
        "s_cmp_eq_u32 s43, 1\n"
        "v_mfma_f32_4x4x4_16b_bf16 v[100:103], v[232:233], v[232:233], v[100:103]\n"
        "s_cselect_b32 s42, s47, s42\n"
        "s_waitcnt lgkmcnt(1)\n"
        "v_mfma_f32_32x32x16_bf16 v[80:95], v[232:235], v[64:67], v[80:95]\n"
        "ds_read_b64_tr_b16 v[112:113], v23 offset:45056\n"
        "ds_read_b64_tr_b16 v[114:115], v23 offset:45824\n"
        "v_mfma_f32_4x4x4_16b_bf16 v[104:107], v[40:41], v[234:235], v[104:107]\n"
        "v_mfma_f32_4x4x4_16b_bf16 v[108:111], v[234:235], v[234:235], v[108:111]\n"
        "s_waitcnt lgkmcnt(2)\n"
        "v_mfma_f32_4x4x4_16b_bf16 v[96:99], v[40:41], v[236:237], v[96:99]\n"
        "v_mfma_f32_4x4x4_16b_bf16 v[100:103], v[236:237], v[236:237], v[100:103]\n"
        "v_mfma_f32_4x4x4_16b_bf16 v[104:107], v[40:41], v[238:239], v[104:107]\n"
        "v_mfma_f32_4x4x4_16b_bf16 v[108:111], v[238:239], v[238:239], v[108:111]\n"
        "s_waitcnt vmcnt(6) lgkmcnt(0)\n"
        "s_barrier\n"
        "v_mfma_f32_32x32x16_bf16 v[80:95], v[236:239], v[112:115], v[80:95]\n"
        "v_add_u32 v16, 0xe000, v16\n"
        "v_add_u32 v17, 0xe000, v17\n"
        "v_add_u32 v18, 0xe000, v18\n"
        "v_add_u32 v19, 0xe000, v19\n"
        "v_add_u32 v20, 0xe000, v20\n"
        "v_add_u32 v21, 0xe000, v21\n"
        "v_add_u32 v22, 0xe000, v22\n"
        "v_add_u32 v23, 0xe000, v23\n"
        "ds_read_b128 v[240:243], v16 offset:0\n"
        "ds_read_b64_tr_b16 v[64:65], v20 offset:16384\n"
        "ds_read_b64_tr_b16 v[66:67], v20 offset:17152\n"
        "ds_read_b128 v[244:247], v17 offset:0\n"
        "ds_read_b128 v[248:251], v18 offset:0\n"
        "ds_read_b128 v[252:255], v19 offset:0\n"
        "s_add_u32 m0, s51, 0\n"
        "s_nop 0\n"
        "buffer_load_dwordx4 v32, s[68:71], s42 offen lds\n"
        "s_waitcnt lgkmcnt(3)\n"
        "v_mfma_f32_32x32x16_bf16 v[80:95], v[240:243], v[64:67], v[80:95]\n"
        "ds_read_b64_tr_b16 v[112:113], v21 offset:16384\n"
        "ds_read_b64_tr_b16 v[114:115], v21 offset:17152\n"
        "v_mfma_f32_4x4x4_16b_bf16 v[44:47], v[40:41], v[64:65], v[44:47]\n"
        "v_mfma_f32_4x4x4_16b_bf16 v[96:99], v[40:41], v[240:241], v[96:99]\n"
        "v_mfma_f32_4x4x4_16b_bf16 v[100:103], v[240:241], v[240:241], v[100:103]\n"
        "v_mfma_f32_4x4x4_16b_bf16 v[104:107], v[40:41], v[242:243], v[104:107]\n"
        "v_mfma_f32_4x4x4_16b_bf16 v[108:111], v[242:243], v[242:243], v[108:111]\n"
        "s_add_u32 m0, s52, 0\n"
        "s_waitcnt lgkmcnt(4)\n"
        "v_mfma_f32_4x4x4_16b_bf16 v[96:99], v[40:41], v[244:245], v[96:99]\n"
        "v_mfma_f32_4x4x4_16b_bf16 v[100:103], v[244:245], v[244:245], v[100:103]\n"
        "v_mfma_f32_4x4x4_16b_bf16 v[104:107], v[40:41], v[246:247], v[104:107]\n"
        "buffer_load_dwordx4 v33, s[72:75], s42 offen lds\n"
        "v_mfma_f32_4x4x4_16b_bf16 v[108:111], v[246:247], v[246:247], v[108:111]\n"
        "v_mfma_f32_4x4x4_16b_bf16 v[44:47], v[40:41], v[66:67], v[44:47]\n"
        "s_waitcnt lgkmcnt(0)\n"
        "v_mfma_f32_32x32x16_bf16 v[80:95], v[244:247], v[112:115], v[80:95]\n"
        "ds_read_b64_tr_b16 v[64:65], v22 offset:16384\n"
        "ds_read_b64_tr_b16 v[66:67], v22 offset:17152\n"
        "s_add_u32 m0, s51, 16416\n"
        "v_mfma_f32_4x4x4_16b_bf16 v[96:99], v[40:41], v[248:249], v[96:99]\n"
        "buffer_load_dwordx4 v34, s[76:79], s42 offen lds\n"
        "s_add_u32 s42, s42, 176128\n"
        "s_cmp_eq_u32 s43, 2\n"
        "v_mfma_f32_4x4x4_16b_bf16 v[100:103], v[248:249], v[248:249], v[100:103]\n"
        "s_cselect_b32 s42, s47, s42\n"
        "v_lshlrev_b32 v3, 4, v2\n"
        "s_lshl_b32 s39, s21, 2\n"
        "v_mfma_f32_4x4x4_16b_bf16 v[104:107], v[40:41], v[250:251], v[104:107]\n"
        "s_add_u32 s39, s39, 158208\n"
        "v_add_u32 v4, s39, v3\n"
        "v_and_b32 v5, 31, v2\n"
        "v_mfma_f32_4x4x4_16b_bf16 v[108:111], v[250:251], v[250:251], v[108:111]\n"
        "s_lshl_b32 s56, s21, 5\n"
        "v_add_u32 v5, s56, v5\n"
        "v_lshlrev_b32 v6, 4, v5\n"
        "v_mfma_f32_4x4x4_16b_bf16 v[96:99], v[40:41], v[252:253], v[96:99]\n"
        "v_add_u32 v6, 153600, v6\n"
        "v_bfe_u32 v7, v2, 4, 1\n"
        "v_lshrrev_b32 v8, 5, v2\n"
        "v_mfma_f32_4x4x4_16b_bf16 v[100:103], v[252:253], v[252:253], v[100:103]\n"
        "v_and_b32 v9, 15, v2\n"
        "v_lshlrev_b32 v9, 4, v9\n"
        "v_lshl_or_b32 v9, v8, 8, v9\n"
        "v_mfma_f32_4x4x4_16b_bf16 v[104:107], v[40:41], v[254:255], v[104:107]\n"
        "v_lshl_or_b32 v43, v7, 17, v9\n"
        "s_lshl_b32 s39, s21, 7\n"
        "s_add_u32 s39, s39, 155648\n"
        "v_mfma_f32_4x4x4_16b_bf16 v[108:111], v[254:255], v[254:255], v[108:111]\n"
        "v_lshlrev_b32 v42, 4, v8\n"
        "v_add_u32 v42, s39, v42\n"
        "s_waitcnt lgkmcnt(0)\n"
        "v_mfma_f32_32x32x16_bf16 v[80:95], v[248:251], v[64:67], v[80:95]\n"
        "ds_read_b64_tr_b16 v[112:113], v23 offset:16384\n"
        "ds_read_b64_tr_b16 v[114:115], v23 offset:17152\n"
        "ds_write_b32 v4, v44 offset:0\n"
        "v_add_u32 v4, 158208, v3\n"
        "v_add_f32 v11, v96, v104\n"
        "v_add_f32 v7, v100, v108\n"
        "v_add_f32 v8, v101, v109\n"
        "v_add_f32 v9, v102, v110\n"
        "v_add_f32 v10, v103, v111\n"
        "s_mov_b32 s56, 0x22222222\n"
        "s_mov_b32 s57, s56\n"
        "s_lshl_b64 s[58:59], s[56:57], 1\n"
        "v_cndmask_b32 v7, v7, v8, s[56:57]\n"
        "s_lshl_b64 s[56:57], s[56:57], 2\n"
        "v_cndmask_b32 v7, v7, v9, s[58:59]\n"
        "v_cndmask_b32 v7, v7, v10, s[56:57]\n"
        "v_mov_b32 v8, v11\n"
        "v_mov_b32 v9, v7\n"
        "s_nop 0\n"
        "v_permlane32_swap_b32 v11, v8\n"
        "v_permlane32_swap_b32 v7, v9\n"
        "v_add_f32 v11, v11, v8\n"
        "v_add_f32 v7, v7, v9\n"
        "v_mul_f32 v8, 0x3b000000, v11\n"
        "v_mul_f32 v7, 0x3b000000, v7\n"
        "v_fma_f32 v7, -v8, v8, v7\n"
        "v_max_f32 v7, 0, v7\n"
        "v_add_f32 v10, 0x3727c5ac, v7\n"
        "v_rsq_f32 v9, v10\n"
        "v_lshlrev_b32 v7, 2, v5\n"
        "v_add_u32 v7, 155648, v7\n"
        "ds_write_b96 v6, v[8:10]\n"
        "ds_write_b32 v7, v9\n"
        "s_waitcnt lgkmcnt(0)\n"
        "s_nop 4\n"
        "s_cmp_lg_u32 s28, 0\n"
        "s_cbranch_scc1 .LB3_%=\n"
        "s_waitcnt vmcnt(6) lgkmcnt(0)\n"
        "s_barrier\n"
        "ds_read_b64 v[8:9], v6\n"
        "ds_read_b128 v[12:15], v4 offset:0\n"
        "v_mfma_f32_32x32x16_bf16 v[80:95], v[252:255], v[112:115], v[80:95]\n"
        "ds_read_b64_tr_b16 v[64:65], v36 offset:28672\n"
        "ds_read_b64_tr_b16 v[66:67], v36 offset:29440\n"
        "ds_read_b64_tr_b16 v[68:69], v36 offset:28704\n"
        "ds_read_b64_tr_b16 v[70:71], v36 offset:29472\n"
        "ds_read_b64_tr_b16 v[72:73], v36 offset:28736\n"
        "ds_read_b64_tr_b16 v[74:75], v36 offset:29504\n"
        "s_add_u32 m0, s51, 28672\n"
        "s_nop 0\n"
        "buffer_load_dwordx4 v32, s[68:71], s42 offen lds\n"
        "s_add_u32 m0, s52, 28672\n"
        "s_nop 0\n"
        "buffer_load_dwordx4 v33, s[72:75], s42 offen lds\n"
        "s_add_u32 m0, s51, 45088\n"
        "s_nop 0\n"
        "buffer_load_dwordx4 v34, s[76:79], s42 offen lds\n"
        "s_add_u32 s42, s42, 176128\n"
        "s_cmp_eq_u32 s43, 3\n"
        "s_cselect_b32 s42, s47, s42\n"
        "v_mov_b32 v46, 0\n"
        "v_mov_b32 v47, 0\n"
        "v_mov_b32 v50, 0\n"
        "v_mov_b32 v51, 0\n"
        "s_waitcnt lgkmcnt(7)\n"
        "v_cvt_pk_bf16_f32 v44, -v8, -v8\n"
        "v_lshlrev_b32 v7, 16, v44\n"
        "v_sub_f32 v7, -v8, v7\n"
        "v_cvt_pk_bf16_f32 v44, -v8, v7\n"
        "v_mov_b32 v45, v44\n"
        "s_waitcnt lgkmcnt(6)\n"
        "v_add_f32 v12, v12, v13\n"
        "v_add_f32 v14, v14, v15\n"
        "v_add_f32 v12, v12, v14\n"
        "v_cvt_pk_bf16_f32 v48, v12, v12\n"
        "v_lshlrev_b32 v13, 16, v48\n"
        "v_sub_f32 v12, v12, v13\n"
        "v_cvt_pk_bf16_f32 v49, v12, v12\n"
        "s_nop 1\n"
        "v_mfma_f32_32x32x16_bf16 v[0:15], v[44:47], v[48:51], v[80:95]\n"
        "s_waitcnt lgkmcnt(4)\n"
        "v_mfma_f32_32x32x16_bf16 v[80:95], v[64:67], v[128:131], 0\n"
        "ds_read_b64_tr_b16 v[76:77], v37 offset:28672\n"
        "ds_read_b64_tr_b16 v[78:79], v37 offset:29440\n"
        "ds_read_b128 v[48:51], v42 offset:0\n"
        "ds_read_b128 v[52:55], v42 offset:32\n"
        "s_waitcnt lgkmcnt(1)\n"
        "s_nop 4\n"
        "v_mul_f32 v0, v0, v48\n"
        "v_mul_f32 v1, v1, v49\n"
        "v_mfma_f32_32x32x16_bf16 v[96:111], v[68:71], v[128:131], 0\n"
        "ds_read_b64_tr_b16 v[56:57], v37 offset:28704\n"
        "ds_read_b64_tr_b16 v[58:59], v37 offset:29472\n"
        "v_mul_f32 v2, v2, v50\n"
        "v_mul_f32 v3, v3, v51\n"
        "ds_read_b128 v[48:51], v42 offset:64\n"
        "v_mfma_f32_32x32x16_bf16 v[112:127], v[72:75], v[128:131], 0\n"
        "ds_read_b64_tr_b16 v[60:61], v37 offset:28736\n"
        "ds_read_b64_tr_b16 v[62:63], v37 offset:29504\n"
        "s_waitcnt lgkmcnt(5)\n"
        "v_mul_f32 v4, v4, v52\n"
        "v_mul_f32 v5, v5, v53\n"
        "v_mul_f32 v6, v6, v54\n"
        "v_mfma_f32_32x32x16_bf16 v[80:95], v[76:79], v[132:135], v[80:95]\n"
        "ds_read_b64_tr_b16 v[64:65], v38 offset:28672\n"
        "ds_read_b64_tr_b16 v[66:67], v38 offset:29440\n"
        "v_mul_f32 v7, v7, v55\n"
        "ds_read_b128 v[52:55], v42 offset:96\n"
        "s_waitcnt lgkmcnt(5)\n"
        "v_mul_f32 v8, v8, v48\n"
        "v_mul_f32 v9, v9, v49\n"
        "v_mfma_f32_32x32x16_bf16 v[96:111], v[56:59], v[132:135], v[96:111]\n"
        "ds_read_b64_tr_b16 v[68:69], v38 offset:28704\n"
        "ds_read_b64_tr_b16 v[70:71], v38 offset:29472\n"
        "v_mul_f32 v10, v10, v50\n"
        "v_mul_f32 v11, v11, v51\n"
        "s_waitcnt lgkmcnt(2)\n"
        "v_mul_f32 v12, v12, v52\n"
        "v_mfma_f32_32x32x16_bf16 v[112:127], v[60:63], v[132:135], v[112:127]\n"
        "ds_read_b64_tr_b16 v[72:73], v38 offset:28736\n"
        "ds_read_b64_tr_b16 v[74:75], v38 offset:29504\n"
        "v_mul_f32 v13, v13, v53\n"
        "v_mul_f32 v14, v14, v54\n"
        "v_mul_f32 v15, v15, v55\n"
        "v_mfma_f32_32x32x16_bf16 v[80:95], v[64:67], v[136:139], v[80:95]\n"
        "ds_read_b64_tr_b16 v[76:77], v39 offset:28672\n"
        "ds_read_b64_tr_b16 v[78:79], v39 offset:29440\n"
        "v_cvt_pk_bf16_f32 v0, v0, v1\n"
        "v_cvt_pk_bf16_f32 v1, v2, v3\n"
        "v_cvt_pk_bf16_f32 v2, v4, v5\n"
        "v_cvt_pk_bf16_f32 v3, v6, v7\n"
        "s_waitcnt lgkmcnt(4)\n"
        "v_mfma_f32_32x32x16_bf16 v[96:111], v[68:71], v[136:139], v[96:111]\n"
        "ds_read_b64_tr_b16 v[56:57], v39 offset:28704\n"
        "ds_read_b64_tr_b16 v[58:59], v39 offset:29472\n"
        "v_cvt_pk_bf16_f32 v4, v8, v9\n"
        "v_cvt_pk_bf16_f32 v5, v10, v11\n"
        "v_cvt_pk_bf16_f32 v6, v12, v13\n"
        "s_waitcnt lgkmcnt(4)\n"
        "v_mfma_f32_32x32x16_bf16 v[112:127], v[72:75], v[136:139], v[112:127]\n"
        "ds_read_b64_tr_b16 v[60:61], v39 offset:28736\n"
        "ds_read_b64_tr_b16 v[62:63], v39 offset:29504\n"
        "v_cvt_pk_bf16_f32 v7, v14, v15\n"
        "buffer_store_dwordx4 v[0:3], v43, s[32:35], s92 offen sc1\n"
        "buffer_store_dwordx4 v[4:7], v43, s[32:35], s94 offen sc1\n"
        "s_waitcnt lgkmcnt(0)\n"
        "s_branch .LC3_%=\n"
        ".LB3_%=:\n"
        "s_waitcnt vmcnt(6) lgkmcnt(0)\n"
        "s_barrier\n"
        "ds_read_b64 v[8:9], v6\n"
        "ds_read_b128 v[12:15], v4 offset:0\n"
        "v_mfma_f32_32x32x16_bf16 v[80:95], v[252:255], v[112:115], v[80:95]\n"
        "ds_read_b64_tr_b16 v[64:65], v36 offset:28672\n"
        "ds_read_b64_tr_b16 v[66:67], v36 offset:29440\n"
        "ds_read_b64_tr_b16 v[68:69], v36 offset:28704\n"
        "ds_read_b64_tr_b16 v[70:71], v36 offset:29472\n"
        "s_add_u32 m0, s51, 28672\n"
        "s_nop 0\n"
        "buffer_load_dwordx4 v32, s[68:71], s42 offen lds\n"
        "s_add_u32 m0, s52, 28672\n"
        "s_nop 0\n"
        "buffer_load_dwordx4 v33, s[72:75], s42 offen lds\n"
        "s_add_u32 m0, s51, 45088\n"
        "s_nop 0\n"
        "buffer_load_dwordx4 v34, s[76:79], s42 offen lds\n"
        "s_add_u32 s42, s42, 176128\n"
        "s_cmp_eq_u32 s43, 3\n"
        "s_cselect_b32 s42, s47, s42\n"
        "v_mov_b32 v46, 0\n"
        "v_mov_b32 v47, 0\n"
        "v_mov_b32 v50, 0\n"
        "v_mov_b32 v51, 0\n"
        "s_waitcnt lgkmcnt(5)\n"
        "v_cvt_pk_bf16_f32 v44, -v8, -v8\n"
        "v_lshlrev_b32 v7, 16, v44\n"
        "v_sub_f32 v7, -v8, v7\n"
        "v_cvt_pk_bf16_f32 v44, -v8, v7\n"
        "v_mov_b32 v45, v44\n"
        "s_waitcnt lgkmcnt(4)\n"
        "v_add_f32 v12, v12, v13\n"
        "v_add_f32 v14, v14, v15\n"
        "v_add_f32 v12, v12, v14\n"
        "v_cvt_pk_bf16_f32 v48, v12, v12\n"
        "v_lshlrev_b32 v13, 16, v48\n"
        "v_sub_f32 v12, v12, v13\n"
        "v_cvt_pk_bf16_f32 v49, v12, v12\n"
        "s_nop 1\n"
        "v_mfma_f32_32x32x16_bf16 v[0:15], v[44:47], v[48:51], v[80:95]\n"
        "s_waitcnt lgkmcnt(2)\n"
        "v_mfma_f32_32x32x16_bf16 v[80:95], v[64:67], v[128:131], 0\n"
        "ds_read_b64_tr_b16 v[76:77], v37 offset:28672\n"
        "ds_read_b64_tr_b16 v[78:79], v37 offset:29440\n"
        "ds_read_b128 v[48:51], v42 offset:0\n"
        "ds_read_b128 v[52:55], v42 offset:32\n"
        "s_waitcnt lgkmcnt(1)\n"
        "s_nop 4\n"
        "v_mul_f32 v0, v0, v48\n"
        "v_mul_f32 v1, v1, v49\n"
        "v_mul_f32 v2, v2, v50\n"
        "v_mfma_f32_32x32x16_bf16 v[96:111], v[68:71], v[128:131], 0\n"
        "ds_read_b64_tr_b16 v[56:57], v37 offset:28704\n"
        "ds_read_b64_tr_b16 v[58:59], v37 offset:29472\n"
        "v_mul_f32 v3, v3, v51\n"
        "ds_read_b128 v[48:51], v42 offset:64\n"
        "s_waitcnt lgkmcnt(3)\n"
        "v_mul_f32 v4, v4, v52\n"
        "v_mul_f32 v5, v5, v53\n"
        "v_mul_f32 v6, v6, v54\n"
        "v_mfma_f32_32x32x16_bf16 v[80:95], v[76:79], v[132:135], v[80:95]\n"
        "ds_read_b64_tr_b16 v[64:65], v38 offset:28672\n"
        "ds_read_b64_tr_b16 v[66:67], v38 offset:29440\n"
        "v_mul_f32 v7, v7, v55\n"
        "ds_read_b128 v[52:55], v42 offset:96\n"
        "s_waitcnt lgkmcnt(3)\n"
        "v_mul_f32 v8, v8, v48\n"
        "v_mul_f32 v9, v9, v49\n"
        "v_mul_f32 v10, v10, v50\n"
        "v_mfma_f32_32x32x16_bf16 v[96:111], v[56:59], v[132:135], v[96:111]\n"
        "ds_read_b64_tr_b16 v[68:69], v38 offset:28704\n"
        "ds_read_b64_tr_b16 v[70:71], v38 offset:29472\n"
        "v_mul_f32 v11, v11, v51\n"
        "s_waitcnt lgkmcnt(2)\n"
        "v_mul_f32 v12, v12, v52\n"
        "v_mul_f32 v13, v13, v53\n"
        "v_mul_f32 v14, v14, v54\n"
        "v_mul_f32 v15, v15, v55\n"
        "v_mfma_f32_32x32x16_bf16 v[80:95], v[64:67], v[136:139], v[80:95]\n"
        "ds_read_b64_tr_b16 v[76:77], v39 offset:28672\n"
        "ds_read_b64_tr_b16 v[78:79], v39 offset:29440\n"
        "v_cvt_pk_bf16_f32 v0, v0, v1\n"
        "v_cvt_pk_bf16_f32 v1, v2, v3\n"
        "v_cvt_pk_bf16_f32 v2, v4, v5\n"
        "v_cvt_pk_bf16_f32 v3, v6, v7\n"
        "v_cvt_pk_bf16_f32 v4, v8, v9\n"
        "s_waitcnt lgkmcnt(2)\n"
        "v_mfma_f32_32x32x16_bf16 v[96:111], v[68:71], v[136:139], v[96:111]\n"
        "ds_read_b64_tr_b16 v[56:57], v39 offset:28704\n"
        "ds_read_b64_tr_b16 v[58:59], v39 offset:29472\n"
        "v_cvt_pk_bf16_f32 v5, v10, v11\n"
        "v_cvt_pk_bf16_f32 v6, v12, v13\n"
        "v_cvt_pk_bf16_f32 v7, v14, v15\n"
        "buffer_store_dwordx4 v[0:3], v43, s[32:35], s92 offen sc1\n"
        "buffer_store_dwordx4 v[4:7], v43, s[32:35], s94 offen sc1\n"
        "s_waitcnt lgkmcnt(0)\n"
        "s_branch .LC2_%=\n"
        ".LC2_%=:\n"
        "s_waitcnt vmcnt(8) lgkmcnt(0)\n"
        "s_barrier\n"
        "v_mfma_f32_32x32x16_bf16 v[80:95], v[76:79], v[140:143], v[80:95]\n"
        "v_add_u32 v36, 0xe000, v36\n"
        "v_add_u32 v37, 0xe000, v37\n"
        "v_add_u32 v38, 0xe000, v38\n"
        "v_add_u32 v39, 0xe000, v39\n"
        "ds_read_b64_tr_b16 v[64:65], v36 offset:0\n"
        "ds_read_b64_tr_b16 v[66:67], v36 offset:768\n"
        "v_mfma_f32_32x32x16_bf16 v[96:111], v[56:59], v[140:143], v[96:111]\n"
        "ds_read_b64_tr_b16 v[68:69], v36 offset:32\n"
        "ds_read_b64_tr_b16 v[70:71], v36 offset:800\n"
        "v_mfma_f32_4x4x4_16b_bf16 v[44:47], v[40:41], v[76:77], 0\n"
        "v_mfma_f32_4x4x4_16b_bf16 v[48:51], v[40:41], v[56:57], 0\n"
        "s_nop 0\n"
        "v_mfma_f32_4x4x4_16b_bf16 v[44:47], v[40:41], v[78:79], v[44:47]\n"
        "s_add_u32 m0, s51, 57344\n"
        "v_mfma_f32_4x4x4_16b_bf16 v[48:51], v[40:41], v[58:59], v[48:51]\n"
        "buffer_load_dwordx4 v32, s[68:71], s42 offen lds\n"
        "s_waitcnt lgkmcnt(2)\n"
        "v_mfma_f32_32x32x16_bf16 v[80:95], v[64:67], v[144:147], v[80:95]\n"
        "ds_read_b64_tr_b16 v[76:77], v37 offset:0\n"
        "ds_read_b64_tr_b16 v[78:79], v37 offset:768\n"
        "s_waitcnt lgkmcnt(2)\n"
        "v_mfma_f32_32x32x16_bf16 v[96:111], v[68:71], v[144:147], v[96:111]\n"
        "ds_read_b64_tr_b16 v[56:57], v37 offset:32\n"
        "ds_read_b64_tr_b16 v[58:59], v37 offset:800\n"
        "v_mfma_f32_4x4x4_16b_bf16 v[44:47], v[40:41], v[64:65], v[44:47]\n"
        "s_add_u32 m0, s52, 57344\n"
        "s_nop 0\n"
        "buffer_load_dwordx4 v33, s[72:75], s42 offen lds\n"
        "v_mfma_f32_4x4x4_16b_bf16 v[48:51], v[40:41], v[68:69], v[48:51]\n"
        "v_mbcnt_lo_u32_b32 v11, -1, 0\n"
        "v_mbcnt_hi_u32_b32 v11, -1, v11\n"
        "v_mfma_f32_4x4x4_16b_bf16 v[44:47], v[40:41], v[66:67], v[44:47]\n"
        "v_lshlrev_b32 v15, 4, v11\n"
        "s_lshr_b32 s39, s37, 1\n"
        "v_mfma_f32_4x4x4_16b_bf16 v[48:51], v[40:41], v[70:71], v[48:51]\n"
        "s_add_u32 s56, s39, 16\n"
        "s_waitcnt lgkmcnt(2)\n"
        "v_mfma_f32_32x32x16_bf16 v[80:95], v[76:79], v[148:151], v[80:95]\n"
        "ds_read_b64_tr_b16 v[64:65], v38 offset:0\n"
        "ds_read_b64_tr_b16 v[66:67], v38 offset:768\n"
        "s_add_u32 m0, s51, 73760\n"
        "s_nop 0\n"
        "buffer_load_dwordx4 v34, s[76:79], s42 offen lds\n"
        "s_add_u32 s42, s42, 176128\n"
        "s_cmp_eq_u32 s43, 4\n"
        "s_cselect_b32 s42, s47, s42\n"
        "s_waitcnt lgkmcnt(2)\n"
        "v_mfma_f32_32x32x16_bf16 v[96:111], v[56:59], v[148:151], v[96:111]\n"
        "ds_read_b64_tr_b16 v[68:69], v38 offset:32\n"
        "ds_read_b64_tr_b16 v[70:71], v38 offset:800\n"
        "s_add_u32 s57, s39, 32\n"
        "s_mul_i32 s39, s39, 171\n"
        "s_mul_i32 s56, s56, 171\n"
        "s_mul_i32 s57, s57, 171\n"
        "s_lshr_b32 s39, s39, 9\n"
        "s_waitcnt lgkmcnt(2)\n"
        "v_mfma_f32_32x32x16_bf16 v[80:95], v[64:67], v[152:155], v[80:95]\n"
        "ds_read_b64_tr_b16 v[76:77], v39 offset:0\n"
        "ds_read_b64_tr_b16 v[78:79], v39 offset:768\n"
        "s_lshr_b32 s56, s56, 9\n"
        "s_lshr_b32 s57, s57, 9\n"
        "v_and_b32 v26, 15, v11\n"
        "s_waitcnt lgkmcnt(2)\n"
        "v_mfma_f32_32x32x16_bf16 v[96:111], v[68:71], v[152:155], v[96:111]\n"
        "ds_read_b64_tr_b16 v[56:57], v39 offset:32\n"
        "ds_read_b64_tr_b16 v[58:59], v39 offset:800\n"
        "v_lshlrev_b32 v26, 4, v26\n"
        "v_add_u32 v16, s57, v26\n"
        "s_waitcnt vmcnt(3) lgkmcnt(0)\n"
        "s_barrier\n"
        "v_mfma_f32_32x32x16_bf16 v[80:95], v[76:79], v[156:159], v[80:95]\n"
        "v_add_u32 v36, 0xfffe4000, v36\n"
        "v_add_u32 v37, 0xfffe4000, v37\n"
        "v_add_u32 v38, 0xfffe4000, v38\n"
        "v_add_u32 v39, 0xfffe4000, v39\n"
        "ds_read_b64_tr_b16 v[64:65], v36 offset:0\n"
        "ds_read_b64_tr_b16 v[66:67], v36 offset:768\n"
        "s_add_u32 m0, s51, 86016\n"
        "s_nop 0\n"
        "buffer_load_dwordx4 v32, s[68:71], s42 offen lds\n"
        "v_mfma_f32_32x32x16_bf16 v[96:111], v[56:59], v[156:159], v[96:111]\n"
        "ds_read_b64_tr_b16 v[68:69], v36 offset:32\n"
        "ds_read_b64_tr_b16 v[70:71], v36 offset:800\n"
        "v_add_u32 v26, s56, v26\n"
        "s_lshl_b32 s56, s38, 5\n"
        "s_add_u32 s39, s39, s56\n"
        "v_and_b32 v17, 1, v11\n"
        "v_lshl_add_u32 v17, v17, 4, s39\n"
        "s_cmp_lg_u32 s20, 0\n"
        "s_cbranch_scc1 .Lnf2_%=\n"
        "s_mov_b64 exec, 1\n"
        "v_mov_b32 v28, s30\n"
        "v_mov_b32 v29, s31\n"
        "v_mov_b32 v30, 0\n"
        "global_store_dwordx2 v30, v[28:29], s[54:55] sc1\n"
        "s_mov_b64 exec, -1\n"
        ".Lnf2_%=:\n"
        "s_waitcnt lgkmcnt(2)\n"
        "v_mfma_f32_32x32x16_bf16 v[80:95], v[64:67], v[160:163], v[80:95]\n"
        "ds_read_b64_tr_b16 v[76:77], v37 offset:0\n"
        "ds_read_b64_tr_b16 v[78:79], v37 offset:768\n"
        "s_waitcnt lgkmcnt(2)\n"
        "v_mfma_f32_32x32x16_bf16 v[96:111], v[68:71], v[160:163], v[96:111]\n"
        "ds_read_b64_tr_b16 v[56:57], v37 offset:32\n"
        "ds_read_b64_tr_b16 v[58:59], v37 offset:800\n"
        "v_mfma_f32_4x4x4_16b_bf16 v[44:47], v[40:41], v[64:65], v[44:47]\n"
        "s_add_u32 m0, s52, 86016\n"
        "s_nop 0\n"
        "buffer_load_dwordx4 v33, s[72:75], s42 offen lds\n"
        "v_mfma_f32_4x4x4_16b_bf16 v[48:51], v[40:41], v[68:69], v[48:51]\n"
        "s_mov_b32 s56, 0xffff0000\n"
        "s_mov_b32 s57, 0\n"
        "v_mfma_f32_4x4x4_16b_bf16 v[44:47], v[40:41], v[66:67], v[44:47]\n"
        "s_mov_b32 s58, 0\n"
        "s_mov_b32 s59, -1\n"
        "v_mfma_f32_4x4x4_16b_bf16 v[48:51], v[40:41], v[70:71], v[48:51]\n"
        "v_cndmask_b32 v26, v26, v16, s[56:57]\n"
        "s_waitcnt lgkmcnt(2)\n"
        "v_mfma_f32_32x32x16_bf16 v[80:95], v[76:79], v[164:167], v[80:95]\n"
        "ds_read_b64_tr_b16 v[64:65], v38 offset:0\n"
        "ds_read_b64_tr_b16 v[66:67], v38 offset:768\n"
        "s_add_u32 m0, s51, 102432\n"
        "s_nop 0\n"
        "buffer_load_dwordx4 v34, s[76:79], s42 offen lds\n"
        "s_add_u32 s42, s42, 176128\n"
        "s_cmp_eq_u32 s43, 5\n"
        "s_cselect_b32 s42, s47, s42\n"
        "s_waitcnt lgkmcnt(2)\n"
        "v_mfma_f32_32x32x16_bf16 v[96:111], v[56:59], v[164:167], v[96:111]\n"
        "ds_read_b64_tr_b16 v[68:69], v38 offset:32\n"
        "ds_read_b64_tr_b16 v[70:71], v38 offset:800\n"
        "v_cndmask_b32 v26, v26, v17, s[58:59]\n"
        "v_lshlrev_b32 v26, 3, v26\n"
        "v_add_u32 v26, 4096, v26\n"
        "s_lshl_b32 s39, s37, 6\n"
        "s_lshl_b32 s56, s38, 3\n"
        "s_waitcnt lgkmcnt(2)\n"
        "v_mfma_f32_32x32x16_bf16 v[80:95], v[64:67], v[168:171], v[80:95]\n"
        "ds_read_b64_tr_b16 v[76:77], v39 offset:0\n"
        "ds_read_b64_tr_b16 v[78:79], v39 offset:768\n"
        "s_add_u32 s39, s39, s56\n"
        "s_lshl_b32 s56, s21, 1\n"
        "s_add_u32 s39, s39, s56\n"
        "s_waitcnt lgkmcnt(2)\n"
        "v_mfma_f32_32x32x16_bf16 v[96:111], v[68:71], v[168:171], v[96:111]\n"
        "ds_read_b64_tr_b16 v[56:57], v39 offset:32\n"
        "ds_read_b64_tr_b16 v[58:59], v39 offset:800\n"
        "s_lshl_b32 s39, s39, 10\n"
        "s_add_u32 s90, s39, 65536\n"
        "s_waitcnt vmcnt(8) lgkmcnt(0)\n"
        "s_barrier\n"
        "v_mfma_f32_32x32x16_bf16 v[80:95], v[76:79], v[172:175], v[80:95]\n"
        "ds_read_b64_tr_b16 v[64:65], v36 offset:28672\n"
        "ds_read_b64_tr_b16 v[66:67], v36 offset:29440\n"
        "s_add_u32 m0, s51, 114688\n"
        "s_nop 0\n"
        "buffer_load_dwordx4 v32, s[68:71], s42 offen lds\n"
        "s_lshl_b32 s39, s37, 17\n"
        "s_add_u32 s39, s39, 2162688\n"
        "v_mfma_f32_32x32x16_bf16 v[96:111], v[56:59], v[172:175], v[96:111]\n"
        "ds_read_b64_tr_b16 v[68:69], v36 offset:28704\n"
        "ds_read_b64_tr_b16 v[70:71], v36 offset:29472\n"
        "s_add_u32 s92, s18, s39\n"
        "s_addc_u32 s93, s19, 0\n"
        "s_and_b32 s93, s93, 0xffff\n"
        "s_waitcnt lgkmcnt(2)\n"
        "v_mfma_f32_32x32x16_bf16 v[80:95], v[64:67], v[176:179], v[80:95]\n"
        "ds_read_b64_tr_b16 v[76:77], v37 offset:28672\n"
        "ds_read_b64_tr_b16 v[78:79], v37 offset:29440\n"
        "s_waitcnt lgkmcnt(2)\n"
        "v_mfma_f32_32x32x16_bf16 v[96:111], v[68:71], v[176:179], v[96:111]\n"
        "ds_read_b64_tr_b16 v[56:57], v37 offset:28704\n"
        "ds_read_b64_tr_b16 v[58:59], v37 offset:29472\n"
        "v_mfma_f32_4x4x4_16b_bf16 v[44:47], v[40:41], v[64:65], v[44:47]\n"
        "s_add_u32 m0, s52, 114688\n"
        "s_nop 0\n"
        "buffer_load_dwordx4 v33, s[72:75], s42 offen lds\n"
        "v_mfma_f32_4x4x4_16b_bf16 v[48:51], v[40:41], v[68:69], v[48:51]\n"
        "s_mov_b32 s94, 0x20000\n"
        "s_mov_b32 s95, 0x20000\n"
        "v_mfma_f32_4x4x4_16b_bf16 v[44:47], v[40:41], v[66:67], v[44:47]\n"
        "s_lshl_b32 s88, s38, 14\n"
        "s_add_u32 s88, s88, s50\n"
        "v_mfma_f32_4x4x4_16b_bf16 v[48:51], v[40:41], v[70:71], v[48:51]\n"
        "s_add_u32 s87, s50, 32768\n"
        "s_waitcnt lgkmcnt(2)\n"
        "v_mfma_f32_32x32x16_bf16 v[80:95], v[76:79], v[180:183], v[80:95]\n"
        "ds_read_b64_tr_b16 v[64:65], v38 offset:28672\n"
        "ds_read_b64_tr_b16 v[66:67], v38 offset:29440\n"
        "s_add_u32 m0, s51, 131104\n"
        "s_nop 0\n"
        "buffer_load_dwordx4 v34, s[76:79], s42 offen lds\n"
        "s_add_u32 s42, s42, 176128\n"
        "s_cmp_eq_u32 s43, 6\n"
        "s_cselect_b32 s42, s47, s42\n"
        "s_waitcnt lgkmcnt(2)\n"
        "v_mfma_f32_32x32x16_bf16 v[96:111], v[56:59], v[180:183], v[96:111]\n"
        "ds_read_b64_tr_b16 v[68:69], v38 offset:28704\n"
        "ds_read_b64_tr_b16 v[70:71], v38 offset:29472\n"
        "s_xor_b32 s89, s88, 0x10000\n"
        "s_lshl_b32 s40, s23, 2\n"
        "s_lshr_b32 s39, s24, 3\n"
        "s_add_u32 s40, s40, s39\n"
        "s_and_b32 s41, s24, 7\n"
        "s_waitcnt lgkmcnt(2)\n"
        "v_mfma_f32_32x32x16_bf16 v[80:95], v[64:67], v[184:187], v[80:95]\n"
        "ds_read_b64_tr_b16 v[76:77], v39 offset:28672\n"
        "ds_read_b64_tr_b16 v[78:79], v39 offset:29440\n"
        "s_and_b32 s96, s20, 1\n"
        "s_lshr_b32 s48, s20, 1\n"
        "s_lshl_b32 s84, s20, 12\n"
        "s_waitcnt lgkmcnt(2)\n"
        "v_mfma_f32_32x32x16_bf16 v[96:111], v[68:71], v[184:187], v[96:111]\n"
        "ds_read_b64_tr_b16 v[56:57], v39 offset:28704\n"
        "ds_read_b64_tr_b16 v[58:59], v39 offset:29472\n"
        "v_lshrrev_b32 v16, 5, v11\n"
        "v_and_b32 v17, 15, v11\n"
        "s_waitcnt vmcnt(6) lgkmcnt(0)\n"
        "s_barrier\n"
        "v_mfma_f32_32x32x16_bf16 v[80:95], v[76:79], v[188:191], v[80:95]\n"
        "v_add_u32 v36, 0xe000, v36\n"
        "v_add_u32 v37, 0xe000, v37\n"
        "v_add_u32 v38, 0xe000, v38\n"
        "v_add_u32 v39, 0xe000, v39\n"
        "ds_read_b64_tr_b16 v[64:65], v36 offset:0\n"
        "ds_read_b64_tr_b16 v[66:67], v36 offset:768\n"
        "s_add_u32 m0, s51, 0\n"
        "s_nop 0\n"
        "buffer_load_dwordx4 v32, s[68:71], s42 offen lds\n"
        "v_mfma_f32_32x32x16_bf16 v[96:111], v[56:59], v[188:191], v[96:111]\n"
        "ds_read_b64_tr_b16 v[68:69], v36 offset:32\n"
        "ds_read_b64_tr_b16 v[70:71], v36 offset:800\n"
        "v_bfe_u32 v18, v11, 4, 1\n"
        "s_lshl_b32 s39, s22, 16\n"
        "s_add_u32 s39, s39, 32768\n"
        "v_add_u32 v8, s39, v15\n"
        "v_lshlrev_b32 v9, 4, v17\n"
        "v_mbcnt_lo_u32_b32 v27, -1, 0\n"
        "v_mbcnt_hi_u32_b32 v27, -1, v27\n"
        "v_lshlrev_b32 v27, 7, v27\n"
        "s_lshl_b32 s98, s24, 14\n"
        "s_lshl_b32 s99, s20, 11\n"
        "s_add_u32 s98, s98, s99\n"
        "v_add_u32 v27, s98, v27\n"
        "s_mov_b64 s[100:101], s[8:9]\n"
        "s_mov_b32 exec_lo, 0xffff\n"
        "s_mov_b32 exec_hi, 0x0\n"
        "global_load_dword v31, v27, s[100:101]\n"
        "s_mov_b64 exec, -1\n"
        "s_waitcnt lgkmcnt(2)\n"
        "v_mfma_f32_32x32x16_bf16 v[80:95], v[64:67], v[192:195], v[80:95]\n"
        "ds_read_b64_tr_b16 v[76:77], v37 offset:0\n"
        "ds_read_b64_tr_b16 v[78:79], v37 offset:768\n"
        "s_waitcnt lgkmcnt(2)\n"
        "v_mfma_f32_32x32x16_bf16 v[96:111], v[68:71], v[192:195], v[96:111]\n"
        "ds_read_b64_tr_b16 v[56:57], v37 offset:32\n"
        "ds_read_b64_tr_b16 v[58:59], v37 offset:800\n"
        "v_mfma_f32_4x4x4_16b_bf16 v[44:47], v[40:41], v[64:65], v[44:47]\n"
        "s_add_u32 m0, s52, 0\n"
        "s_nop 0\n"
        "buffer_load_dwordx4 v33, s[72:75], s42 offen lds\n"
        "v_mfma_f32_4x4x4_16b_bf16 v[48:51], v[40:41], v[68:69], v[48:51]\n"
        "v_lshl_or_b32 v9, v16, 8, v9\n"
        "s_add_u32 s39, s39, 1024\n"
        "v_mfma_f32_4x4x4_16b_bf16 v[44:47], v[40:41], v[66:67], v[44:47]\n"
        "v_add_u32 v9, s39, v9\n"
        "s_lshr_b32 s39, s40, 1\n"
        "v_mfma_f32_4x4x4_16b_bf16 v[48:51], v[40:41], v[70:71], v[48:51]\n"
        "s_lshl_b32 s39, s39, 7\n"
        "s_waitcnt lgkmcnt(2)\n"
        "v_mfma_f32_32x32x16_bf16 v[80:95], v[76:79], v[196:199], v[80:95]\n"
        "ds_read_b64_tr_b16 v[64:65], v38 offset:0\n"
        "ds_read_b64_tr_b16 v[66:67], v38 offset:768\n"
        "s_add_u32 m0, s51, 16416\n"
        "s_nop 0\n"
        "buffer_load_dwordx4 v34, s[76:79], s42 offen lds\n"
        "s_add_u32 s42, s42, 176128\n"
        "s_and_b32 s56, s40, 1\n"
        "s_lshl_b32 s56, s56, 1\n"
        "s_waitcnt lgkmcnt(2)\n"
        "v_mfma_f32_32x32x16_bf16 v[96:111], v[56:59], v[196:199], v[96:111]\n"
        "ds_read_b64_tr_b16 v[68:69], v38 offset:32\n"
        "ds_read_b64_tr_b16 v[70:71], v38 offset:800\n"
        "s_add_u32 s56, s56, s96\n"
        "s_add_u32 s39, s39, s56\n"
        "v_lshlrev_b32 v14, 3, v17\n"
        "v_mov_b64 v[160:161], 0\n"
        "v_mov_b64 v[162:163], 0\n"
        "s_waitcnt lgkmcnt(2)\n"
        "v_mfma_f32_32x32x16_bf16 v[80:95], v[64:67], v[200:203], v[80:95]\n"
        "ds_read_b64_tr_b16 v[76:77], v39 offset:0\n"
        "ds_read_b64_tr_b16 v[78:79], v39 offset:768\n"
        "v_lshl_add_u32 v14, v18, 2, v14\n"
        "v_add_u32 v14, s39, v14\n"
        "s_lshr_b32 s39, s40, 3\n"
        "s_lshl_b32 s56, s48, 2\n"
        "s_waitcnt lgkmcnt(2)\n"
        "v_mfma_f32_32x32x16_bf16 v[96:111], v[68:71], v[200:203], v[96:111]\n"
        "ds_read_b64_tr_b16 v[56:57], v39 offset:32\n"
        "ds_read_b64_tr_b16 v[58:59], v39 offset:800\n"
        "s_add_u32 s39, s39, s56\n"
        "v_mov_b64 v[164:165], 0\n"
        "v_mov_b64 v[166:167], 0\n"
        "s_waitcnt vmcnt(7) lgkmcnt(0)\n"
        "s_barrier\n"
        "v_mfma_f32_32x32x16_bf16 v[80:95], v[76:79], v[204:207], v[80:95]\n"
        "ds_read_b64_tr_b16 v[64:65], v36 offset:28672\n"
        "ds_read_b64_tr_b16 v[66:67], v36 offset:29440\n"
        "s_lshl_b32 s39, s39, 4\n"
        "s_and_b32 s56, s40, 4\n"
        "s_add_u32 s39, s39, s56\n"
        "s_lshl_b32 s39, s39, 3\n"
        "v_mfma_f32_32x32x16_bf16 v[96:111], v[56:59], v[204:207], v[96:111]\n"
        "ds_read_b64_tr_b16 v[68:69], v36 offset:28704\n"
        "ds_read_b64_tr_b16 v[70:71], v36 offset:29472\n"
        "s_lshl_b32 s56, s96, 2\n"
        "v_mov_b64 v[168:169], 0\n"
        "v_mov_b64 v[170:171], 0\n"
        "s_waitcnt lgkmcnt(2)\n"
        "v_mfma_f32_32x32x16_bf16 v[80:95], v[64:67], v[208:211], v[80:95]\n"
        "ds_read_b64_tr_b16 v[76:77], v37 offset:28672\n"
        "ds_read_b64_tr_b16 v[78:79], v37 offset:29440\n"
        "s_waitcnt lgkmcnt(2)\n"
        "v_mfma_f32_32x32x16_bf16 v[96:111], v[68:71], v[208:211], v[96:111]\n"
        "ds_read_b64_tr_b16 v[56:57], v37 offset:28704\n"
        "ds_read_b64_tr_b16 v[58:59], v37 offset:29472\n"
        "v_mfma_f32_4x4x4_16b_bf16 v[44:47], v[40:41], v[64:65], v[44:47]\n"
        "s_add_u32 s39, s39, s56\n"
        "s_and_b32 s56, s40, 3\n"
        "v_mfma_f32_4x4x4_16b_bf16 v[48:51], v[40:41], v[68:69], v[48:51]\n"
        "s_add_u32 s39, s39, s56\n"
        "v_and_b32 v19, 3, v11\n"
        "v_mfma_f32_4x4x4_16b_bf16 v[44:47], v[40:41], v[66:67], v[44:47]\n"
        "v_bfe_u32 v20, v11, 2, 1\n"
        "v_mov_b64 v[172:173], 0\n"
        "v_mfma_f32_4x4x4_16b_bf16 v[48:51], v[40:41], v[70:71], v[48:51]\n"
        "v_mov_b64 v[174:175], 0\n"
        "s_cmp_lg_u32 s20, 0\n"
        "s_cbranch_scc1 .Lnp2_%=\n"
        "global_load_dwordx2 v[24:25], v26, s[18:19] sc1\n"
        ".Lnp2_%=:\n"
        "s_waitcnt lgkmcnt(2)\n"
        "v_mfma_f32_32x32x16_bf16 v[80:95], v[76:79], v[212:215], v[80:95]\n"
        "ds_read_b64_tr_b16 v[64:65], v38 offset:28672\n"
        "ds_read_b64_tr_b16 v[66:67], v38 offset:29440\n"
        "v_lshl_add_u32 v19, v20, 3, v19\n"
        "v_lshl_add_u32 v19, v19, 3, s39\n"
        "s_mov_b32 s56, 0\n"
        "s_mov_b32 s57, -1\n"
        "s_waitcnt lgkmcnt(2)\n"
        "v_mfma_f32_32x32x16_bf16 v[96:111], v[56:59], v[212:215], v[96:111]\n"
        "ds_read_b64_tr_b16 v[68:69], v38 offset:28704\n"
        "ds_read_b64_tr_b16 v[70:71], v38 offset:29472\n"
        "v_cndmask_b32 v14, v14, v19, s[56:57]\n"
        "v_mov_b64 v[176:177], 0\n"
        "v_mov_b64 v[178:179], 0\n"
        "s_waitcnt lgkmcnt(2)\n"
        "v_mfma_f32_32x32x16_bf16 v[80:95], v[64:67], v[216:219], v[80:95]\n"
        "ds_read_b64_tr_b16 v[76:77], v39 offset:28672\n"
        "ds_read_b64_tr_b16 v[78:79], v39 offset:29440\n"
        "v_lshlrev_b32 v14, 3, v14\n"
        "v_add_u32 v14, 16384, v14\n"
        "v_and_b32 v19, 3, v11\n"
        "v_lshlrev_b32 v19, 3, v19\n"
        "s_waitcnt lgkmcnt(2)\n"
        "v_mfma_f32_32x32x16_bf16 v[96:111], v[68:71], v[216:219], v[96:111]\n"
        "ds_read_b64_tr_b16 v[56:57], v39 offset:28704\n"
        "ds_read_b64_tr_b16 v[58:59], v39 offset:29472\n"
        "v_lshl_or_b32 v19, v18, 5, v19\n"
        "v_mov_b64 v[180:181], 0\n"
        "v_mov_b64 v[182:183], 0\n"
        "s_cmp_lg_u32 s20, 0\n"
        "s_cbranch_scc1 .Lf1ok2_%=\n"
        "s_waitcnt vmcnt(0)\n"
        "v_cmp_ne_u64 vcc, s[30:31], v[24:25]\n"
        "s_cbranch_vccz .Lf1ok2_%=\n"
        "s_mov_b32 s81, 0x20000\n"
        ".Lpf12_%=:\n"
        "global_load_dwordx2 v[24:25], v26, s[18:19] sc1\n"
        "s_waitcnt vmcnt(0)\n"
        "v_cmp_ne_u64 vcc, s[30:31], v[24:25]\n"
        "s_cbranch_vccz .Lqf12_%=\n"
        "s_sleep 6\n"
        "s_sub_u32 s81, s81, 1\n"
        "s_cbranch_scc0 .Lpf12_%=\n"
        ".Lqf12_%=:\n"
        ".Lf1ok2_%=:\n"
        "s_waitcnt vmcnt(4) lgkmcnt(0)\n"
        "s_barrier\n"
        "v_mfma_f32_32x32x16_bf16 v[80:95], v[76:79], v[220:223], v[80:95]\n"
        "v_add_u32 v36, 0xe000, v36\n"
        "v_add_u32 v37, 0xe000, v37\n"
        "v_add_u32 v38, 0xe000, v38\n"
        "v_add_u32 v39, 0xe000, v39\n"
        "ds_read_b64_tr_b16 v[64:65], v36 offset:0\n"
        "ds_read_b64_tr_b16 v[66:67], v36 offset:768\n"
        "buffer_load_dwordx4 v[0:3], v15, s[32:35], s90 offen sc1\n"
        "buffer_load_dwordx4 v[4:7], v15, s[32:35], s90 offen offset:1024 sc1\n"
        "v_add_u32 v19, s84, v19\n"
        "v_mfma_f32_32x32x16_bf16 v[96:111], v[56:59], v[220:223], v[96:111]\n"
        "ds_read_b64_tr_b16 v[68:69], v36 offset:32\n"
        "ds_read_b64_tr_b16 v[70:71], v36 offset:800\n"
        "v_bfe_u32 v17, v11, 2, 2\n"
        "v_lshl_add_u32 v13, v17, 10, v19\n"
        "v_lshl_add_u32 v13, v16, 7, v13\n"
        "v_bfe_u32 v17, v11, 2, 1\n"
        "v_mov_b64 v[184:185], 0\n"
        "v_mov_b64 v[186:187], 0\n"
        "s_waitcnt lgkmcnt(2)\n"
        "v_mfma_f32_32x32x16_bf16 v[80:95], v[64:67], v[224:227], v[80:95]\n"
        "ds_read_b64_tr_b16 v[76:77], v37 offset:0\n"
        "ds_read_b64_tr_b16 v[78:79], v37 offset:768\n"
        "s_mov_b32 m0, s87\n"
        "s_waitcnt lgkmcnt(2)\n"
        "v_mfma_f32_32x32x16_bf16 v[96:111], v[68:71], v[224:227], v[96:111]\n"
        "ds_read_b64_tr_b16 v[56:57], v37 offset:32\n"
        "ds_read_b64_tr_b16 v[58:59], v37 offset:800\n"
        "v_mfma_f32_4x4x4_16b_bf16 v[44:47], v[40:41], v[64:65], v[44:47]\n"
        "buffer_load_dwordx4 v15, s[92:95], s88 offen sc1 lds\n"
        "buffer_load_dwordx4 v15, s[92:95], s88 offen offset:1024 sc1 lds\n"
        "v_mfma_f32_4x4x4_16b_bf16 v[48:51], v[40:41], v[68:69], v[48:51]\n"
        "v_lshl_add_u32 v12, v17, 10, v19\n"
        "v_bfe_u32 v17, v11, 3, 1\n"
        "v_lshl_add_u32 v17, v16, 1, v17\n"
        "v_mfma_f32_4x4x4_16b_bf16 v[44:47], v[40:41], v[66:67], v[44:47]\n"
        "v_lshl_add_u32 v12, v17, 7, v12\n"
        "v_and_b32 v20, 31, v11\n"
        "v_mfma_f32_4x4x4_16b_bf16 v[48:51], v[40:41], v[70:71], v[48:51]\n"
        "v_mov_b64 v[188:189], 0\n"
        "v_mov_b64 v[190:191], 0\n"
        "s_waitcnt lgkmcnt(2)\n"
        "v_mfma_f32_32x32x16_bf16 v[80:95], v[76:79], v[228:231], v[80:95]\n"
        "ds_read_b64_tr_b16 v[64:65], v38 offset:0\n"
        "ds_read_b64_tr_b16 v[66:67], v38 offset:768\n"
        "s_add_u32 m0, s87, 0x10000\n"
        "s_nop 0\n"
        "buffer_load_dwordx4 v15, s[92:95], s89 offen sc1 lds\n"
        "s_lshl_b32 s39, s21, 9\n"
        "s_add_u32 s39, s39, 153600\n"
        "s_waitcnt lgkmcnt(2)\n"
        "v_mfma_f32_32x32x16_bf16 v[96:111], v[56:59], v[228:231], v[96:111]\n"
        "ds_read_b64_tr_b16 v[68:69], v38 offset:32\n"
        "ds_read_b64_tr_b16 v[70:71], v38 offset:800\n"
        "v_lshl_add_u32 v20, v20, 4, s39\n"
        "s_mul_i32 s39, s22, 3072\n"
        "s_add_u32 s39, s39, 156160\n"
        "s_waitcnt lgkmcnt(2)\n"
        "v_mfma_f32_32x32x16_bf16 v[80:95], v[64:67], v[232:235], v[80:95]\n"
        "ds_read_b64_tr_b16 v[76:77], v39 offset:0\n"
        "ds_read_b64_tr_b16 v[78:79], v39 offset:768\n"
        "buffer_load_dwordx4 v15, s[92:95], s89 offen offset:1024 sc1 lds\n"
        "v_add_u32 v21, s39, v15\n"
        "s_lshl_b32 s39, s21, 2\n"
        "s_waitcnt lgkmcnt(2)\n"
        "v_mfma_f32_32x32x16_bf16 v[96:111], v[68:71], v[232:235], v[96:111]\n"
        "ds_read_b64_tr_b16 v[56:57], v39 offset:32\n"
        "ds_read_b64_tr_b16 v[58:59], v39 offset:800\n"
        "v_add_u32 v22, s39, v21\n"
        "v_and_b32 v17, 27, v11\n"
        "v_cmp_eq_u32 s[56:57], 16, v17\n"
        "s_waitcnt vmcnt(6) lgkmcnt(0)\n"
        "s_barrier\n"
        "v_mfma_f32_32x32x16_bf16 v[80:95], v[76:79], v[236:239], v[80:95]\n"
        "v_add_u32 v36, 0xfffe4000, v36\n"
        "v_add_u32 v37, 0xfffe4000, v37\n"
        "v_add_u32 v38, 0xfffe4000, v38\n"
        "v_add_u32 v39, 0xfffe4000, v39\n"
        "ds_read_b64_tr_b16 v[64:65], v36 offset:0\n"
        "ds_read_b64_tr_b16 v[66:67], v36 offset:768\n"
        "v_mov_b32 v23, 0x3f803f80\n"
        "v_mfma_f32_32x32x16_bf16 v[96:111], v[56:59], v[236:239], v[96:111]\n"
        "ds_read_b64_tr_b16 v[68:69], v36 offset:32\n"
        "ds_read_b64_tr_b16 v[70:71], v36 offset:800\n"
        "v_and_b32 v17, 0x70, v15\n"
        "v_lshrrev_b32 v16, 3, v11\n"
        "v_cndmask_b32 v23, 0, v23, s[56:57]\n"
        "v_lshl_or_b32 v10, v16, 12, v17\n"
        "s_waitcnt lgkmcnt(2)\n"
        "v_mfma_f32_32x32x16_bf16 v[80:95], v[64:67], v[240:243], v[80:95]\n"
        "ds_read_b64_tr_b16 v[76:77], v37 offset:0\n"
        "ds_read_b64_tr_b16 v[78:79], v37 offset:768\n"
        "s_waitcnt lgkmcnt(2)\n"
        "v_mfma_f32_32x32x16_bf16 v[96:111], v[68:71], v[240:243], v[96:111]\n"
        "ds_read_b64_tr_b16 v[56:57], v37 offset:32\n"
        "ds_read_b64_tr_b16 v[58:59], v37 offset:800\n"
        "v_mfma_f32_4x4x4_16b_bf16 v[44:47], v[40:41], v[64:65], v[44:47]\n"
        "v_mfma_f32_4x4x4_16b_bf16 v[48:51], v[40:41], v[68:69], v[48:51]\n"
        "s_nop 0\n"
        "v_mfma_f32_4x4x4_16b_bf16 v[44:47], v[40:41], v[66:67], v[44:47]\n"
        "v_lshl_or_b32 v11, v16, 11, v17\n"
        "v_mfma_f32_4x4x4_16b_bf16 v[48:51], v[40:41], v[70:71], v[48:51]\n"
        "s_waitcnt lgkmcnt(2)\n"
        "v_mfma_f32_32x32x16_bf16 v[80:95], v[76:79], v[244:247], v[80:95]\n"
        "ds_read_b64_tr_b16 v[64:65], v38 offset:0\n"
        "ds_read_b64_tr_b16 v[66:67], v38 offset:768\n"
        "v_mov_b64 v[24:25], 0\n"
        "v_mov_b64 v[26:27], 0\n"
        "s_waitcnt lgkmcnt(2)\n"
        "v_mfma_f32_32x32x16_bf16 v[96:111], v[56:59], v[244:247], v[96:111]\n"
        "ds_read_b64_tr_b16 v[68:69], v38 offset:32\n"
        "ds_read_b64_tr_b16 v[70:71], v38 offset:800\n"
        "v_mov_b64 v[28:29], 0\n"
        "v_mov_b64 v[30:31], 0\n"
        "s_waitcnt lgkmcnt(2)\n"
        "v_mfma_f32_32x32x16_bf16 v[80:95], v[64:67], v[248:251], v[80:95]\n"
        "ds_read_b64_tr_b16 v[76:77], v39 offset:0\n"
        "ds_read_b64_tr_b16 v[78:79], v39 offset:768\n"
        "ds_write_b32 v22, v44 offset:0\n"
        "s_waitcnt lgkmcnt(3)\n"
        "v_mfma_f32_32x32x16_bf16 v[96:111], v[68:71], v[248:251], v[96:111]\n"
        "ds_read_b64_tr_b16 v[56:57], v39 offset:32\n"
        "ds_read_b64_tr_b16 v[58:59], v39 offset:800\n"
        "ds_write_b32 v22, v48 offset:1024\n"
        "s_waitcnt lgkmcnt(4)\n"
        "v_mfma_f32_32x32x16_bf16 v[80:95], v[76:79], v[252:255], v[80:95]\n"
        "s_waitcnt lgkmcnt(1)\n"
        "v_mfma_f32_32x32x16_bf16 v[96:111], v[56:59], v[252:255], v[96:111]\n"
        "s_branch .LD_%=\n"
        ".LC3_%=:\n"
        "s_waitcnt vmcnt(8) lgkmcnt(0)\n"
        "s_barrier\n"
        "v_mfma_f32_32x32x16_bf16 v[80:95], v[76:79], v[140:143], v[80:95]\n"
        "v_add_u32 v36, 0xe000, v36\n"
        "v_add_u32 v37, 0xe000, v37\n"
        "v_add_u32 v38, 0xe000, v38\n"
        "v_add_u32 v39, 0xe000, v39\n"
        "ds_read_b64_tr_b16 v[64:65], v36 offset:0\n"
        "ds_read_b64_tr_b16 v[66:67], v36 offset:768\n"
        "v_mfma_f32_32x32x16_bf16 v[96:111], v[56:59], v[140:143], v[96:111]\n"
        "ds_read_b64_tr_b16 v[68:69], v36 offset:32\n"
        "ds_read_b64_tr_b16 v[70:71], v36 offset:800\n"
        "v_mfma_f32_32x32x16_bf16 v[112:127], v[60:63], v[140:143], v[112:127]\n"
        "ds_read_b64_tr_b16 v[72:73], v36 offset:64\n"
        "ds_read_b64_tr_b16 v[74:75], v36 offset:832\n"
        "v_mfma_f32_4x4x4_16b_bf16 v[44:47], v[40:41], v[76:77], 0\n"
        "v_mfma_f32_4x4x4_16b_bf16 v[48:51], v[40:41], v[56:57], 0\n"
        "v_mfma_f32_4x4x4_16b_bf16 v[52:55], v[40:41], v[60:61], 0\n"
        "v_mfma_f32_4x4x4_16b_bf16 v[44:47], v[40:41], v[78:79], v[44:47]\n"
        "v_mfma_f32_4x4x4_16b_bf16 v[48:51], v[40:41], v[58:59], v[48:51]\n"
        "s_add_u32 m0, s51, 57344\n"
        "v_mfma_f32_4x4x4_16b_bf16 v[52:55], v[40:41], v[62:63], v[52:55]\n"
        "buffer_load_dwordx4 v32, s[68:71], s42 offen lds\n"
        "s_waitcnt lgkmcnt(4)\n"
        "v_mfma_f32_32x32x16_bf16 v[80:95], v[64:67], v[144:147], v[80:95]\n"
        "ds_read_b64_tr_b16 v[76:77], v37 offset:0\n"
        "ds_read_b64_tr_b16 v[78:79], v37 offset:768\n"
        "s_waitcnt lgkmcnt(4)\n"
        "v_mfma_f32_32x32x16_bf16 v[96:111], v[68:71], v[144:147], v[96:111]\n"
        "ds_read_b64_tr_b16 v[56:57], v37 offset:32\n"
        "ds_read_b64_tr_b16 v[58:59], v37 offset:800\n"
        "s_waitcnt lgkmcnt(4)\n"
        "v_mfma_f32_32x32x16_bf16 v[112:127], v[72:75], v[144:147], v[112:127]\n"
        "ds_read_b64_tr_b16 v[60:61], v37 offset:64\n"
        "ds_read_b64_tr_b16 v[62:63], v37 offset:832\n"
        "v_mfma_f32_4x4x4_16b_bf16 v[44:47], v[40:41], v[64:65], v[44:47]\n"
        "v_mfma_f32_4x4x4_16b_bf16 v[48:51], v[40:41], v[68:69], v[48:51]\n"
        "s_add_u32 m0, s52, 57344\n"
        "s_nop 0\n"
        "buffer_load_dwordx4 v33, s[72:75], s42 offen lds\n"
        "v_mfma_f32_4x4x4_16b_bf16 v[52:55], v[40:41], v[72:73], v[52:55]\n"
        "v_mbcnt_lo_u32_b32 v11, -1, 0\n"
        "v_mfma_f32_4x4x4_16b_bf16 v[44:47], v[40:41], v[66:67], v[44:47]\n"
        "v_mbcnt_hi_u32_b32 v11, -1, v11\n"
        "v_lshlrev_b32 v15, 4, v11\n"
        "v_mfma_f32_4x4x4_16b_bf16 v[48:51], v[40:41], v[70:71], v[48:51]\n"
        "s_lshr_b32 s39, s37, 1\n"
        "v_mfma_f32_4x4x4_16b_bf16 v[52:55], v[40:41], v[74:75], v[52:55]\n"
        "s_add_u32 s56, s39, 16\n"
        "s_waitcnt lgkmcnt(4)\n"
        "v_mfma_f32_32x32x16_bf16 v[80:95], v[76:79], v[148:151], v[80:95]\n"
        "ds_read_b64_tr_b16 v[64:65], v38 offset:0\n"
        "ds_read_b64_tr_b16 v[66:67], v38 offset:768\n"
        "s_add_u32 m0, s51, 73760\n"
        "s_nop 0\n"
        "buffer_load_dwordx4 v34, s[76:79], s42 offen lds\n"
        "s_add_u32 s42, s42, 176128\n"
        "s_cmp_eq_u32 s43, 4\n"
        "s_waitcnt lgkmcnt(4)\n"
        "v_mfma_f32_32x32x16_bf16 v[96:111], v[56:59], v[148:151], v[96:111]\n"
        "ds_read_b64_tr_b16 v[68:69], v38 offset:32\n"
        "ds_read_b64_tr_b16 v[70:71], v38 offset:800\n"
        "s_cselect_b32 s42, s47, s42\n"
        "s_add_u32 s57, s39, 32\n"
        "s_mul_i32 s39, s39, 171\n"
        "s_waitcnt lgkmcnt(4)\n"
        "v_mfma_f32_32x32x16_bf16 v[112:127], v[60:63], v[148:151], v[112:127]\n"
        "ds_read_b64_tr_b16 v[72:73], v38 offset:64\n"
        "ds_read_b64_tr_b16 v[74:75], v38 offset:832\n"
        "s_mul_i32 s56, s56, 171\n"
        "s_mul_i32 s57, s57, 171\n"
        "s_lshr_b32 s39, s39, 9\n"
        "s_waitcnt lgkmcnt(4)\n"
        "v_mfma_f32_32x32x16_bf16 v[80:95], v[64:67], v[152:155], v[80:95]\n"
        "ds_read_b64_tr_b16 v[76:77], v39 offset:0\n"
        "ds_read_b64_tr_b16 v[78:79], v39 offset:768\n"
        "s_lshr_b32 s56, s56, 9\n"
        "s_lshr_b32 s57, s57, 9\n"
        "s_waitcnt lgkmcnt(4)\n"
        "v_mfma_f32_32x32x16_bf16 v[96:111], v[68:71], v[152:155], v[96:111]\n"
        "ds_read_b64_tr_b16 v[56:57], v39 offset:32\n"
        "ds_read_b64_tr_b16 v[58:59], v39 offset:800\n"
        "v_and_b32 v26, 15, v11\n"
        "v_lshlrev_b32 v26, 4, v26\n"
        "s_waitcnt lgkmcnt(4)\n"
        "v_mfma_f32_32x32x16_bf16 v[112:127], v[72:75], v[152:155], v[112:127]\n"
        "ds_read_b64_tr_b16 v[60:61], v39 offset:64\n"
        "ds_read_b64_tr_b16 v[62:63], v39 offset:832\n"
        "v_add_u32 v16, s57, v26\n"
        "s_waitcnt vmcnt(3) lgkmcnt(0)\n"
        "s_barrier\n"
        "v_mfma_f32_32x32x16_bf16 v[80:95], v[76:79], v[156:159], v[80:95]\n"
        "v_add_u32 v36, 0xfffe4000, v36\n"
        "v_add_u32 v37, 0xfffe4000, v37\n"
        "v_add_u32 v38, 0xfffe4000, v38\n"
        "v_add_u32 v39, 0xfffe4000, v39\n"
        "ds_read_b64_tr_b16 v[64:65], v36 offset:0\n"
        "ds_read_b64_tr_b16 v[66:67], v36 offset:768\n"
        "v_mfma_f32_32x32x16_bf16 v[96:111], v[56:59], v[156:159], v[96:111]\n"
        "ds_read_b64_tr_b16 v[68:69], v36 offset:32\n"
        "ds_read_b64_tr_b16 v[70:71], v36 offset:800\n"
        "s_add_u32 m0, s51, 86016\n"
        "s_nop 0\n"
        "buffer_load_dwordx4 v32, s[68:71], s42 offen lds\n"
        "v_add_u32 v26, s56, v26\n"
        "s_lshl_b32 s56, s38, 5\n"
        "v_mfma_f32_32x32x16_bf16 v[112:127], v[60:63], v[156:159], v[112:127]\n"
        "ds_read_b64_tr_b16 v[72:73], v36 offset:64\n"
        "ds_read_b64_tr_b16 v[74:75], v36 offset:832\n"
        "s_add_u32 s39, s39, s56\n"
        "v_and_b32 v17, 1, v11\n"
        "v_lshl_add_u32 v17, v17, 4, s39\n"
        "s_cmp_lg_u32 s20, 0\n"
        "s_cbranch_scc1 .Lnf3_%=\n"
        "s_mov_b64 exec, 1\n"
        "v_mov_b32 v28, s30\n"
        "v_mov_b32 v29, s31\n"
        "v_mov_b32 v30, 0\n"
        "global_store_dwordx2 v30, v[28:29], s[54:55] sc1\n"
        "s_mov_b64 exec, -1\n"
        ".Lnf3_%=:\n"
        "s_waitcnt lgkmcnt(4)\n"
        "v_mfma_f32_32x32x16_bf16 v[80:95], v[64:67], v[160:163], v[80:95]\n"
        "ds_read_b64_tr_b16 v[76:77], v37 offset:0\n"
        "ds_read_b64_tr_b16 v[78:79], v37 offset:768\n"
        "s_waitcnt lgkmcnt(4)\n"
        "v_mfma_f32_32x32x16_bf16 v[96:111], v[68:71], v[160:163], v[96:111]\n"
        "ds_read_b64_tr_b16 v[56:57], v37 offset:32\n"
        "ds_read_b64_tr_b16 v[58:59], v37 offset:800\n"
        "s_waitcnt lgkmcnt(4)\n"
        "v_mfma_f32_32x32x16_bf16 v[112:127], v[72:75], v[160:163], v[112:127]\n"
        "ds_read_b64_tr_b16 v[60:61], v37 offset:64\n"
        "ds_read_b64_tr_b16 v[62:63], v37 offset:832\n"
        "v_mfma_f32_4x4x4_16b_bf16 v[44:47], v[40:41], v[64:65], v[44:47]\n"
        "v_mfma_f32_4x4x4_16b_bf16 v[48:51], v[40:41], v[68:69], v[48:51]\n"
        "s_add_u32 m0, s52, 86016\n"
        "s_nop 0\n"
        "buffer_load_dwordx4 v33, s[72:75], s42 offen lds\n"
        "v_mfma_f32_4x4x4_16b_bf16 v[52:55], v[40:41], v[72:73], v[52:55]\n"
        "s_mov_b32 s56, 0xffff0000\n"
        "v_mfma_f32_4x4x4_16b_bf16 v[44:47], v[40:41], v[66:67], v[44:47]\n"
        "s_mov_b32 s57, 0\n"
        "s_mov_b32 s58, 0\n"
        "v_mfma_f32_4x4x4_16b_bf16 v[48:51], v[40:41], v[70:71], v[48:51]\n"
        "s_mov_b32 s59, -1\n"
        "v_mfma_f32_4x4x4_16b_bf16 v[52:55], v[40:41], v[74:75], v[52:55]\n"
        "v_cndmask_b32 v26, v26, v16, s[56:57]\n"
        "s_waitcnt lgkmcnt(4)\n"
        "v_mfma_f32_32x32x16_bf16 v[80:95], v[76:79], v[164:167], v[80:95]\n"
        "ds_read_b64_tr_b16 v[64:65], v38 offset:0\n"
        "ds_read_b64_tr_b16 v[66:67], v38 offset:768\n"
        "s_add_u32 m0, s51, 102432\n"
        "s_nop 0\n"
        "buffer_load_dwordx4 v34, s[76:79], s42 offen lds\n"
        "s_add_u32 s42, s42, 176128\n"
        "s_cmp_eq_u32 s43, 5\n"
        "s_waitcnt lgkmcnt(4)\n"
        "v_mfma_f32_32x32x16_bf16 v[96:111], v[56:59], v[164:167], v[96:111]\n"
        "ds_read_b64_tr_b16 v[68:69], v38 offset:32\n"
        "ds_read_b64_tr_b16 v[70:71], v38 offset:800\n"
        "s_cselect_b32 s42, s47, s42\n"
        "v_cndmask_b32 v26, v26, v17, s[58:59]\n"
        "v_lshlrev_b32 v26, 3, v26\n"
        "s_waitcnt lgkmcnt(4)\n"
        "v_mfma_f32_32x32x16_bf16 v[112:127], v[60:63], v[164:167], v[112:127]\n"
        "ds_read_b64_tr_b16 v[72:73], v38 offset:64\n"
        "ds_read_b64_tr_b16 v[74:75], v38 offset:832\n"
        "v_add_u32 v26, 4096, v26\n"
        "s_lshl_b32 s39, s37, 6\n"
        "s_lshl_b32 s56, s38, 3\n"
        "s_waitcnt lgkmcnt(4)\n"
        "v_mfma_f32_32x32x16_bf16 v[80:95], v[64:67], v[168:171], v[80:95]\n"
        "ds_read_b64_tr_b16 v[76:77], v39 offset:0\n"
        "ds_read_b64_tr_b16 v[78:79], v39 offset:768\n"
        "s_add_u32 s39, s39, s56\n"
        "s_lshl_b32 s56, s21, 1\n"
        "s_waitcnt lgkmcnt(4)\n"
        "v_mfma_f32_32x32x16_bf16 v[96:111], v[68:71], v[168:171], v[96:111]\n"
        "ds_read_b64_tr_b16 v[56:57], v39 offset:32\n"
        "ds_read_b64_tr_b16 v[58:59], v39 offset:800\n"
        "s_add_u32 s39, s39, s56\n"
        "s_lshl_b32 s39, s39, 10\n"
        "s_waitcnt lgkmcnt(4)\n"
        "v_mfma_f32_32x32x16_bf16 v[112:127], v[72:75], v[168:171], v[112:127]\n"
        "ds_read_b64_tr_b16 v[60:61], v39 offset:64\n"
        "ds_read_b64_tr_b16 v[62:63], v39 offset:832\n"
        "s_add_u32 s90, s39, 65536\n"
        "s_waitcnt vmcnt(8) lgkmcnt(0)\n"
        "s_barrier\n"
        "v_mfma_f32_32x32x16_bf16 v[80:95], v[76:79], v[172:175], v[80:95]\n"
        "ds_read_b64_tr_b16 v[64:65], v36 offset:28672\n"
        "ds_read_b64_tr_b16 v[66:67], v36 offset:29440\n"
        "s_add_u32 m0, s51, 114688\n"
        "s_nop 0\n"
        "buffer_load_dwordx4 v32, s[68:71], s42 offen lds\n"
        "s_lshl_b32 s39, s37, 17\n"
        "v_mfma_f32_32x32x16_bf16 v[96:111], v[56:59], v[172:175], v[96:111]\n"
        "ds_read_b64_tr_b16 v[68:69], v36 offset:28704\n"
        "ds_read_b64_tr_b16 v[70:71], v36 offset:29472\n"
        "s_add_u32 s39, s39, 2162688\n"
        "s_add_u32 s92, s18, s39\n"
        "v_mfma_f32_32x32x16_bf16 v[112:127], v[60:63], v[172:175], v[112:127]\n"
        "ds_read_b64_tr_b16 v[72:73], v36 offset:28736\n"
        "ds_read_b64_tr_b16 v[74:75], v36 offset:29504\n"
        "s_addc_u32 s93, s19, 0\n"
        "s_and_b32 s93, s93, 0xffff\n"
        "s_waitcnt lgkmcnt(4)\n"
        "v_mfma_f32_32x32x16_bf16 v[80:95], v[64:67], v[176:179], v[80:95]\n"
        "ds_read_b64_tr_b16 v[76:77], v37 offset:28672\n"
        "ds_read_b64_tr_b16 v[78:79], v37 offset:29440\n"
        "s_waitcnt lgkmcnt(4)\n"
        "v_mfma_f32_32x32x16_bf16 v[96:111], v[68:71], v[176:179], v[96:111]\n"
        "ds_read_b64_tr_b16 v[56:57], v37 offset:28704\n"
        "ds_read_b64_tr_b16 v[58:59], v37 offset:29472\n"
        "s_waitcnt lgkmcnt(4)\n"
        "v_mfma_f32_32x32x16_bf16 v[112:127], v[72:75], v[176:179], v[112:127]\n"
        "ds_read_b64_tr_b16 v[60:61], v37 offset:28736\n"
        "ds_read_b64_tr_b16 v[62:63], v37 offset:29504\n"
        "v_mfma_f32_4x4x4_16b_bf16 v[44:47], v[40:41], v[64:65], v[44:47]\n"
        "v_mfma_f32_4x4x4_16b_bf16 v[48:51], v[40:41], v[68:69], v[48:51]\n"
        "s_add_u32 m0, s52, 114688\n"
        "s_nop 0\n"
        "buffer_load_dwordx4 v33, s[72:75], s42 offen lds\n"
        "v_mfma_f32_4x4x4_16b_bf16 v[52:55], v[40:41], v[72:73], v[52:55]\n"
        "s_mov_b32 s94, 0x20000\n"
        "v_mfma_f32_4x4x4_16b_bf16 v[44:47], v[40:41], v[66:67], v[44:47]\n"
        "s_mov_b32 s95, 0x20000\n"
        "s_lshl_b32 s88, s38, 14\n"
        "v_mfma_f32_4x4x4_16b_bf16 v[48:51], v[40:41], v[70:71], v[48:51]\n"
        "s_add_u32 s88, s88, s50\n"
        "v_mfma_f32_4x4x4_16b_bf16 v[52:55], v[40:41], v[74:75], v[52:55]\n"
        "s_add_u32 s87, s50, 32768\n"
        "s_waitcnt lgkmcnt(4)\n"
        "v_mfma_f32_32x32x16_bf16 v[80:95], v[76:79], v[180:183], v[80:95]\n"
        "ds_read_b64_tr_b16 v[64:65], v38 offset:28672\n"
        "ds_read_b64_tr_b16 v[66:67], v38 offset:29440\n"
        "s_add_u32 m0, s51, 131104\n"
        "s_nop 0\n"
        "buffer_load_dwordx4 v34, s[76:79], s42 offen lds\n"
        "s_add_u32 s42, s42, 176128\n"
        "s_cmp_eq_u32 s43, 6\n"
        "s_waitcnt lgkmcnt(4)\n"
        "v_mfma_f32_32x32x16_bf16 v[96:111], v[56:59], v[180:183], v[96:111]\n"
        "ds_read_b64_tr_b16 v[68:69], v38 offset:28704\n"
        "ds_read_b64_tr_b16 v[70:71], v38 offset:29472\n"
        "s_cselect_b32 s42, s47, s42\n"
        "s_xor_b32 s89, s88, 0x10000\n"
        "s_lshl_b32 s40, s23, 2\n"
        "s_waitcnt lgkmcnt(4)\n"
        "v_mfma_f32_32x32x16_bf16 v[112:127], v[60:63], v[180:183], v[112:127]\n"
        "ds_read_b64_tr_b16 v[72:73], v38 offset:28736\n"
        "ds_read_b64_tr_b16 v[74:75], v38 offset:29504\n"
        "s_lshr_b32 s39, s24, 3\n"
        "s_add_u32 s40, s40, s39\n"
        "s_and_b32 s41, s24, 7\n"
        "s_waitcnt lgkmcnt(4)\n"
        "v_mfma_f32_32x32x16_bf16 v[80:95], v[64:67], v[184:187], v[80:95]\n"
        "ds_read_b64_tr_b16 v[76:77], v39 offset:28672\n"
        "ds_read_b64_tr_b16 v[78:79], v39 offset:29440\n"
        "s_and_b32 s96, s20, 1\n"
        "s_lshr_b32 s48, s20, 1\n"
        "s_waitcnt lgkmcnt(4)\n"
        "v_mfma_f32_32x32x16_bf16 v[96:111], v[68:71], v[184:187], v[96:111]\n"
        "ds_read_b64_tr_b16 v[56:57], v39 offset:28704\n"
        "ds_read_b64_tr_b16 v[58:59], v39 offset:29472\n"
        "s_lshl_b32 s84, s20, 12\n"
        "v_lshrrev_b32 v16, 5, v11\n"
        "s_waitcnt lgkmcnt(4)\n"
        "v_mfma_f32_32x32x16_bf16 v[112:127], v[72:75], v[184:187], v[112:127]\n"
        "ds_read_b64_tr_b16 v[60:61], v39 offset:28736\n"
        "ds_read_b64_tr_b16 v[62:63], v39 offset:29504\n"
        "v_and_b32 v17, 15, v11\n"
        "s_waitcnt vmcnt(6) lgkmcnt(0)\n"
        "s_barrier\n"
        "v_mfma_f32_32x32x16_bf16 v[80:95], v[76:79], v[188:191], v[80:95]\n"
        "v_add_u32 v36, 0xe000, v36\n"
        "v_add_u32 v37, 0xe000, v37\n"
        "v_add_u32 v38, 0xe000, v38\n"
        "v_add_u32 v39, 0xe000, v39\n"
        "ds_read_b64_tr_b16 v[64:65], v36 offset:0\n"
        "ds_read_b64_tr_b16 v[66:67], v36 offset:768\n"
        "v_mfma_f32_32x32x16_bf16 v[96:111], v[56:59], v[188:191], v[96:111]\n"
        "ds_read_b64_tr_b16 v[68:69], v36 offset:32\n"
        "ds_read_b64_tr_b16 v[70:71], v36 offset:800\n"
        "s_add_u32 m0, s51, 0\n"
        "s_nop 0\n"
        "buffer_load_dwordx4 v32, s[68:71], s42 offen lds\n"
        "v_bfe_u32 v18, v11, 4, 1\n"
        "s_lshl_b32 s39, s22, 16\n"
        "v_mfma_f32_32x32x16_bf16 v[112:127], v[60:63], v[188:191], v[112:127]\n"
        "ds_read_b64_tr_b16 v[72:73], v36 offset:64\n"
        "ds_read_b64_tr_b16 v[74:75], v36 offset:832\n"
        "s_add_u32 s39, s39, 32768\n"
        "v_add_u32 v8, s39, v15\n"
        "v_lshlrev_b32 v9, 4, v17\n"
        "v_mbcnt_lo_u32_b32 v27, -1, 0\n"
        "v_mbcnt_hi_u32_b32 v27, -1, v27\n"
        "v_lshlrev_b32 v27, 7, v27\n"
        "s_lshl_b32 s98, s24, 14\n"
        "s_lshl_b32 s99, s20, 11\n"
        "s_add_u32 s98, s98, s99\n"
        "v_add_u32 v27, s98, v27\n"
        "s_mov_b64 s[100:101], s[8:9]\n"
        "s_mov_b32 exec_lo, 0xffff\n"
        "s_mov_b32 exec_hi, 0x0\n"
        "global_load_dword v31, v27, s[100:101]\n"
        "s_mov_b64 exec, -1\n"
        "s_waitcnt lgkmcnt(4)\n"
        "v_mfma_f32_32x32x16_bf16 v[80:95], v[64:67], v[192:195], v[80:95]\n"
        "ds_read_b64_tr_b16 v[76:77], v37 offset:0\n"
        "ds_read_b64_tr_b16 v[78:79], v37 offset:768\n"
        "s_waitcnt lgkmcnt(4)\n"
        "v_mfma_f32_32x32x16_bf16 v[96:111], v[68:71], v[192:195], v[96:111]\n"
        "ds_read_b64_tr_b16 v[56:57], v37 offset:32\n"
        "ds_read_b64_tr_b16 v[58:59], v37 offset:800\n"
        "s_waitcnt lgkmcnt(4)\n"
        "v_mfma_f32_32x32x16_bf16 v[112:127], v[72:75], v[192:195], v[112:127]\n"
        "ds_read_b64_tr_b16 v[60:61], v37 offset:64\n"
        "ds_read_b64_tr_b16 v[62:63], v37 offset:832\n"
        "v_mfma_f32_4x4x4_16b_bf16 v[44:47], v[40:41], v[64:65], v[44:47]\n"
        "v_mfma_f32_4x4x4_16b_bf16 v[48:51], v[40:41], v[68:69], v[48:51]\n"
        "s_add_u32 m0, s52, 0\n"
        "s_nop 0\n"
        "buffer_load_dwordx4 v33, s[72:75], s42 offen lds\n"
        "v_mfma_f32_4x4x4_16b_bf16 v[52:55], v[40:41], v[72:73], v[52:55]\n"
        "v_lshl_or_b32 v9, v16, 8, v9\n"
        "v_mfma_f32_4x4x4_16b_bf16 v[44:47], v[40:41], v[66:67], v[44:47]\n"
        "s_add_u32 s39, s39, 1024\n"
        "v_add_u32 v9, s39, v9\n"
        "v_mfma_f32_4x4x4_16b_bf16 v[48:51], v[40:41], v[70:71], v[48:51]\n"
        "s_lshr_b32 s39, s40, 1\n"
        "v_mfma_f32_4x4x4_16b_bf16 v[52:55], v[40:41], v[74:75], v[52:55]\n"
        "s_lshl_b32 s39, s39, 7\n"
        "s_waitcnt lgkmcnt(4)\n"
        "v_mfma_f32_32x32x16_bf16 v[80:95], v[76:79], v[196:199], v[80:95]\n"
        "ds_read_b64_tr_b16 v[64:65], v38 offset:0\n"
        "ds_read_b64_tr_b16 v[66:67], v38 offset:768\n"
        "s_add_u32 m0, s51, 16416\n"
        "s_nop 0\n"
        "buffer_load_dwordx4 v34, s[76:79], s42 offen lds\n"
        "s_add_u32 s42, s42, 176128\n"
        "s_and_b32 s56, s40, 1\n"
        "s_waitcnt lgkmcnt(4)\n"
        "v_mfma_f32_32x32x16_bf16 v[96:111], v[56:59], v[196:199], v[96:111]\n"
        "ds_read_b64_tr_b16 v[68:69], v38 offset:32\n"
        "ds_read_b64_tr_b16 v[70:71], v38 offset:800\n"
        "s_lshl_b32 s56, s56, 1\n"
        "s_add_u32 s56, s56, s96\n"
        "s_add_u32 s39, s39, s56\n"
        "s_waitcnt lgkmcnt(4)\n"
        "v_mfma_f32_32x32x16_bf16 v[112:127], v[60:63], v[196:199], v[112:127]\n"
        "ds_read_b64_tr_b16 v[72:73], v38 offset:64\n"
        "ds_read_b64_tr_b16 v[74:75], v38 offset:832\n"
        "v_lshlrev_b32 v14, 3, v17\n"
        "v_mov_b64 v[160:161], 0\n"
        "v_mov_b64 v[162:163], 0\n"
        "s_waitcnt lgkmcnt(4)\n"
        "v_mfma_f32_32x32x16_bf16 v[80:95], v[64:67], v[200:203], v[80:95]\n"
        "ds_read_b64_tr_b16 v[76:77], v39 offset:0\n"
        "ds_read_b64_tr_b16 v[78:79], v39 offset:768\n"
        "v_lshl_add_u32 v14, v18, 2, v14\n"
        "v_add_u32 v14, s39, v14\n"
        "s_lshr_b32 s39, s40, 3\n"
        "s_waitcnt lgkmcnt(4)\n"
        "v_mfma_f32_32x32x16_bf16 v[96:111], v[68:71], v[200:203], v[96:111]\n"
        "ds_read_b64_tr_b16 v[56:57], v39 offset:32\n"
        "ds_read_b64_tr_b16 v[58:59], v39 offset:800\n"
        "s_lshl_b32 s56, s48, 2\n"
        "s_add_u32 s39, s39, s56\n"
        "s_waitcnt lgkmcnt(4)\n"
        "v_mfma_f32_32x32x16_bf16 v[112:127], v[72:75], v[200:203], v[112:127]\n"
        "ds_read_b64_tr_b16 v[60:61], v39 offset:64\n"
        "ds_read_b64_tr_b16 v[62:63], v39 offset:832\n"
        "v_mov_b64 v[164:165], 0\n"
        "v_mov_b64 v[166:167], 0\n"
        "s_waitcnt vmcnt(7) lgkmcnt(0)\n"
        "s_barrier\n"
        "v_mfma_f32_32x32x16_bf16 v[80:95], v[76:79], v[204:207], v[80:95]\n"
        "ds_read_b64_tr_b16 v[64:65], v36 offset:28672\n"
        "ds_read_b64_tr_b16 v[66:67], v36 offset:29440\n"
        "s_lshl_b32 s39, s39, 4\n"
        "s_and_b32 s56, s40, 4\n"
        "s_add_u32 s39, s39, s56\n"
        "v_mfma_f32_32x32x16_bf16 v[96:111], v[56:59], v[204:207], v[96:111]\n"
        "ds_read_b64_tr_b16 v[68:69], v36 offset:28704\n"
        "ds_read_b64_tr_b16 v[70:71], v36 offset:29472\n"
        "s_lshl_b32 s39, s39, 3\n"
        "s_lshl_b32 s56, s96, 2\n"
        "v_mfma_f32_32x32x16_bf16 v[112:127], v[60:63], v[204:207], v[112:127]\n"
        "ds_read_b64_tr_b16 v[72:73], v36 offset:28736\n"
        "ds_read_b64_tr_b16 v[74:75], v36 offset:29504\n"
        "v_mov_b64 v[168:169], 0\n"
        "v_mov_b64 v[170:171], 0\n"
        "s_waitcnt lgkmcnt(4)\n"
        "v_mfma_f32_32x32x16_bf16 v[80:95], v[64:67], v[208:211], v[80:95]\n"
        "ds_read_b64_tr_b16 v[76:77], v37 offset:28672\n"
        "ds_read_b64_tr_b16 v[78:79], v37 offset:29440\n"
        "s_waitcnt lgkmcnt(4)\n"
        "v_mfma_f32_32x32x16_bf16 v[96:111], v[68:71], v[208:211], v[96:111]\n"
        "ds_read_b64_tr_b16 v[56:57], v37 offset:28704\n"
        "ds_read_b64_tr_b16 v[58:59], v37 offset:29472\n"
        "s_waitcnt lgkmcnt(4)\n"
        "v_mfma_f32_32x32x16_bf16 v[112:127], v[72:75], v[208:211], v[112:127]\n"
        "ds_read_b64_tr_b16 v[60:61], v37 offset:28736\n"
        "ds_read_b64_tr_b16 v[62:63], v37 offset:29504\n"
        "v_mfma_f32_4x4x4_16b_bf16 v[44:47], v[40:41], v[64:65], v[44:47]\n"
        "v_mfma_f32_4x4x4_16b_bf16 v[48:51], v[40:41], v[68:69], v[48:51]\n"
        "s_add_u32 s39, s39, s56\n"
        "s_and_b32 s56, s40, 3\n"
        "v_mfma_f32_4x4x4_16b_bf16 v[52:55], v[40:41], v[72:73], v[52:55]\n"
        "s_add_u32 s39, s39, s56\n"
        "v_mfma_f32_4x4x4_16b_bf16 v[44:47], v[40:41], v[66:67], v[44:47]\n"
        "v_and_b32 v19, 3, v11\n"
        "v_bfe_u32 v20, v11, 2, 1\n"
        "v_mfma_f32_4x4x4_16b_bf16 v[48:51], v[40:41], v[70:71], v[48:51]\n"
        "v_mov_b64 v[172:173], 0\n"
        "v_mfma_f32_4x4x4_16b_bf16 v[52:55], v[40:41], v[74:75], v[52:55]\n"
        "v_mov_b64 v[174:175], 0\n"
        "s_cmp_lg_u32 s20, 0\n"
        "s_cbranch_scc1 .Lnp3_%=\n"
        "global_load_dwordx2 v[24:25], v26, s[18:19] sc1\n"
        ".Lnp3_%=:\n"
        "s_waitcnt lgkmcnt(4)\n"
        "v_mfma_f32_32x32x16_bf16 v[80:95], v[76:79], v[212:215], v[80:95]\n"
        "ds_read_b64_tr_b16 v[64:65], v38 offset:28672\n"
        "ds_read_b64_tr_b16 v[66:67], v38 offset:29440\n"
        "v_lshl_add_u32 v19, v20, 3, v19\n"
        "v_lshl_add_u32 v19, v19, 3, s39\n"
        "s_mov_b32 s56, 0\n"
        "s_waitcnt lgkmcnt(4)\n"
        "v_mfma_f32_32x32x16_bf16 v[96:111], v[56:59], v[212:215], v[96:111]\n"
        "ds_read_b64_tr_b16 v[68:69], v38 offset:28704\n"
        "ds_read_b64_tr_b16 v[70:71], v38 offset:29472\n"
        "s_mov_b32 s57, -1\n"
        "v_cndmask_b32 v14, v14, v19, s[56:57]\n"
        "s_waitcnt lgkmcnt(4)\n"
        "v_mfma_f32_32x32x16_bf16 v[112:127], v[60:63], v[212:215], v[112:127]\n"
        "ds_read_b64_tr_b16 v[72:73], v38 offset:28736\n"
        "ds_read_b64_tr_b16 v[74:75], v38 offset:29504\n"
        "v_mov_b64 v[176:177], 0\n"
        "v_mov_b64 v[178:179], 0\n"
        "s_waitcnt lgkmcnt(4)\n"
        "v_mfma_f32_32x32x16_bf16 v[80:95], v[64:67], v[216:219], v[80:95]\n"
        "ds_read_b64_tr_b16 v[76:77], v39 offset:28672\n"
        "ds_read_b64_tr_b16 v[78:79], v39 offset:29440\n"
        "v_lshlrev_b32 v14, 3, v14\n"
        "v_add_u32 v14, 16384, v14\n"
        "v_and_b32 v19, 3, v11\n"
        "s_waitcnt lgkmcnt(4)\n"
        "v_mfma_f32_32x32x16_bf16 v[96:111], v[68:71], v[216:219], v[96:111]\n"
        "ds_read_b64_tr_b16 v[56:57], v39 offset:28704\n"
        "ds_read_b64_tr_b16 v[58:59], v39 offset:29472\n"
        "v_lshlrev_b32 v19, 3, v19\n"
        "v_lshl_or_b32 v19, v18, 5, v19\n"
        "s_waitcnt lgkmcnt(4)\n"
        "v_mfma_f32_32x32x16_bf16 v[112:127], v[72:75], v[216:219], v[112:127]\n"
        "ds_read_b64_tr_b16 v[60:61], v39 offset:28736\n"
        "ds_read_b64_tr_b16 v[62:63], v39 offset:29504\n"
        "v_mov_b64 v[180:181], 0\n"
        "v_mov_b64 v[182:183], 0\n"
        "s_cmp_lg_u32 s20, 0\n"
        "s_cbranch_scc1 .Lf1ok3_%=\n"
        "s_waitcnt vmcnt(0)\n"
        "v_cmp_ne_u64 vcc, s[30:31], v[24:25]\n"
        "s_cbranch_vccz .Lf1ok3_%=\n"
        "s_mov_b32 s81, 0x20000\n"
        ".Lpf13_%=:\n"
        "global_load_dwordx2 v[24:25], v26, s[18:19] sc1\n"
        "s_waitcnt vmcnt(0)\n"
        "v_cmp_ne_u64 vcc, s[30:31], v[24:25]\n"
        "s_cbranch_vccz .Lqf13_%=\n"
        "s_sleep 6\n"
        "s_sub_u32 s81, s81, 1\n"
        "s_cbranch_scc0 .Lpf13_%=\n"
        ".Lqf13_%=:\n"
        ".Lf1ok3_%=:\n"
        "s_waitcnt vmcnt(4) lgkmcnt(0)\n"
        "s_barrier\n"
        "v_mfma_f32_32x32x16_bf16 v[80:95], v[76:79], v[220:223], v[80:95]\n"
        "v_add_u32 v36, 0xe000, v36\n"
        "v_add_u32 v37, 0xe000, v37\n"
        "v_add_u32 v38, 0xe000, v38\n"
        "v_add_u32 v39, 0xe000, v39\n"
        "ds_read_b64_tr_b16 v[64:65], v36 offset:0\n"
        "ds_read_b64_tr_b16 v[66:67], v36 offset:768\n"
        "buffer_load_dwordx4 v[0:3], v15, s[32:35], s90 offen sc1\n"
        "v_mfma_f32_32x32x16_bf16 v[96:111], v[56:59], v[220:223], v[96:111]\n"
        "ds_read_b64_tr_b16 v[68:69], v36 offset:32\n"
        "ds_read_b64_tr_b16 v[70:71], v36 offset:800\n"
        "buffer_load_dwordx4 v[4:7], v15, s[32:35], s90 offen offset:1024 sc1\n"
        "v_add_u32 v19, s84, v19\n"
        "v_bfe_u32 v17, v11, 2, 2\n"
        "v_lshl_add_u32 v13, v17, 10, v19\n"
        "v_mfma_f32_32x32x16_bf16 v[112:127], v[60:63], v[220:223], v[112:127]\n"
        "ds_read_b64_tr_b16 v[72:73], v36 offset:64\n"
        "ds_read_b64_tr_b16 v[74:75], v36 offset:832\n"
        "v_lshl_add_u32 v13, v16, 7, v13\n"
        "v_bfe_u32 v17, v11, 2, 1\n"
        "v_mov_b64 v[184:185], 0\n"
        "v_mov_b64 v[186:187], 0\n"
        "s_waitcnt lgkmcnt(4)\n"
        "v_mfma_f32_32x32x16_bf16 v[80:95], v[64:67], v[224:227], v[80:95]\n"
        "ds_read_b64_tr_b16 v[76:77], v37 offset:0\n"
        "ds_read_b64_tr_b16 v[78:79], v37 offset:768\n"
        "s_waitcnt lgkmcnt(4)\n"
        "v_mfma_f32_32x32x16_bf16 v[96:111], v[68:71], v[224:227], v[96:111]\n"
        "ds_read_b64_tr_b16 v[56:57], v37 offset:32\n"
        "ds_read_b64_tr_b16 v[58:59], v37 offset:800\n"
        "s_waitcnt lgkmcnt(4)\n"
        "v_mfma_f32_32x32x16_bf16 v[112:127], v[72:75], v[224:227], v[112:127]\n"
        "ds_read_b64_tr_b16 v[60:61], v37 offset:64\n"
        "ds_read_b64_tr_b16 v[62:63], v37 offset:832\n"
        "v_mfma_f32_4x4x4_16b_bf16 v[44:47], v[40:41], v[64:65], v[44:47]\n"
        "s_mov_b32 m0, s87\n"
        "s_nop 0\n"
        "buffer_load_dwordx4 v15, s[92:95], s88 offen sc1 lds\n"
        "v_mfma_f32_4x4x4_16b_bf16 v[48:51], v[40:41], v[68:69], v[48:51]\n"
        "buffer_load_dwordx4 v15, s[92:95], s88 offen offset:1024 sc1 lds\n"
        "v_mfma_f32_4x4x4_16b_bf16 v[52:55], v[40:41], v[72:73], v[52:55]\n"
        "v_lshl_add_u32 v12, v17, 10, v19\n"
        "v_bfe_u32 v17, v11, 3, 1\n"
        "v_mfma_f32_4x4x4_16b_bf16 v[44:47], v[40:41], v[66:67], v[44:47]\n"
        "v_lshl_add_u32 v17, v16, 1, v17\n"
        "v_lshl_add_u32 v12, v17, 7, v12\n"
        "v_mfma_f32_4x4x4_16b_bf16 v[48:51], v[40:41], v[70:71], v[48:51]\n"
        "v_and_b32 v20, 31, v11\n"
        "v_mov_b64 v[188:189], 0\n"
        "v_mfma_f32_4x4x4_16b_bf16 v[52:55], v[40:41], v[74:75], v[52:55]\n"
        "v_mov_b64 v[190:191], 0\n"
        "s_waitcnt lgkmcnt(4)\n"
        "v_mfma_f32_32x32x16_bf16 v[80:95], v[76:79], v[228:231], v[80:95]\n"
        "ds_read_b64_tr_b16 v[64:65], v38 offset:0\n"
        "ds_read_b64_tr_b16 v[66:67], v38 offset:768\n"
        "s_add_u32 m0, s87, 0x10000\n"
        "s_nop 0\n"
        "buffer_load_dwordx4 v15, s[92:95], s89 offen sc1 lds\n"
        "s_lshl_b32 s39, s21, 9\n"
        "s_waitcnt lgkmcnt(4)\n"
        "v_mfma_f32_32x32x16_bf16 v[96:111], v[56:59], v[228:231], v[96:111]\n"
        "ds_read_b64_tr_b16 v[68:69], v38 offset:32\n"
        "ds_read_b64_tr_b16 v[70:71], v38 offset:800\n"
        "s_add_u32 s39, s39, 153600\n"
        "v_lshl_add_u32 v20, v20, 4, s39\n"
        "s_waitcnt lgkmcnt(4)\n"
        "v_mfma_f32_32x32x16_bf16 v[112:127], v[60:63], v[228:231], v[112:127]\n"
        "ds_read_b64_tr_b16 v[72:73], v38 offset:64\n"
        "ds_read_b64_tr_b16 v[74:75], v38 offset:832\n"
        "s_mul_i32 s39, s22, 3072\n"
        "s_add_u32 s39, s39, 156160\n"
        "s_waitcnt lgkmcnt(4)\n"
        "v_mfma_f32_32x32x16_bf16 v[80:95], v[64:67], v[232:235], v[80:95]\n"
        "ds_read_b64_tr_b16 v[76:77], v39 offset:0\n"
        "ds_read_b64_tr_b16 v[78:79], v39 offset:768\n"
        "buffer_load_dwordx4 v15, s[92:95], s89 offen offset:1024 sc1 lds\n"
        "v_add_u32 v21, s39, v15\n"
        "s_waitcnt lgkmcnt(4)\n"
        "v_mfma_f32_32x32x16_bf16 v[96:111], v[68:71], v[232:235], v[96:111]\n"
        "ds_read_b64_tr_b16 v[56:57], v39 offset:32\n"
        "ds_read_b64_tr_b16 v[58:59], v39 offset:800\n"
        "s_lshl_b32 s39, s21, 2\n"
        "v_add_u32 v22, s39, v21\n"
        "s_waitcnt lgkmcnt(4)\n"
        "v_mfma_f32_32x32x16_bf16 v[112:127], v[72:75], v[232:235], v[112:127]\n"
        "ds_read_b64_tr_b16 v[60:61], v39 offset:64\n"
        "ds_read_b64_tr_b16 v[62:63], v39 offset:832\n"
        "v_and_b32 v17, 27, v11\n"
        "v_cmp_eq_u32 s[56:57], 16, v17\n"
        "s_waitcnt vmcnt(6) lgkmcnt(0)\n"
        "s_barrier\n"
        "v_mfma_f32_32x32x16_bf16 v[80:95], v[76:79], v[236:239], v[80:95]\n"
        "v_add_u32 v36, 0xfffe4000, v36\n"
        "v_add_u32 v37, 0xfffe4000, v37\n"
        "v_add_u32 v38, 0xfffe4000, v38\n"
        "v_add_u32 v39, 0xfffe4000, v39\n"
        "ds_read_b64_tr_b16 v[64:65], v36 offset:0\n"
        "ds_read_b64_tr_b16 v[66:67], v36 offset:768\n"
        "v_mfma_f32_32x32x16_bf16 v[96:111], v[56:59], v[236:239], v[96:111]\n"
        "ds_read_b64_tr_b16 v[68:69], v36 offset:32\n"
        "ds_read_b64_tr_b16 v[70:71], v36 offset:800\n"
        "v_mov_b32 v23, 0x3f803f80\n"
        "v_and_b32 v17, 0x70, v15\n"
        "v_mfma_f32_32x32x16_bf16 v[112:127], v[60:63], v[236:239], v[112:127]\n"
        "ds_read_b64_tr_b16 v[72:73], v36 offset:64\n"
        "ds_read_b64_tr_b16 v[74:75], v36 offset:832\n"
        "v_lshrrev_b32 v16, 3, v11\n"
        "v_cndmask_b32 v23, 0, v23, s[56:57]\n"
        "v_lshl_or_b32 v10, v16, 12, v17\n"
        "s_waitcnt lgkmcnt(4)\n"
        "v_mfma_f32_32x32x16_bf16 v[80:95], v[64:67], v[240:243], v[80:95]\n"
        "ds_read_b64_tr_b16 v[76:77], v37 offset:0\n"
        "ds_read_b64_tr_b16 v[78:79], v37 offset:768\n"
        "s_waitcnt lgkmcnt(4)\n"
        "v_mfma_f32_32x32x16_bf16 v[96:111], v[68:71], v[240:243], v[96:111]\n"
        "ds_read_b64_tr_b16 v[56:57], v37 offset:32\n"
        "ds_read_b64_tr_b16 v[58:59], v37 offset:800\n"
        "s_waitcnt lgkmcnt(4)\n"
        "v_mfma_f32_32x32x16_bf16 v[112:127], v[72:75], v[240:243], v[112:127]\n"
        "ds_read_b64_tr_b16 v[60:61], v37 offset:64\n"
        "ds_read_b64_tr_b16 v[62:63], v37 offset:832\n"
        "v_mfma_f32_4x4x4_16b_bf16 v[44:47], v[40:41], v[64:65], v[44:47]\n"
        "v_mfma_f32_4x4x4_16b_bf16 v[48:51], v[40:41], v[68:69], v[48:51]\n"
        "v_mfma_f32_4x4x4_16b_bf16 v[52:55], v[40:41], v[72:73], v[52:55]\n"
        "v_mfma_f32_4x4x4_16b_bf16 v[44:47], v[40:41], v[66:67], v[44:47]\n"
        "v_mfma_f32_4x4x4_16b_bf16 v[48:51], v[40:41], v[70:71], v[48:51]\n"
        "v_lshl_or_b32 v11, v16, 11, v17\n"
        "v_mfma_f32_4x4x4_16b_bf16 v[52:55], v[40:41], v[74:75], v[52:55]\n"
        "s_waitcnt lgkmcnt(4)\n"
        "v_mfma_f32_32x32x16_bf16 v[80:95], v[76:79], v[244:247], v[80:95]\n"
        "ds_read_b64_tr_b16 v[64:65], v38 offset:0\n"
        "ds_read_b64_tr_b16 v[66:67], v38 offset:768\n"
        "v_mov_b64 v[24:25], 0\n"
        "v_mov_b64 v[26:27], 0\n"
        "s_waitcnt lgkmcnt(4)\n"
        "v_mfma_f32_32x32x16_bf16 v[96:111], v[56:59], v[244:247], v[96:111]\n"
        "ds_read_b64_tr_b16 v[68:69], v38 offset:32\n"
        "ds_read_b64_tr_b16 v[70:71], v38 offset:800\n"
        "v_mov_b64 v[28:29], 0\n"
        "s_waitcnt lgkmcnt(4)\n"
        "v_mfma_f32_32x32x16_bf16 v[112:127], v[60:63], v[244:247], v[112:127]\n"
        "ds_read_b64_tr_b16 v[72:73], v38 offset:64\n"
        "ds_read_b64_tr_b16 v[74:75], v38 offset:832\n"
        "v_mov_b64 v[30:31], 0\n"
        "s_waitcnt lgkmcnt(4)\n"
        "v_mfma_f32_32x32x16_bf16 v[80:95], v[64:67], v[248:251], v[80:95]\n"
        "ds_read_b64_tr_b16 v[76:77], v39 offset:0\n"
        "ds_read_b64_tr_b16 v[78:79], v39 offset:768\n"
        "ds_write_b32 v22, v44 offset:0\n"
        "s_waitcnt lgkmcnt(5)\n"
        "v_mfma_f32_32x32x16_bf16 v[96:111], v[68:71], v[248:251], v[96:111]\n"
        "ds_read_b64_tr_b16 v[56:57], v39 offset:32\n"
        "ds_read_b64_tr_b16 v[58:59], v39 offset:800\n"
        "ds_write_b32 v22, v48 offset:1024\n"
        "s_waitcnt lgkmcnt(6)\n"
        "v_mfma_f32_32x32x16_bf16 v[112:127], v[72:75], v[248:251], v[112:127]\n"
        "ds_read_b64_tr_b16 v[60:61], v39 offset:64\n"
        "ds_read_b64_tr_b16 v[62:63], v39 offset:832\n"
        "ds_write_b32 v22, v52 offset:2048\n"
        "s_waitcnt lgkmcnt(7)\n"
        "v_mfma_f32_32x32x16_bf16 v[80:95], v[76:79], v[252:255], v[80:95]\n"
        "s_waitcnt lgkmcnt(4)\n"
        "v_mfma_f32_32x32x16_bf16 v[96:111], v[56:59], v[252:255], v[96:111]\n"
        "s_waitcnt lgkmcnt(1)\n"
        "v_mfma_f32_32x32x16_bf16 v[112:127], v[60:63], v[252:255], v[112:127]\n"
        ".LD_%=:\n"
        "s_nop 7\n"
        "s_waitcnt vmcnt(0) lgkmcnt(0)\n"
        "s_barrier\n"
        "ds_read_b96 v[32:34], v20\n"
        "ds_read_b128 v[40:43], v21 offset:0\n"
        "ds_read_b128 v[44:47], v21 offset:1024\n"
        "ds_read_b128 v[48:51], v21 offset:2048\n"
        "s_lshl_b32 s56, s22, 16\n"
        "v_subrev_u32 v16, s56, v8\n"
        "ds_read_b128 v[52:55], v16\n"
        "ds_read_b128 v[56:59], v16 offset:2048\n"
        "v_mov_b64 v[66:67], 0\n"
        "v_mov_b64 v[70:71], 0\n"
        "v_mov_b64 v[74:75], 0\n"
        "v_mov_b64 v[78:79], 0\n"
        "s_waitcnt lgkmcnt(5)\n"
        "v_cvt_pk_bf16_f32 v64, -v32, -v32\n"
        "v_lshlrev_b32 v35, 16, v64\n"
        "v_sub_f32 v35, -v32, v35\n"
        "v_cvt_pk_bf16_f32 v64, -v32, v35\n"
        "v_mov_b32 v65, v64\n"
        "v_mul_f32 v36, 0xc0135761, v33\n"
        "v_mul_f32 v38, v33, v33\n"
        "v_mul_f32 v38, v38, v33\n"
        "v_mul_f32 v38, 0xbdd2d3e8, v38\n"
        "s_waitcnt lgkmcnt(0)\n"
        "s_barrier\n"
        "v_add_f32 v40, v40, v41\n"
        "v_add_f32 v42, v42, v43\n"
        "v_add_f32 v40, v40, v42\n"
        "v_cvt_pk_bf16_f32 v68, v40, v40\n"
        "v_lshlrev_b32 v41, 16, v68\n"
        "v_sub_f32 v40, v40, v41\n"
        "v_cvt_pk_bf16_f32 v69, v40, v40\n"
        "s_nop 1\n"
        "v_mfma_f32_32x32x16_bf16 v[80:95], v[68:71], v[64:67], v[80:95]\n"
        "v_add_f32 v44, v44, v45\n"
        "v_add_f32 v46, v46, v47\n"
        "v_add_f32 v44, v44, v46\n"
        "v_cvt_pk_bf16_f32 v72, v44, v44\n"
        "v_lshlrev_b32 v45, 16, v72\n"
        "v_sub_f32 v44, v44, v45\n"
        "v_cvt_pk_bf16_f32 v73, v44, v44\n"
        "s_nop 1\n"
        "v_mfma_f32_32x32x16_bf16 v[96:111], v[72:75], v[64:67], v[96:111]\n"
        "v_add_f32 v48, v48, v49\n"
        "v_add_f32 v50, v50, v51\n"
        "v_add_f32 v48, v48, v50\n"
        "v_cvt_pk_bf16_f32 v76, v48, v48\n"
        "v_lshlrev_b32 v49, 16, v76\n"
        "v_sub_f32 v48, v48, v49\n"
        "v_cvt_pk_bf16_f32 v77, v48, v48\n"
        "s_nop 1\n"
        "v_mfma_f32_32x32x16_bf16 v[112:127], v[76:79], v[64:67], v[112:127]\n"
        "s_mul_i32 s39, s29, 86\n"
        "s_add_u32 s39, s39, s27\n"
        "s_mul_i32 s56, s22, 3\n"
        "s_add_u32 s39, s39, s56\n"
        "s_lshl_b32 s39, s39, 10\n"
        "s_add_u32 s49, s39, 6356992\n"
        "v_pk_mul_f32 v[40:41], v[80:81], v[80:81]\n"
        "s_mov_b32 s60, 0xffff\n"
        "v_pk_mul_f32 v[42:43], v[82:83], v[82:83]\n"
        "s_mov_b32 s61, 0xffff\n"
        "v_pk_mul_f32 v[44:45], v[84:85], v[84:85]\n"
        "s_lshl_b32 s62, s20, 12\n"
        "v_pk_mul_f32 v[46:47], v[86:87], v[86:87]\n"
        "s_mul_i32 s91, s48, 22\n"
        "v_pk_fma_f32 v[40:41], v[40:41], v[38:39], v[36:37] op_sel_hi:[1,0,0]\n"
        "s_mul_i32 s59, s48, 21\n"
        "v_pk_fma_f32 v[42:43], v[42:43], v[38:39], v[36:37] op_sel_hi:[1,0,0]\n"
        "s_add_u32 s59, s59, 2\n"
        "v_mfma_f32_32x32x16_bf16 v[192:207], v[52:55], v[0:3], 0\n"
        "s_cmp_lt_u32 s48, 2\n"
        "v_pk_fma_f32 v[44:45], v[44:45], v[38:39], v[36:37] op_sel_hi:[1,0,0]\n"
        "s_cselect_b32 s91, s91, s59\n"
        "v_pk_fma_f32 v[46:47], v[46:47], v[38:39], v[36:37] op_sel_hi:[1,0,0]\n"
        "s_cselect_b32 s59, 22, 21\n"
        "v_pk_mul_f32 v[40:41], v[40:41], v[80:81]\n"
        "s_add_u32 s59, s59, s91\n"
        "v_pk_mul_f32 v[42:43], v[42:43], v[82:83]\n"
        "s_lshl_b32 s66, s59, 10\n"
        "v_pk_mul_f32 v[44:45], v[44:45], v[84:85]\n"
        "s_mov_b32 s67, 0x20000\n"
        "v_pk_mul_f32 v[46:47], v[46:47], v[86:87]\n"
        "s_lshl_b32 s91, s91, 10\n"
        "v_exp_f32 v40, v40\n"
        "s_add_u32 s90, s91, 22528\n"
        "v_pk_mul_f32 v[80:81], v[80:81], v[88:89]\n"
        "s_and_b32 s97, s40, 3\n"
        "v_mfma_f32_32x32x16_bf16 v[208:223], v[56:59], v[0:3], 0\n"
        "s_lshl_b32 s39, s97, 1\n"
        "v_exp_f32 v41, v41\n"
        "s_nop 0\n"
        "v_exp_f32 v42, v42\n"
        "s_add_u32 s39, s39, s41\n"
        "v_pk_mul_f32 v[82:83], v[82:83], v[90:91]\n"
        "s_mov_b32 s39, 0\n"
        "v_exp_f32 v43, v43\n"
        "s_mov_b32 s97, 0\n"
        "v_exp_f32 v44, v44\n"
        "s_mul_i32 s39, s39, 11\n"
        "v_pk_mul_f32 v[84:85], v[84:85], v[92:93]\n"
        "s_lshr_b32 s39, s39, 2\n"
        "v_exp_f32 v45, v45\n"
        "s_lshl_b32 s39, s39, 10\n"
        "v_exp_f32 v46, v46\n"
        "s_add_u32 s78, s91, s39\n"
        "v_mfma_f32_32x32x16_bf16 v[224:239], v[52:55], v[4:7], 0\n"
        "s_lshl_b32 s63, s78, 4\n"
        "v_pk_mul_f32 v[86:87], v[86:87], v[94:95]\n"
        "s_lshl_b32 s46, s40, 1\n"
        "v_exp_f32 v47, v47\n"
        "s_add_u32 s46, s46, s96\n"
        "v_pk_fma_f32 v[40:41], v[40:41], v[34:35], v[34:35] op_sel_hi:[1,0,0]\n"
        "s_mul_i32 s39, s46, 88064\n"
        "v_pk_fma_f32 v[42:43], v[42:43], v[34:35], v[34:35] op_sel_hi:[1,0,0]\n"
        "s_add_u32 s39, s39, 6356992\n"
        "v_pk_fma_f32 v[44:45], v[44:45], v[34:35], v[34:35] op_sel_hi:[1,0,0]\n"
        "s_add_u32 s64, s18, s39\n"
        "v_pk_fma_f32 v[46:47], v[46:47], v[34:35], v[34:35] op_sel_hi:[1,0,0]\n"
        "s_addc_u32 s65, s19, 0\n"
        "v_rcp_f32 v40, v40\n"
        "s_and_b32 s65, s65, 0xffff\n"
        "v_rcp_f32 v41, v41\n"
        "s_lshl_b32 s47, s41, 7\n"
        "v_mfma_f32_32x32x16_bf16 v[240:255], v[56:59], v[4:7], 0\n"
        "s_add_u32 s63, s63, s47\n"
        "v_rcp_f32 v42, v42\n"
        "s_mov_b32 s68, s14\n"
        "v_rcp_f32 v43, v43\n"
        "s_and_b32 s69, s15, 0xffff\n"
        "v_rcp_f32 v44, v44\n"
        "s_mov_b32 s70, 1409024\n"
        "v_rcp_f32 v45, v45\n"
        "s_mov_b32 s71, 0x20000\n"
        "v_rcp_f32 v46, v46\n"
        "s_lshl_b32 s39, s48, 17\n"
        "v_rcp_f32 v47, v47\n"
        "s_add_u32 s39, s39, s47\n"
        "v_pk_mul_f32 v[80:81], v[80:81], v[40:41]\n"
        "s_add_u32 s72, s8, s39\n"
        "v_pk_mul_f32 v[82:83], v[82:83], v[42:43]\n"
        "s_addc_u32 s73, s9, 0\n"
        "v_pk_mul_f32 v[84:85], v[84:85], v[44:45]\n"
        "s_and_b32 s73, s73, 0xffff\n"
        "v_pk_mul_f32 v[86:87], v[86:87], v[46:47]\n"
        "s_sub_u32 s74, 0x20000, s47\n"
        "v_cvt_pk_bf16_f32 v88, v80, v81\n"
        "s_mov_b32 s75, 0x20000\n"
        "v_cvt_pk_bf16_f32 v89, v82, v83\n"
        "s_lshl_b32 s79, s97, 15\n"
        "v_cvt_pk_bf16_f32 v90, v84, v85\n"
        "s_lshl_b32 s39, s25, 4\n"
        "v_cvt_pk_bf16_f32 v91, v86, v87\n"
        "s_add_u32 s39, s39, s26\n"
        "buffer_store_dwordx4 v[88:91], v15, s[32:35], s49 offen offset:0 sc1\n"
        "s_lshl_b32 s39, s39, 3\n"
        "s_add_u32 s39, s39, s20\n"
        "s_lshl_b32 s39, s39, 3\n"
        "s_add_u32 s39, s39, 16384\n"
        "s_add_u32 s76, s18, s39\n"
        "s_addc_u32 s77, s19, 0\n"
        "s_mov_b32 s85, 36928\n"
        "s_cmp_eq_u32 s22, 0\n"
        "s_cselect_b32 s85, 8706, s85\n"
        "s_mov_b32 s80, 0\n"
        "s_mov_b32 s86, 3\n"
        "v_pk_mul_f32 v[40:41], v[96:97], v[96:97]\n"
        "v_max_f32 v60, v192, v208\n"
        "v_pk_mul_f32 v[42:43], v[98:99], v[98:99]\n"
        "v_max_f32 v61, v224, v240\n"
        "v_pk_mul_f32 v[44:45], v[100:101], v[100:101]\n"
        "v_pk_mul_f32 v[46:47], v[102:103], v[102:103]\n"
        "v_max3_f32 v60, v60, v193, v209\n"
        "v_pk_fma_f32 v[40:41], v[40:41], v[38:39], v[36:37] op_sel_hi:[1,0,0]\n"
        "v_max3_f32 v61, v61, v225, v241\n"
        "v_pk_fma_f32 v[42:43], v[42:43], v[38:39], v[36:37] op_sel_hi:[1,0,0]\n"
        "v_max3_f32 v60, v60, v194, v210\n"
        "v_pk_fma_f32 v[44:45], v[44:45], v[38:39], v[36:37] op_sel_hi:[1,0,0]\n"
        "v_pk_fma_f32 v[46:47], v[46:47], v[38:39], v[36:37] op_sel_hi:[1,0,0]\n"
        "v_max3_f32 v61, v61, v226, v242\n"
        "v_pk_mul_f32 v[40:41], v[40:41], v[96:97]\n"
        "v_max3_f32 v60, v60, v195, v211\n"
        "v_pk_mul_f32 v[42:43], v[42:43], v[98:99]\n"
        "v_pk_mul_f32 v[44:45], v[44:45], v[100:101]\n"
        "v_max3_f32 v61, v61, v227, v243\n"
        "v_pk_mul_f32 v[46:47], v[46:47], v[102:103]\n"
        "v_max3_f32 v60, v60, v196, v212\n"
        "v_exp_f32 v40, v40\n"
        "v_max3_f32 v61, v61, v228, v244\n"
        "v_pk_mul_f32 v[96:97], v[96:97], v[104:105]\n"
        "v_exp_f32 v41, v41\n"
        "v_max3_f32 v60, v60, v197, v213\n"
        "v_exp_f32 v42, v42\n"
        "v_max3_f32 v61, v61, v229, v245\n"
        "v_pk_mul_f32 v[98:99], v[98:99], v[106:107]\n"
        "v_exp_f32 v43, v43\n"
        "v_max3_f32 v60, v60, v198, v214\n"
        "v_exp_f32 v44, v44\n"
        "v_max3_f32 v61, v61, v230, v246\n"
        "v_pk_mul_f32 v[100:101], v[100:101], v[108:109]\n"
        "v_max3_f32 v60, v60, v199, v215\n"
        "v_exp_f32 v45, v45\n"
        "v_exp_f32 v46, v46\n"
        "v_max3_f32 v61, v61, v231, v247\n"
        "v_pk_mul_f32 v[102:103], v[102:103], v[110:111]\n"
        "v_max3_f32 v60, v60, v200, v216\n"
        "v_exp_f32 v47, v47\n"
        "v_pk_fma_f32 v[40:41], v[40:41], v[34:35], v[34:35] op_sel_hi:[1,0,0]\n"
        "v_max3_f32 v61, v61, v232, v248\n"
        "v_pk_fma_f32 v[42:43], v[42:43], v[34:35], v[34:35] op_sel_hi:[1,0,0]\n"
        "v_max3_f32 v60, v60, v201, v217\n"
        "v_pk_fma_f32 v[44:45], v[44:45], v[34:35], v[34:35] op_sel_hi:[1,0,0]\n"
        "v_max3_f32 v61, v61, v233, v249\n"
        "v_pk_fma_f32 v[46:47], v[46:47], v[34:35], v[34:35] op_sel_hi:[1,0,0]\n"
        "v_rcp_f32 v40, v40\n"
        "v_max3_f32 v60, v60, v202, v218\n"
        "v_rcp_f32 v41, v41\n"
        "v_max3_f32 v61, v61, v234, v250\n"
        "v_rcp_f32 v42, v42\n"
        "v_rcp_f32 v43, v43\n"
        "v_max3_f32 v60, v60, v203, v219\n"
        "v_rcp_f32 v44, v44\n"
        "v_max3_f32 v61, v61, v235, v251\n"
        "v_rcp_f32 v45, v45\n"
        "v_max3_f32 v60, v60, v204, v220\n"
        "v_rcp_f32 v46, v46\n"
        "v_rcp_f32 v47, v47\n"
        "v_max3_f32 v61, v61, v236, v252\n"
        "v_pk_mul_f32 v[96:97], v[96:97], v[40:41]\n"
        "v_max3_f32 v60, v60, v205, v221\n"
        "v_pk_mul_f32 v[98:99], v[98:99], v[42:43]\n"
        "v_pk_mul_f32 v[100:101], v[100:101], v[44:45]\n"
        "v_max3_f32 v61, v61, v237, v253\n"
        "v_pk_mul_f32 v[102:103], v[102:103], v[46:47]\n"
        "v_max3_f32 v60, v60, v206, v222\n"
        "v_cvt_pk_bf16_f32 v104, v96, v97\n"
        "v_max3_f32 v61, v61, v238, v254\n"
        "v_cvt_pk_bf16_f32 v105, v98, v99\n"
        "v_cvt_pk_bf16_f32 v106, v100, v101\n"
        "v_max3_f32 v60, v60, v207, v223\n"
        "v_cvt_pk_bf16_f32 v107, v102, v103\n"
        "v_max3_f32 v61, v61, v239, v255\n"
        "buffer_store_dwordx4 v[104:107], v15, s[32:35], s49 offen offset:1024 sc1\n"
        "v_mov_b32 v62, v60\n"
        "v_mov_b32 v63, v61\n"
        "s_nop 0\n"
        "v_permlane32_swap_b32 v60, v62\n"
        "v_permlane32_swap_b32 v61, v63\n"
        "v_max_f32 v60, v60, v62\n"
        "v_max_f32 v62, v61, v63\n"
        "v_sub_f32 v60, 0xc2b00000, v60\n"
        "v_sub_f32 v62, 0xc2b00000, v62\n"
        "v_cmp_gt_u32 vcc, 512, v15\n"
        "v_mov_b32 v64, 0x3f80\n"
        "v_mov_b32 v65, 0\n"
        "v_mov_b32 v69, 0\n"
        "v_mov_b32 v73, 0\n"
        "v_cndmask_b32 v64, 0, v64, vcc\n"
        "v_cvt_pk_bf16_f32 v68, v60, v65\n"
        "v_cvt_pk_bf16_f32 v72, v62, v65\n"
        "s_nop 0\n"
        "v_mfma_f32_32x32x16_bf16 v[128:143], v[64:67], v[68:71], 0\n"
        "v_mov_b64 v[48:49], 0\n"
        "v_mov_b64 v[50:51], 0\n"
        "v_mfma_f32_32x32x16_bf16 v[144:159], v[64:67], v[72:75], 0\n"
        "v_mov_b64 v[52:53], 0\n"
        "v_mov_b64 v[54:55], 0\n"
        "ds_read_b128 v[60:63], v8\n"
        "v_mfma_f32_32x32x16_bf16 v[96:111], v[48:51], v[48:51], 0\n"
        "s_cmp_lg_u32 s28, 0\n"
        "s_cbranch_scc1 .Lg2_%=\n"
        "v_pk_mul_f32 v[40:41], v[112:113], v[112:113]\n"
        "v_pk_mul_f32 v[42:43], v[114:115], v[114:115]\n"
        "v_pk_mul_f32 v[44:45], v[116:117], v[116:117]\n"
        "v_pk_mul_f32 v[46:47], v[118:119], v[118:119]\n"
        "v_pk_fma_f32 v[40:41], v[40:41], v[38:39], v[36:37] op_sel_hi:[1,0,0]\n"
        "v_pk_fma_f32 v[42:43], v[42:43], v[38:39], v[36:37] op_sel_hi:[1,0,0]\n"
        "v_pk_fma_f32 v[44:45], v[44:45], v[38:39], v[36:37] op_sel_hi:[1,0,0]\n"
        "v_pk_fma_f32 v[46:47], v[46:47], v[38:39], v[36:37] op_sel_hi:[1,0,0]\n"
        "v_pk_mul_f32 v[40:41], v[40:41], v[112:113]\n"
        "v_pk_mul_f32 v[42:43], v[42:43], v[114:115]\n"
        "v_pk_mul_f32 v[44:45], v[44:45], v[116:117]\n"
        "v_pk_mul_f32 v[46:47], v[46:47], v[118:119]\n"
        "v_exp_f32 v40, v40\n"
        "v_pk_mul_f32 v[112:113], v[112:113], v[120:121]\n"
        "v_exp_f32 v41, v41\n"
        "v_exp_f32 v42, v42\n"
        "v_pk_mul_f32 v[114:115], v[114:115], v[122:123]\n"
        "v_exp_f32 v43, v43\n"
        "v_exp_f32 v44, v44\n"
        "v_pk_mul_f32 v[116:117], v[116:117], v[124:125]\n"
        "v_exp_f32 v45, v45\n"
        "v_exp_f32 v46, v46\n"
        "v_pk_mul_f32 v[118:119], v[118:119], v[126:127]\n"
        "v_exp_f32 v47, v47\n"
        "v_pk_fma_f32 v[40:41], v[40:41], v[34:35], v[34:35] op_sel_hi:[1,0,0]\n"
        "v_pk_fma_f32 v[42:43], v[42:43], v[34:35], v[34:35] op_sel_hi:[1,0,0]\n"
        "v_pk_fma_f32 v[44:45], v[44:45], v[34:35], v[34:35] op_sel_hi:[1,0,0]\n"
        "v_pk_fma_f32 v[46:47], v[46:47], v[34:35], v[34:35] op_sel_hi:[1,0,0]\n"
        "v_rcp_f32 v40, v40\n"
        "v_rcp_f32 v41, v41\n"
        "v_rcp_f32 v42, v42\n"
        "v_rcp_f32 v43, v43\n"
        "v_rcp_f32 v44, v44\n"
        "v_rcp_f32 v45, v45\n"
        "v_rcp_f32 v46, v46\n"
        "v_rcp_f32 v47, v47\n"
        "v_pk_mul_f32 v[112:113], v[112:113], v[40:41]\n"
        "v_pk_mul_f32 v[114:115], v[114:115], v[42:43]\n"
        "v_pk_mul_f32 v[116:117], v[116:117], v[44:45]\n"
        "v_pk_mul_f32 v[118:119], v[118:119], v[46:47]\n"
        "v_cvt_pk_bf16_f32 v120, v112, v113\n"
        "v_cvt_pk_bf16_f32 v121, v114, v115\n"
        "v_cvt_pk_bf16_f32 v122, v116, v117\n"
        "v_cvt_pk_bf16_f32 v123, v118, v119\n"
        "buffer_store_dwordx4 v[120:123], v15, s[32:35], s49 offen offset:2048 sc1\n"
        ".Lg2_%=:\n"
        "v_mov_b64 v[16:17], -1\n"
        "v_mov_b64 v[18:19], -1\n"
        "buffer_store_dwordx4 v[16:19], v15, s[32:35], s3 offen sc1\n"
        "s_waitcnt lgkmcnt(0)\n"
        "v_mfma_f32_32x32x16_bf16 v[64:79], v[60:63], v[0:3], v[128:143]\n"
        "ds_read_b128 v[44:47], v8\n"
        "v_mov_b32 v32, v23\n"
        "v_mov_b32 v33, v23\n"
        "v_mov_b64 v[34:35], v[32:33]\n"
        "v_mov_b64 v[36:37], v[32:33]\n"
        "v_mov_b64 v[38:39], v[32:33]\n"
        "v_mov_b64 v[40:41], v[32:33]\n"
        "v_mov_b64 v[42:43], v[32:33]\n"
        "s_mov_b64 exec, s[60:61]\n"
        "ds_read_b128 v[40:43], v9\n"
        "ds_read_b128 v[36:39], v9 offset:512\n"
        "s_mov_b64 exec, -1\n"
        "v_mfma_f32_32x32x16_bf16 v[112:127], v[48:51], v[48:51], 0\n"
        "s_waitcnt lgkmcnt(0)\n"
        "s_setprio 3\n"
        "s_branch .Lpre_%=\n"
        ".Lspec_%=:\n"
        "s_cmp_lg_u32 s80, 3\n"
        "s_cbranch_scc1 .Lpre_%=\n"
        "s_waitcnt vmcnt(4)\n"
        "s_barrier\n"
        "v_lshrrev_b32 v59, 1, v11\n"
        "v_and_b32 v57, 0x70, v15\n"
        "v_and_b32 v59, 0x1c00, v59\n"
        "v_or_b32 v59, v59, v57\n"
        "s_lshl_b32 s39, s40, 16\n"
        "s_lshl_b32 s56, s20, 13\n"
        "s_add_u32 s39, s39, s56\n"
        "s_add_u32 s53, s39, s47\n"
        "s_add_u32 s82, s4, s53\n"
        "s_addc_u32 s83, s5, 0\n"
        "global_load_dwordx4 v[60:63], v59, s[82:83]\n"
        "global_load_dwordx2 v[56:57], v14, s[18:19] sc1\n"
        ".Lpre_%=:\n"
        "s_bitcmp0_b32 s85, s80\n"
        "s_cbranch_scc1 .Lpda_%=\n"
        "s_sub_u32 s86, s86, 1\n"
        "s_cmp_eq_u32 s86, 2\n"
        "s_cbranch_scc1 .Lp2a_%=\n"
        "s_cmp_eq_u32 s86, 1\n"
        "s_cbranch_scc1 .Lp1a_%=\n"
        "s_setprio 0\n"
        "s_branch .Lpda_%=\n"
        ".Lp2a_%=:\n"
        "s_setprio 2\n"
        "s_branch .Lpda_%=\n"
        ".Lp1a_%=:\n"
        "s_setprio 1\n"
        ".Lpda_%=:\n"
        "v_mfma_f32_32x32x16_bf16 v[80:95], v[44:47], v[4:7], v[144:159]\n"
        "ds_read_b128 v[44:47], v8 offset:2048\n"
        "v_exp_f32 v64, v64\n"
        "v_exp_f32 v65, v65\n"
        "v_exp_f32 v66, v66\n"
        "v_cvt_pk_bf16_f32 v48, v64, v65\n"
        "v_exp_f32 v67, v67\n"
        "v_exp_f32 v68, v68\n"
        "v_cvt_pk_bf16_f32 v49, v66, v67\n"
        "v_exp_f32 v69, v69\n"
        "v_mfma_f32_32x32x16_bf16 v[112:127], v[32:35], v[52:55], v[112:127]\n"
        "s_cmp_gt_u32 s80, 2\n"
        "s_cbranch_scc1 .Lkv0_%=\n"
        "s_add_u32 s88, s88, 0x4000\n"
        "s_add_u32 m0, s87, 16384\n"
        "s_and_b32 s88, s88, 0x1ffff\n"
        "buffer_load_dwordx4 v15, s[92:95], s88 offen sc1 lds\n"
        "buffer_load_dwordx4 v15, s[92:95], s88 offen offset:1024 sc1 lds\n"
        ".Lkv0_%=:\n"
        "v_exp_f32 v70, v70\n"
        "v_cvt_pk_bf16_f32 v50, v68, v69\n"
        "v_exp_f32 v71, v71\n"
        "v_exp_f32 v72, v72\n"
        "v_cvt_pk_bf16_f32 v51, v70, v71\n"
        "v_exp_f32 v73, v73\n"
        "v_exp_f32 v74, v74\n"
        "s_waitcnt lgkmcnt(0)\n"
        "v_mfma_f32_32x32x16_bf16 v[96:111], v[40:43], v[48:51], v[96:111]\n"
        "v_exp_f32 v75, v75\n"
        "v_cvt_pk_bf16_f32 v52, v72, v73\n"
        "v_exp_f32 v76, v76\n"
        "v_exp_f32 v77, v77\n"
        "v_cvt_pk_bf16_f32 v53, v74, v75\n"
        "v_exp_f32 v78, v78\n"
        "v_exp_f32 v79, v79\n"
        "v_cvt_pk_bf16_f32 v54, v76, v77\n"
        "v_cvt_pk_bf16_f32 v55, v78, v79\n"
        "v_mfma_f32_32x32x16_bf16 v[64:79], v[44:47], v[0:3], v[128:143]\n"
        "v_exp_f32 v80, v80\n"
        "v_exp_f32 v81, v81\n"
        "v_exp_f32 v82, v82\n"
        "v_cvt_pk_bf16_f32 v48, v80, v81\n"
        "v_exp_f32 v83, v83\n"
        "v_exp_f32 v84, v84\n"
        "v_cvt_pk_bf16_f32 v49, v82, v83\n"
        "v_exp_f32 v85, v85\n"
        "v_mfma_f32_32x32x16_bf16 v[96:111], v[36:39], v[52:55], v[96:111]\n"
        "s_cmp_gt_u32 s80, 2\n"
        "s_cbranch_scc1 .Lkv1_%=\n"
        "s_add_u32 s89, s89, 0x4000\n"
        "s_add_u32 m0, s87, 81920\n"
        "s_and_b32 s89, s89, 0x1ffff\n"
        "buffer_load_dwordx4 v15, s[92:95], s89 offen sc1 lds\n"
        "buffer_load_dwordx4 v15, s[92:95], s89 offen offset:1024 sc1 lds\n"
        ".Lkv1_%=:\n"
        "v_exp_f32 v86, v86\n"
        "v_cvt_pk_bf16_f32 v50, v84, v85\n"
        "v_exp_f32 v87, v87\n"
        "v_exp_f32 v88, v88\n"
        "v_cvt_pk_bf16_f32 v51, v86, v87\n"
        "v_exp_f32 v89, v89\n"
        "v_exp_f32 v90, v90\n"
        "v_mfma_f32_32x32x16_bf16 v[112:127], v[40:43], v[48:51], v[112:127]\n"
        "s_mov_b64 exec, s[60:61]\n"
        "ds_read_b128 v[40:43], v9 offset:2048\n"
        "ds_read_b128 v[32:35], v9 offset:2560\n"
        "s_mov_b64 exec, -1\n"
        "v_exp_f32 v91, v91\n"
        "v_cvt_pk_bf16_f32 v52, v88, v89\n"
        "v_exp_f32 v92, v92\n"
        "v_exp_f32 v93, v93\n"
        "v_cvt_pk_bf16_f32 v53, v90, v91\n"
        "v_exp_f32 v94, v94\n"
        "v_exp_f32 v95, v95\n"
        "v_cvt_pk_bf16_f32 v54, v92, v93\n"
        "v_cvt_pk_bf16_f32 v55, v94, v95\n"
        "v_mfma_f32_32x32x16_bf16 v[80:95], v[44:47], v[4:7], v[144:159]\n"
        "ds_read_b128 v[44:47], v8 offset:4096\n"
        "v_exp_f32 v64, v64\n"
        "v_exp_f32 v65, v65\n"
        "v_exp_f32 v66, v66\n"
        "v_cvt_pk_bf16_f32 v48, v64, v65\n"
        "v_exp_f32 v67, v67\n"
        "v_exp_f32 v68, v68\n"
        "v_cvt_pk_bf16_f32 v49, v66, v67\n"
        "v_exp_f32 v69, v69\n"
        "v_mfma_f32_32x32x16_bf16 v[112:127], v[36:39], v[52:55], v[112:127]\n"
        "s_cmp_eq_u32 s80, 0\n"
        "s_cbranch_scc1 .Lmid0_%=\n"
        "s_cmp_eq_u32 s80, 1\n"
        "s_cbranch_scc1 .Lmid1_%=\n"
        "s_cmp_eq_u32 s80, 2\n"
        "s_cbranch_scc1 .Lmid2_%=\n"
        "s_waitcnt vmcnt(2)\n"
        "ds_read_b64_tr_b16 v[224:225], v13 offset:0\n"
        "ds_read_b64_tr_b16 v[226:227], v13 offset:256\n"
        "ds_read_b64_tr_b16 v[228:229], v13 offset:64\n"
        "ds_read_b64_tr_b16 v[230:231], v13 offset:320\n"
        "ds_read_b64_tr_b16 v[232:233], v13 offset:512\n"
        "ds_read_b64_tr_b16 v[234:235], v13 offset:768\n"
        "ds_read_b64_tr_b16 v[236:237], v13 offset:576\n"
        "ds_read_b64_tr_b16 v[238:239], v13 offset:832\n"
        "s_waitcnt lgkmcnt(0)\n"
        "s_branch .Lmid_%=\n"
        ".Lmid0_%=:\n"
        "s_waitcnt vmcnt(4)\n"
        "s_mov_b64 exec, 1\n"
        "v_mov_b32 v16, s30\n"
        "v_mov_b32 v17, s31\n"
        "global_store_dwordx2 v15, v[16:17], s[76:77] sc1\n"
        "s_mov_b64 exec, -1\n"
        "s_branch .Lmid_%=\n"
        ".Lmid1_%=:\n"
        "s_waitcnt vmcnt(4)\n"
        "ds_read_b64_tr_b16 v[192:193], v13 offset:0\n"
        "ds_read_b64_tr_b16 v[194:195], v13 offset:256\n"
        "ds_read_b64_tr_b16 v[196:197], v13 offset:64\n"
        "ds_read_b64_tr_b16 v[198:199], v13 offset:320\n"
        "ds_read_b64_tr_b16 v[200:201], v13 offset:512\n"
        "ds_read_b64_tr_b16 v[202:203], v13 offset:768\n"
        "ds_read_b64_tr_b16 v[204:205], v13 offset:576\n"
        "ds_read_b64_tr_b16 v[206:207], v13 offset:832\n"
        "s_waitcnt lgkmcnt(0)\n"
        "s_branch .Lmid_%=\n"
        ".Lmid2_%=:\n"
        "s_waitcnt vmcnt(4)\n"
        "ds_read_b64_tr_b16 v[208:209], v13 offset:0\n"
        "ds_read_b64_tr_b16 v[210:211], v13 offset:256\n"
        "ds_read_b64_tr_b16 v[212:213], v13 offset:64\n"
        "ds_read_b64_tr_b16 v[214:215], v13 offset:320\n"
        "ds_read_b64_tr_b16 v[216:217], v13 offset:512\n"
        "ds_read_b64_tr_b16 v[218:219], v13 offset:768\n"
        "ds_read_b64_tr_b16 v[220:221], v13 offset:576\n"
        "ds_read_b64_tr_b16 v[222:223], v13 offset:832\n"
        "s_waitcnt lgkmcnt(0)\n"
        ".Lmid_%=:\n"
        "s_mov_b32 m0, s62\n"
        "s_nop 0\n"
        "buffer_load_dwordx4 v10, s[72:75], s79 offen lds\n"
        "buffer_load_dwordx4 v10, s[72:75], s79 offen offset:1024 lds\n"
        "v_exp_f32 v70, v70\n"
        "v_cvt_pk_bf16_f32 v50, v68, v69\n"
        "v_exp_f32 v71, v71\n"
        "v_exp_f32 v72, v72\n"
        "v_cvt_pk_bf16_f32 v51, v70, v71\n"
        "v_exp_f32 v73, v73\n"
        "v_exp_f32 v74, v74\n"
        "s_waitcnt lgkmcnt(0)\n"
        "v_mfma_f32_32x32x16_bf16 v[96:111], v[40:43], v[48:51], v[96:111]\n"
        "v_exp_f32 v75, v75\n"
        "v_cvt_pk_bf16_f32 v52, v72, v73\n"
        "v_exp_f32 v76, v76\n"
        "v_exp_f32 v77, v77\n"
        "v_cvt_pk_bf16_f32 v53, v74, v75\n"
        "v_exp_f32 v78, v78\n"
        "v_exp_f32 v79, v79\n"
        "v_cvt_pk_bf16_f32 v54, v76, v77\n"
        "v_cvt_pk_bf16_f32 v55, v78, v79\n"
        "v_mfma_f32_32x32x16_bf16 v[64:79], v[44:47], v[0:3], v[128:143]\n"
        "v_exp_f32 v80, v80\n"
        "v_exp_f32 v81, v81\n"
        "v_exp_f32 v82, v82\n"
        "v_cvt_pk_bf16_f32 v48, v80, v81\n"
        "v_exp_f32 v83, v83\n"
        "v_exp_f32 v84, v84\n"
        "v_cvt_pk_bf16_f32 v49, v82, v83\n"
        "v_exp_f32 v85, v85\n"
        "v_mfma_f32_32x32x16_bf16 v[96:111], v[32:35], v[52:55], v[96:111]\n"
        "buffer_load_dwordx4 v10, s[72:75], s79 offen offset:2048 lds\n"
        "buffer_load_dwordx4 v10, s[72:75], s79 offen offset:3072 lds\n"
        "v_exp_f32 v86, v86\n"
        "v_cvt_pk_bf16_f32 v50, v84, v85\n"
        "v_exp_f32 v87, v87\n"
        "v_exp_f32 v88, v88\n"
        "v_cvt_pk_bf16_f32 v51, v86, v87\n"
        "v_exp_f32 v89, v89\n"
        "v_exp_f32 v90, v90\n"
        "v_mfma_f32_32x32x16_bf16 v[112:127], v[40:43], v[48:51], v[112:127]\n"
        "s_mov_b64 exec, s[60:61]\n"
        "ds_read_b128 v[40:43], v9 offset:4096\n"
        "ds_read_b128 v[36:39], v9 offset:4608\n"
        "s_mov_b64 exec, -1\n"
        "v_exp_f32 v91, v91\n"
        "v_cvt_pk_bf16_f32 v52, v88, v89\n"
        "v_exp_f32 v92, v92\n"
        "v_exp_f32 v93, v93\n"
        "v_cvt_pk_bf16_f32 v53, v90, v91\n"
        "v_exp_f32 v94, v94\n"
        "v_exp_f32 v95, v95\n"
        "v_cvt_pk_bf16_f32 v54, v92, v93\n"
        "v_cvt_pk_bf16_f32 v55, v94, v95\n"
        "v_add_u32 v8, 4096, v8\n"
        "v_add_u32 v9, 4096, v9\n"
        "s_add_u32 s87, s87, 0x4000\n"
        "s_add_u32 s79, s79, 0x8000\n"
        "s_and_b32 s79, s79, 0x1ffff\n"
        "s_add_u32 s80, s80, 1\n"
        "s_cmp_lt_u32 s80, 4\n"
        "s_cbranch_scc1 .Lspec_%=\n"
        "s_waitcnt vmcnt(0)\n"
        "ds_read_b64_tr_b16 v[240:241], v13 offset:0\n"
        "ds_read_b64_tr_b16 v[242:243], v13 offset:256\n"
        "ds_read_b64_tr_b16 v[244:245], v13 offset:64\n"
        "ds_read_b64_tr_b16 v[246:247], v13 offset:320\n"
        "ds_read_b64_tr_b16 v[248:249], v13 offset:512\n"
        "ds_read_b64_tr_b16 v[250:251], v13 offset:768\n"
        "ds_read_b64_tr_b16 v[252:253], v13 offset:576\n"
        "ds_read_b64_tr_b16 v[254:255], v13 offset:832\n"
        "s_waitcnt lgkmcnt(0)\n"
        "v_cmp_ne_u64 vcc, s[30:31], v[56:57]\n"
        "s_cbranch_vccz .Lqf2_%=\n"
        "s_mov_b32 s81, 0x20000\n"
        ".Lpf2_%=:\n"
        "global_load_dwordx2 v[56:57], v14, s[18:19] sc1\n"
        "s_waitcnt vmcnt(0)\n"
        "v_cmp_ne_u64 vcc, s[30:31], v[56:57]\n"
        "s_cbranch_vccz .Lqf2_%=\n"
        "s_sleep 6\n"
        "s_sub_u32 s81, s81, 1\n"
        "s_cbranch_scc0 .Lpf2_%=\n"
        ".Lqf2_%=:\n"
        ".Ldown_%=:\n"
        "s_bitcmp0_b32 s85, s80\n"
        "s_cbranch_scc1 .Lpdb0_%=\n"
        "s_sub_u32 s86, s86, 1\n"
        "s_cmp_eq_u32 s86, 2\n"
        "s_cbranch_scc1 .Lp2b0_%=\n"
        "s_cmp_eq_u32 s86, 1\n"
        "s_cbranch_scc1 .Lp1b0_%=\n"
        "s_setprio 0\n"
        "s_branch .Lpdb0_%=\n"
        ".Lp2b0_%=:\n"
        "s_setprio 2\n"
        "s_branch .Lpdb0_%=\n"
        ".Lp1b0_%=:\n"
        "s_setprio 1\n"
        ".Lpdb0_%=:\n"
        "s_waitcnt vmcnt(3)\n"
        "v_mfma_f32_32x32x16_bf16 v[80:95], v[44:47], v[4:7], v[144:159]\n"
        "ds_read_b128 v[44:47], v8 offset:2048\n"
        "ds_read_b64_tr_b16 v[20:21], v12 offset:0\n"
        "ds_read_b64_tr_b16 v[22:23], v12 offset:512\n"
        "v_exp_f32 v64, v64\n"
        "v_exp_f32 v65, v65\n"
        "v_exp_f32 v66, v66\n"
        "v_cvt_pk_bf16_f32 v48, v64, v65\n"
        "v_exp_f32 v67, v67\n"
        "v_exp_f32 v68, v68\n"
        "v_cvt_pk_bf16_f32 v49, v66, v67\n"
        "v_exp_f32 v69, v69\n"
        "v_mfma_f32_32x32x16_bf16 v[112:127], v[32:35], v[52:55], v[112:127]\n"
        "ds_read_b64_tr_b16 v[16:17], v12 offset:64\n"
        "ds_read_b64_tr_b16 v[18:19], v12 offset:576\n"
        "v_exp_f32 v70, v70\n"
        "v_cvt_pk_bf16_f32 v50, v68, v69\n"
        "v_exp_f32 v71, v71\n"
        "v_exp_f32 v72, v72\n"
        "v_cvt_pk_bf16_f32 v51, v70, v71\n"
        "v_exp_f32 v73, v73\n"
        "v_exp_f32 v74, v74\n"
        "s_waitcnt lgkmcnt(0)\n"
        "v_mfma_f32_32x32x16_bf16 v[96:111], v[40:43], v[48:51], v[96:111]\n"
        "v_exp_f32 v75, v75\n"
        "v_cvt_pk_bf16_f32 v52, v72, v73\n"
        "v_exp_f32 v76, v76\n"
        "v_exp_f32 v77, v77\n"
        "v_cvt_pk_bf16_f32 v53, v74, v75\n"
        "v_exp_f32 v78, v78\n"
        "v_exp_f32 v79, v79\n"
        "v_cvt_pk_bf16_f32 v54, v76, v77\n"
        "v_cvt_pk_bf16_f32 v55, v78, v79\n"
        "v_mfma_f32_32x32x16_bf16 v[64:79], v[44:47], v[0:3], v[128:143]\n"
        "v_exp_f32 v80, v80\n"
        "v_exp_f32 v81, v81\n"
        "v_exp_f32 v82, v82\n"
        "v_cvt_pk_bf16_f32 v48, v80, v81\n"
        "v_exp_f32 v83, v83\n"
        "v_exp_f32 v84, v84\n"
        "v_cvt_pk_bf16_f32 v49, v82, v83\n"
        "v_exp_f32 v85, v85\n"
        "s_add_u32 m0, s62, 0\n"
        "v_mfma_f32_32x32x16_bf16 v[96:111], v[36:39], v[52:55], v[96:111]\n"
        "buffer_load_dwordx4 v11, s[68:71], s63 offen lds\n"
        "buffer_load_dwordx4 v11, s[68:71], s63 offen offset:1024 lds\n"
        "v_exp_f32 v86, v86\n"
        "v_cvt_pk_bf16_f32 v50, v84, v85\n"
        "v_exp_f32 v87, v87\n"
        "v_exp_f32 v88, v88\n"
        "v_cvt_pk_bf16_f32 v51, v86, v87\n"
        "v_mfma_f32_32x32x16_bf16 v[160:175], v[20:23], v[28:31], v[160:175]\n"
        "v_exp_f32 v89, v89\n"
        "v_exp_f32 v90, v90\n"
        "v_mfma_f32_32x32x16_bf16 v[112:127], v[40:43], v[48:51], v[112:127]\n"
        "s_mov_b64 exec, s[60:61]\n"
        "ds_read_b128 v[40:43], v9 offset:2048\n"
        "ds_read_b128 v[32:35], v9 offset:2560\n"
        "s_mov_b64 exec, -1\n"
        "v_exp_f32 v91, v91\n"
        "v_cvt_pk_bf16_f32 v52, v88, v89\n"
        "v_exp_f32 v92, v92\n"
        "v_exp_f32 v93, v93\n"
        "v_cvt_pk_bf16_f32 v53, v90, v91\n"
        "v_mfma_f32_32x32x16_bf16 v[176:191], v[16:19], v[28:31], v[176:191]\n"
        "v_exp_f32 v94, v94\n"
        "v_exp_f32 v95, v95\n"
        "v_cvt_pk_bf16_f32 v54, v92, v93\n"
        "v_cvt_pk_bf16_f32 v55, v94, v95\n"
        "buffer_load_dwordx4 v[28:31], v15, s[64:67], s78 offen sc1\n"
        "s_add_u32 s78, s78, 0x400\n"
        "s_cmp_eq_u32 s78, s90\n"
        "s_cselect_b32 s78, s91, s78\n"
        "s_lshl_b32 s63, s78, 4\n"
        "s_add_u32 s63, s63, s47\n"
        "s_waitcnt vmcnt(3)\n"
        "v_mfma_f32_32x32x16_bf16 v[80:95], v[44:47], v[4:7], v[144:159]\n"
        "ds_read_b128 v[44:47], v8 offset:4096\n"
        "ds_read_b64_tr_b16 v[20:21], v12 offset:2048\n"
        "ds_read_b64_tr_b16 v[22:23], v12 offset:2560\n"
        "v_exp_f32 v64, v64\n"
        "v_exp_f32 v65, v65\n"
        "v_exp_f32 v66, v66\n"
        "v_cvt_pk_bf16_f32 v48, v64, v65\n"
        "v_exp_f32 v67, v67\n"
        "v_exp_f32 v68, v68\n"
        "v_cvt_pk_bf16_f32 v49, v66, v67\n"
        "v_exp_f32 v69, v69\n"
        "v_mfma_f32_32x32x16_bf16 v[112:127], v[36:39], v[52:55], v[112:127]\n"
        "ds_read_b64_tr_b16 v[16:17], v12 offset:2112\n"
        "ds_read_b64_tr_b16 v[18:19], v12 offset:2624\n"
        "v_exp_f32 v70, v70\n"
        "v_cvt_pk_bf16_f32 v50, v68, v69\n"
        "v_exp_f32 v71, v71\n"
        "v_exp_f32 v72, v72\n"
        "v_cvt_pk_bf16_f32 v51, v70, v71\n"
        "v_exp_f32 v73, v73\n"
        "v_exp_f32 v74, v74\n"
        "s_waitcnt lgkmcnt(0)\n"
        "v_mfma_f32_32x32x16_bf16 v[96:111], v[40:43], v[48:51], v[96:111]\n"
        "v_exp_f32 v75, v75\n"
        "v_cvt_pk_bf16_f32 v52, v72, v73\n"
        "v_exp_f32 v76, v76\n"
        "v_exp_f32 v77, v77\n"
        "v_cvt_pk_bf16_f32 v53, v74, v75\n"
        "v_exp_f32 v78, v78\n"
        "v_exp_f32 v79, v79\n"
        "v_cvt_pk_bf16_f32 v54, v76, v77\n"
        "v_cvt_pk_bf16_f32 v55, v78, v79\n"
        "v_mfma_f32_32x32x16_bf16 v[64:79], v[44:47], v[0:3], v[128:143]\n"
        "v_exp_f32 v80, v80\n"
        "v_exp_f32 v81, v81\n"
        "v_exp_f32 v82, v82\n"
        "v_cvt_pk_bf16_f32 v48, v80, v81\n"
        "v_exp_f32 v83, v83\n"
        "v_exp_f32 v84, v84\n"
        "v_cvt_pk_bf16_f32 v49, v82, v83\n"
        "v_exp_f32 v85, v85\n"
        "s_add_u32 m0, s62, 2048\n"
        "v_mfma_f32_32x32x16_bf16 v[96:111], v[32:35], v[52:55], v[96:111]\n"
        "buffer_load_dwordx4 v11, s[68:71], s63 offen lds\n"
        "buffer_load_dwordx4 v11, s[68:71], s63 offen offset:1024 lds\n"
        "v_exp_f32 v86, v86\n"
        "v_cvt_pk_bf16_f32 v50, v84, v85\n"
        "v_exp_f32 v87, v87\n"
        "v_exp_f32 v88, v88\n"
        "v_cvt_pk_bf16_f32 v51, v86, v87\n"
        "v_mfma_f32_32x32x16_bf16 v[160:175], v[20:23], v[24:27], v[160:175]\n"
        "v_exp_f32 v89, v89\n"
        "v_exp_f32 v90, v90\n"
        "v_mfma_f32_32x32x16_bf16 v[112:127], v[40:43], v[48:51], v[112:127]\n"
        "s_mov_b64 exec, s[60:61]\n"
        "ds_read_b128 v[40:43], v9 offset:4096\n"
        "ds_read_b128 v[36:39], v9 offset:4608\n"
        "s_mov_b64 exec, -1\n"
        "v_exp_f32 v91, v91\n"
        "v_cvt_pk_bf16_f32 v52, v88, v89\n"
        "v_exp_f32 v92, v92\n"
        "v_exp_f32 v93, v93\n"
        "v_cvt_pk_bf16_f32 v53, v90, v91\n"
        "v_mfma_f32_32x32x16_bf16 v[176:191], v[16:19], v[24:27], v[176:191]\n"
        "v_exp_f32 v94, v94\n"
        "v_exp_f32 v95, v95\n"
        "v_cvt_pk_bf16_f32 v54, v92, v93\n"
        "v_cvt_pk_bf16_f32 v55, v94, v95\n"
        "buffer_load_dwordx4 v[24:27], v15, s[64:67], s78 offen sc1\n"
        "s_add_u32 s78, s78, 0x400\n"
        "s_cmp_eq_u32 s78, s90\n"
        "s_cselect_b32 s78, s91, s78\n"
        "s_lshl_b32 s63, s78, 4\n"
        "s_add_u32 s63, s63, s47\n"
        "v_add_u32 v8, 4096, v8\n"
        "v_add_u32 v9, 4096, v9\n"
        "s_add_u32 s80, s80, 1\n"
        "s_cmp_lt_u32 s80, 15\n"
        "s_cbranch_scc1 .Ldown_%=\n"
        "s_bitcmp0_b32 s85, s80\n"
        "s_cbranch_scc1 .Lpdc_%=\n"
        "s_sub_u32 s86, s86, 1\n"
        "s_cmp_eq_u32 s86, 2\n"
        "s_cbranch_scc1 .Lp2c_%=\n"
        "s_cmp_eq_u32 s86, 1\n"
        "s_cbranch_scc1 .Lp1c_%=\n"
        "s_setprio 0\n"
        "s_branch .Lpdc_%=\n"
        ".Lp2c_%=:\n"
        "s_setprio 2\n"
        "s_branch .Lpdc_%=\n"
        ".Lp1c_%=:\n"
        "s_setprio 1\n"
        ".Lpdc_%=:\n"
        "s_waitcnt vmcnt(3)\n"
        "v_mfma_f32_32x32x16_bf16 v[80:95], v[44:47], v[4:7], v[144:159]\n"
        "ds_read_b128 v[44:47], v8 offset:2048\n"
        "ds_read_b64_tr_b16 v[20:21], v12 offset:0\n"
        "ds_read_b64_tr_b16 v[22:23], v12 offset:512\n"
        "v_exp_f32 v64, v64\n"
        "v_exp_f32 v65, v65\n"
        "v_exp_f32 v66, v66\n"
        "v_cvt_pk_bf16_f32 v48, v64, v65\n"
        "v_exp_f32 v67, v67\n"
        "v_exp_f32 v68, v68\n"
        "v_cvt_pk_bf16_f32 v49, v66, v67\n"
        "v_exp_f32 v69, v69\n"
        "v_mfma_f32_32x32x16_bf16 v[112:127], v[32:35], v[52:55], v[112:127]\n"
        "ds_read_b64_tr_b16 v[16:17], v12 offset:64\n"
        "ds_read_b64_tr_b16 v[18:19], v12 offset:576\n"
        "v_exp_f32 v70, v70\n"
        "v_cvt_pk_bf16_f32 v50, v68, v69\n"
        "v_exp_f32 v71, v71\n"
        "v_exp_f32 v72, v72\n"
        "v_cvt_pk_bf16_f32 v51, v70, v71\n"
        "v_exp_f32 v73, v73\n"
        "v_exp_f32 v74, v74\n"
        "s_waitcnt lgkmcnt(0)\n"
        "v_mfma_f32_32x32x16_bf16 v[96:111], v[40:43], v[48:51], v[96:111]\n"
        "v_exp_f32 v75, v75\n"
        "v_cvt_pk_bf16_f32 v52, v72, v73\n"
        "v_exp_f32 v76, v76\n"
        "v_exp_f32 v77, v77\n"
        "v_cvt_pk_bf16_f32 v53, v74, v75\n"
        "v_exp_f32 v78, v78\n"
        "v_exp_f32 v79, v79\n"
        "v_cvt_pk_bf16_f32 v54, v76, v77\n"
        "v_cvt_pk_bf16_f32 v55, v78, v79\n"
        "v_mfma_f32_32x32x16_bf16 v[64:79], v[44:47], v[0:3], v[128:143]\n"
        "v_exp_f32 v80, v80\n"
        "v_exp_f32 v81, v81\n"
        "v_exp_f32 v82, v82\n"
        "v_cvt_pk_bf16_f32 v48, v80, v81\n"
        "v_exp_f32 v83, v83\n"
        "v_exp_f32 v84, v84\n"
        "v_cvt_pk_bf16_f32 v49, v82, v83\n"
        "v_exp_f32 v85, v85\n"
        "v_mfma_f32_32x32x16_bf16 v[96:111], v[36:39], v[52:55], v[96:111]\n"
        "v_exp_f32 v86, v86\n"
        "v_cvt_pk_bf16_f32 v50, v84, v85\n"
        "v_exp_f32 v87, v87\n"
        "v_exp_f32 v88, v88\n"
        "v_cvt_pk_bf16_f32 v51, v86, v87\n"
        "v_mfma_f32_32x32x16_bf16 v[160:175], v[20:23], v[28:31], v[160:175]\n"
        "v_exp_f32 v89, v89\n"
        "v_exp_f32 v90, v90\n"
        "v_mfma_f32_32x32x16_bf16 v[112:127], v[40:43], v[48:51], v[112:127]\n"
        "s_mov_b64 exec, s[60:61]\n"
        "ds_read_b128 v[40:43], v9 offset:2048\n"
        "ds_read_b128 v[32:35], v9 offset:2560\n"
        "s_mov_b64 exec, -1\n"
        "v_exp_f32 v91, v91\n"
        "v_cvt_pk_bf16_f32 v52, v88, v89\n"
        "v_exp_f32 v92, v92\n"
        "v_exp_f32 v93, v93\n"
        "v_cvt_pk_bf16_f32 v53, v90, v91\n"
        "v_mfma_f32_32x32x16_bf16 v[176:191], v[16:19], v[28:31], v[176:191]\n"
        "v_exp_f32 v94, v94\n"
        "v_exp_f32 v95, v95\n"
        "v_cvt_pk_bf16_f32 v54, v92, v93\n"
        "v_cvt_pk_bf16_f32 v55, v94, v95\n"
        "s_waitcnt vmcnt(0)\n"
        "v_mfma_f32_32x32x16_bf16 v[80:95], v[44:47], v[4:7], v[144:159]\n"
        "ds_read_b64_tr_b16 v[20:21], v12 offset:2048\n"
        "ds_read_b64_tr_b16 v[22:23], v12 offset:2560\n"
        "v_exp_f32 v64, v64\n"
        "v_exp_f32 v65, v65\n"
        "v_exp_f32 v66, v66\n"
        "v_cvt_pk_bf16_f32 v48, v64, v65\n"
        "v_exp_f32 v67, v67\n"
        "v_exp_f32 v68, v68\n"
        "v_cvt_pk_bf16_f32 v49, v66, v67\n"
        "v_exp_f32 v69, v69\n"
        "v_mfma_f32_32x32x16_bf16 v[112:127], v[36:39], v[52:55], v[112:127]\n"
        "ds_read_b64_tr_b16 v[16:17], v12 offset:2112\n"
        "ds_read_b64_tr_b16 v[18:19], v12 offset:2624\n"
        "v_exp_f32 v70, v70\n"
        "v_cvt_pk_bf16_f32 v50, v68, v69\n"
        "v_exp_f32 v71, v71\n"
        "v_exp_f32 v72, v72\n"
        "v_cvt_pk_bf16_f32 v51, v70, v71\n"
        "v_exp_f32 v73, v73\n"
        "v_exp_f32 v74, v74\n"
        "s_waitcnt lgkmcnt(0)\n"
        "v_mfma_f32_32x32x16_bf16 v[96:111], v[40:43], v[48:51], v[96:111]\n"
        "v_exp_f32 v75, v75\n"
        "v_cvt_pk_bf16_f32 v52, v72, v73\n"
        "v_exp_f32 v76, v76\n"
        "v_exp_f32 v77, v77\n"
        "v_cvt_pk_bf16_f32 v53, v74, v75\n"
        "v_exp_f32 v78, v78\n"
        "v_exp_f32 v79, v79\n"
        "v_cvt_pk_bf16_f32 v54, v76, v77\n"
        "v_cvt_pk_bf16_f32 v55, v78, v79\n"
        "v_exp_f32 v80, v80\n"
        "v_exp_f32 v81, v81\n"
        "v_exp_f32 v82, v82\n"
        "v_cvt_pk_bf16_f32 v48, v80, v81\n"
        "v_exp_f32 v83, v83\n"
        "v_exp_f32 v84, v84\n"
        "v_cvt_pk_bf16_f32 v49, v82, v83\n"
        "v_exp_f32 v85, v85\n"
        "v_mfma_f32_32x32x16_bf16 v[96:111], v[32:35], v[52:55], v[96:111]\n"
        "v_exp_f32 v86, v86\n"
        "v_cvt_pk_bf16_f32 v50, v84, v85\n"
        "v_exp_f32 v87, v87\n"
        "v_exp_f32 v88, v88\n"
        "v_cvt_pk_bf16_f32 v51, v86, v87\n"
        "v_mfma_f32_32x32x16_bf16 v[160:175], v[20:23], v[24:27], v[160:175]\n"
        "v_exp_f32 v89, v89\n"
        "v_exp_f32 v90, v90\n"
        "v_mfma_f32_32x32x16_bf16 v[112:127], v[40:43], v[48:51], v[112:127]\n"
        "v_exp_f32 v91, v91\n"
        "v_cvt_pk_bf16_f32 v52, v88, v89\n"
        "v_exp_f32 v92, v92\n"
        "v_exp_f32 v93, v93\n"
        "v_cvt_pk_bf16_f32 v53, v90, v91\n"
        "v_mfma_f32_32x32x16_bf16 v[176:191], v[16:19], v[24:27], v[176:191]\n"
        "v_exp_f32 v94, v94\n"
        "v_exp_f32 v95, v95\n"
        "v_cvt_pk_bf16_f32 v54, v92, v93\n"
        "v_cvt_pk_bf16_f32 v55, v94, v95\n"
        "s_nop 1\n"
        "v_mfma_f32_32x32x16_bf16 v[112:127], v[32:35], v[52:55], v[112:127]\n"
        "s_setprio 0\n"
        "v_add_u32 v8, s62, v15\n"
        "v_lshrrev_b32 v9, 2, v15\n"
        "v_add_u32 v9, s62, v9\n"
        "v_xor_b32 v10, 0x4000, v8\n"
        "v_xor_b32 v11, 0x4000, v9\n"
        "v_lshlrev_b32 v12, 8, v15\n"
        "v_and_b32 v12, 0x7000, v12\n"
        "v_mov_b32 v13, s62\n"
        "s_lshl_b32 s39, s46, 5\n"
        "s_lshl_b32 s56, s48, 3\n"
        "s_add_u32 s39, s39, s56\n"
        "s_lshl_b32 s39, s39, 10\n"
        "s_add_u32 s88, s39, 11993088\n"
        "s_add_u32 s39, s97, 0\n"
        "s_and_b32 s39, s39, 3\n"
        "s_lshl_b32 s39, s39, 11\n"
        "s_add_u32 s84, s88, s39\n"
        "s_add_u32 s39, s97, 1\n"
        "s_and_b32 s39, s39, 3\n"
        "s_lshl_b32 s39, s39, 11\n"
        "s_add_u32 s85, s88, s39\n"
        "s_add_u32 s39, s97, 2\n"
        "s_and_b32 s39, s39, 3\n"
        "s_lshl_b32 s39, s39, 11\n"
        "s_add_u32 s86, s88, s39\n"
        "s_add_u32 s39, s97, 3\n"
        "s_and_b32 s39, s39, 3\n"
        "s_lshl_b32 s39, s39, 11\n"
        "s_add_u32 s87, s88, s39\n"
        "s_mov_b32 s39, 0x7e800000\n"
        "v_cmpx_gt_f32_e64 vcc, s39, |v96|\n"
        "v_cmpx_gt_f32_e64 vcc, s39, |v97|\n"
        "v_cmpx_gt_f32_e64 vcc, s39, |v98|\n"
        "v_cmpx_gt_f32_e64 vcc, s39, |v99|\n"
        "v_cmpx_gt_f32_e64 vcc, s39, |v100|\n"
        "v_cmpx_gt_f32_e64 vcc, s39, |v101|\n"
        "v_cmpx_gt_f32_e64 vcc, s39, |v102|\n"
        "v_cmpx_gt_f32_e64 vcc, s39, |v103|\n"
        "v_cmpx_gt_f32_e64 vcc, s39, |v104|\n"
        "v_cmpx_gt_f32_e64 vcc, s39, |v112|\n"
        "v_cmpx_gt_f32_e64 vcc, s39, |v113|\n"
        "v_cmpx_gt_f32_e64 vcc, s39, |v114|\n"
        "v_cmpx_gt_f32_e64 vcc, s39, |v115|\n"
        "v_cmpx_gt_f32_e64 vcc, s39, |v116|\n"
        "v_cmpx_gt_f32_e64 vcc, s39, |v117|\n"
        "v_cmpx_gt_f32_e64 vcc, s39, |v118|\n"
        "v_cmpx_gt_f32_e64 vcc, s39, |v119|\n"
        "v_cmpx_gt_f32_e64 vcc, s39, |v120|\n"
        "s_nop 4\n"
        "s_cmp_lg_u64 exec, -1\n"
        "s_cselect_b32 s39, 1, 0\n"
        "s_mov_b64 exec, -1\n"
        "v_mov_b32 v16, s39\n"
        "ds_write_b32 v13, v16 offset:3072\n"
        "s_cmp_lg_u32 s22, 0\n"
        "s_cbranch_scc1 .Lx1_%=\n"
        "ds_write_b128 v8, v[112:115]\n"
        "ds_write_b128 v8, v[116:119] offset:1024\n"
        "ds_write_b32 v9, v120 offset:2048\n"
        "s_waitcnt lgkmcnt(0)\n"
        "s_barrier\n"
        "ds_read_b128 v[64:67], v10\n"
        "ds_read_b128 v[68:71], v10 offset:1024\n"
        "ds_read_b32 v72, v11 offset:2048\n"
        "ds_read_b32 v16, v12 offset:3072\n"
        "s_waitcnt lgkmcnt(0)\n"
        "v_add_f32 v96, v96, v64\n"
        "v_add_f32 v97, v97, v65\n"
        "v_add_f32 v98, v98, v66\n"
        "v_add_f32 v99, v99, v67\n"
        "v_add_f32 v100, v100, v68\n"
        "v_add_f32 v101, v101, v69\n"
        "v_add_f32 v102, v102, v70\n"
        "v_add_f32 v103, v103, v71\n"
        "v_add_f32 v104, v104, v72\n"
        "s_branch .Lx2_%=\n"
        ".Lx1_%=:\n"
        "ds_write_b128 v8, v[96:99]\n"
        "ds_write_b128 v8, v[100:103] offset:1024\n"
        "ds_write_b32 v9, v104 offset:2048\n"
        "s_waitcnt lgkmcnt(0)\n"
        "s_barrier\n"
        "ds_read_b128 v[64:67], v10\n"
        "ds_read_b128 v[68:71], v10 offset:1024\n"
        "ds_read_b32 v72, v11 offset:2048\n"
        "ds_read_b32 v16, v12 offset:3072\n"
        "s_waitcnt lgkmcnt(0)\n"
        "v_add_f32 v96, v112, v64\n"
        "v_add_f32 v97, v113, v65\n"
        "v_add_f32 v98, v114, v66\n"
        "v_add_f32 v99, v115, v67\n"
        "v_add_f32 v100, v116, v68\n"
        "v_add_f32 v101, v117, v69\n"
        "v_add_f32 v102, v118, v70\n"
        "v_add_f32 v103, v119, v71\n"
        "v_add_f32 v104, v120, v72\n"
        "v_mov_b32 v0, v4\n"
        "v_mov_b32 v1, v5\n"
        "v_mov_b32 v2, v6\n"
        "v_mov_b32 v3, v7\n"
        ".Lx2_%=:\n"
        "v_cmp_ne_u32 vcc, 0, v16\n"
        "s_cbranch_vccnz .Lredo_%=\n"
        ".Lback_%=:\n"
        "v_rcp_f32 v16, v104\n"
        "s_nop 0\n"
        "v_pk_mul_f32 v[96:97], v[96:97], v[16:17] op_sel_hi:[1,0]\n"
        "v_pk_mul_f32 v[98:99], v[98:99], v[16:17] op_sel_hi:[1,0]\n"
        "v_pk_mul_f32 v[100:101], v[100:101], v[16:17] op_sel_hi:[1,0]\n"
        "v_pk_mul_f32 v[102:103], v[102:103], v[16:17] op_sel_hi:[1,0]\n"
        "v_cvt_pk_bf16_f32 v20, v96, v97\n"
        "v_cvt_pk_bf16_f32 v21, v98, v99\n"
        "v_cvt_pk_bf16_f32 v22, v100, v101\n"
        "v_cvt_pk_bf16_f32 v23, v102, v103\n"
        "buffer_store_dwordx4 v[20:23], v15, s[32:35], s3 offen sc1\n"
        "v_lshrrev_b32 v24, 4, v15\n"
        "v_and_b32 v25, 31, v24\n"
        "v_lshrrev_b32 v26, 5, v24\n"
        "v_xor_b32 v26, v26, v25\n"
        "v_and_b32 v26, 7, v26\n"
        "v_lshlrev_b32 v26, 4, v26\n"
        "v_lshl_or_b32 v26, v25, 8, v26\n"
        "s_lshl_b32 s39, s20, 13\n"
        "s_add_u32 s39, s39, 32768\n"
        "v_add_u32 v28, s39, v26\n"
        "v_xor_b32 v29, 32, v28\n"
        "v_xor_b32 v30, 64, v28\n"
        "v_xor_b32 v31, 0x60, v28\n"
        "ds_write_b128 v28, v[160:163] offset:0\n"
        "ds_write_b128 v29, v[164:167] offset:0\n"
        "ds_write_b128 v30, v[168:171] offset:0\n"
        "ds_write_b128 v31, v[172:175] offset:0\n"
        "ds_write_b128 v28, v[176:179] offset:128\n"
        "ds_write_b128 v29, v[180:183] offset:128\n"
        "ds_write_b128 v30, v[184:187] offset:128\n"
        "ds_write_b128 v31, v[188:191] offset:128\n"
        "v_lshrrev_b32 v25, 3, v24\n"
        "v_and_b32 v26, 7, v24\n"
        "v_lshlrev_b32 v26, 1, v26\n"
        "v_xor_b32 v26, v26, v25\n"
        "v_lshlrev_b32 v26, 4, v26\n"
        "v_lshl_or_b32 v26, v25, 8, v26\n"
        "s_lshr_b32 s39, s20, 2\n"
        "s_lshl_b32 s39, s39, 13\n"
        "s_and_b32 s56, s20, 3\n"
        "s_lshl_b32 s56, s56, 11\n"
        "s_add_u32 s39, s39, s56\n"
        "s_add_u32 s39, s39, 32768\n"
        "v_add_u32 v32, s39, v26\n"
        "v_xor_b32 v33, 16, v32\n"
        "v_add_u32 v28, 65536, v28\n"
        "v_add_u32 v29, 65536, v29\n"
        "v_add_u32 v30, 65536, v30\n"
        "v_add_u32 v31, 65536, v31\n"
        "v_lshlrev_b32 v34, 6, v15\n"
        "s_waitcnt lgkmcnt(0)\n"
        "s_barrier\n"
        "s_or_b32 s39, s2, s20\n"
        "s_cmp_lg_u32 s39, 0\n"
        "s_cbranch_scc1 .Lne_%=\n"
        "v_add_u32 v36, 4096, v15\n"
        "global_load_dwordx4 v[120:123], v36, s[18:19] sc1\n"
        "global_load_dwordx4 v[124:127], v36, s[18:19] offset:1024 sc1\n"
        ".Lne_%=:\n"
        "buffer_load_dwordx4 v[64:67], v15, s[32:35], s84 offen offset:0 sc1\n"
        "buffer_load_dwordx4 v[68:71], v15, s[32:35], s84 offen offset:1024 sc1\n"
        "buffer_load_dwordx4 v[72:75], v15, s[32:35], s85 offen offset:0 sc1\n"
        "buffer_load_dwordx4 v[76:79], v15, s[32:35], s85 offen offset:1024 sc1\n"
        "buffer_load_dwordx4 v[80:83], v15, s[32:35], s86 offen offset:0 sc1\n"
        "buffer_load_dwordx4 v[84:87], v15, s[32:35], s86 offen offset:1024 sc1\n"
        "buffer_load_dwordx4 v[88:91], v15, s[32:35], s87 offen offset:0 sc1\n"
        "buffer_load_dwordx4 v[92:95], v15, s[32:35], s87 offen offset:1024 sc1\n"
        "ds_read_b128 v[40:43], v32 offset:0\n"
        "ds_read_b128 v[44:47], v33 offset:0\n"
        "ds_read_b128 v[96:99], v32 offset:16384\n"
        "ds_read_b128 v[100:103], v33 offset:16384\n"
        "ds_read_b128 v[104:107], v32 offset:32768\n"
        "ds_read_b128 v[108:111], v33 offset:32768\n"
        "ds_read_b128 v[112:115], v32 offset:49152\n"
        "ds_read_b128 v[116:119], v33 offset:49152\n"
        "s_waitcnt lgkmcnt(5)\n"
        "v_pk_add_f32 v[40:41], v[40:41], v[96:97]\n"
        "v_pk_add_f32 v[42:43], v[42:43], v[98:99]\n"
        "s_waitcnt lgkmcnt(4)\n"
        "v_pk_add_f32 v[44:45], v[44:45], v[100:101]\n"
        "v_pk_add_f32 v[46:47], v[46:47], v[102:103]\n"
        "s_waitcnt lgkmcnt(3)\n"
        "v_pk_add_f32 v[40:41], v[40:41], v[104:105]\n"
        "v_pk_add_f32 v[42:43], v[42:43], v[106:107]\n"
        "s_waitcnt lgkmcnt(2)\n"
        "v_pk_add_f32 v[44:45], v[44:45], v[108:109]\n"
        "v_pk_add_f32 v[46:47], v[46:47], v[110:111]\n"
        "s_waitcnt lgkmcnt(1)\n"
        "v_pk_add_f32 v[40:41], v[40:41], v[112:113]\n"
        "v_pk_add_f32 v[42:43], v[42:43], v[114:115]\n"
        "s_waitcnt lgkmcnt(0)\n"
        "v_pk_add_f32 v[44:45], v[44:45], v[116:117]\n"
        "v_pk_add_f32 v[46:47], v[46:47], v[118:119]\n"
        "v_mul_f32 v48, 0xbfb8aa3b, v40\n"
        "v_mul_f32 v49, 0xbfb8aa3b, v41\n"
        "v_mul_f32 v50, 0xbfb8aa3b, v42\n"
        "v_mul_f32 v51, 0xbfb8aa3b, v43\n"
        "v_mul_f32 v52, 0xbfb8aa3b, v44\n"
        "v_mul_f32 v53, 0xbfb8aa3b, v45\n"
        "v_mul_f32 v54, 0xbfb8aa3b, v46\n"
        "v_mul_f32 v55, 0xbfb8aa3b, v47\n"
        "v_exp_f32 v48, v48\n"
        "v_exp_f32 v49, v49\n"
        "v_exp_f32 v50, v50\n"
        "v_exp_f32 v51, v51\n"
        "v_exp_f32 v52, v52\n"
        "v_exp_f32 v53, v53\n"
        "v_exp_f32 v54, v54\n"
        "v_exp_f32 v55, v55\n"
        "v_add_f32 v48, 1.0, v48\n"
        "v_add_f32 v49, 1.0, v49\n"
        "v_add_f32 v50, 1.0, v50\n"
        "v_add_f32 v51, 1.0, v51\n"
        "v_add_f32 v52, 1.0, v52\n"
        "v_add_f32 v53, 1.0, v53\n"
        "v_add_f32 v54, 1.0, v54\n"
        "v_add_f32 v55, 1.0, v55\n"
        "v_rcp_f32 v48, v48\n"
        "v_rcp_f32 v49, v49\n"
        "v_rcp_f32 v50, v50\n"
        "v_rcp_f32 v51, v51\n"
        "v_rcp_f32 v52, v52\n"
        "v_rcp_f32 v53, v53\n"
        "v_rcp_f32 v54, v54\n"
        "v_rcp_f32 v55, v55\n"
        "v_pk_mul_f32 v[40:41], v[40:41], v[48:49]\n"
        "v_pk_mul_f32 v[42:43], v[42:43], v[50:51]\n"
        "v_pk_mul_f32 v[44:45], v[44:45], v[52:53]\n"
        "v_pk_mul_f32 v[46:47], v[46:47], v[54:55]\n"
        "v_add_u32 v32, 65536, v32\n"
        "v_add_u32 v33, 65536, v33\n"
        "v_lshlrev_b32 v48, 16, v60\n"
        "v_lshlrev_b32 v50, 16, v61\n"
        "v_lshlrev_b32 v52, 16, v62\n"
        "v_lshlrev_b32 v54, 16, v63\n"
        "v_and_b32 v49, 0xffff0000, v60\n"
        "v_and_b32 v51, 0xffff0000, v61\n"
        "v_and_b32 v53, 0xffff0000, v62\n"
        "v_and_b32 v55, 0xffff0000, v63\n"
        "s_add_u32 s82, s16, s53\n"
        "s_addc_u32 s83, s17, 0\n"
        "s_mov_b32 s81, 0x20000\n"
        ".Lwo_%=:\n"
        "s_waitcnt vmcnt(7)\n"
        "v_mfma_f32_32x32x16_bf16 v[160:175], v[192:195], v[64:67], 0\n"
        "v_max_u32 v35, v64, v65\n"
        "v_mfma_f32_32x32x16_bf16 v[176:191], v[196:199], v[64:67], 0\n"
        "v_max3_u32 v35, v35, v66, v67\n"
        "s_waitcnt vmcnt(6)\n"
        "v_mfma_f32_32x32x16_bf16 v[160:175], v[200:203], v[68:71], v[160:175]\n"
        "v_max3_u32 v35, v35, v68, v69\n"
        "v_mfma_f32_32x32x16_bf16 v[176:191], v[204:207], v[68:71], v[176:191]\n"
        "v_max3_u32 v35, v35, v70, v71\n"
        "s_waitcnt vmcnt(5)\n"
        "v_mfma_f32_32x32x16_bf16 v[160:175], v[208:211], v[72:75], v[160:175]\n"
        "v_max3_u32 v35, v35, v72, v73\n"
        "v_mfma_f32_32x32x16_bf16 v[176:191], v[212:215], v[72:75], v[176:191]\n"
        "v_max3_u32 v35, v35, v74, v75\n"
        "s_waitcnt vmcnt(4)\n"
        "v_mfma_f32_32x32x16_bf16 v[160:175], v[216:219], v[76:79], v[160:175]\n"
        "v_max3_u32 v35, v35, v76, v77\n"
        "v_mfma_f32_32x32x16_bf16 v[176:191], v[220:223], v[76:79], v[176:191]\n"
        "v_max3_u32 v35, v35, v78, v79\n"
        "s_waitcnt vmcnt(3)\n"
        "v_mfma_f32_32x32x16_bf16 v[160:175], v[224:227], v[80:83], v[160:175]\n"
        "v_max3_u32 v35, v35, v80, v81\n"
        "v_mfma_f32_32x32x16_bf16 v[176:191], v[228:231], v[80:83], v[176:191]\n"
        "v_max3_u32 v35, v35, v82, v83\n"
        "s_waitcnt vmcnt(2)\n"
        "v_mfma_f32_32x32x16_bf16 v[160:175], v[232:235], v[84:87], v[160:175]\n"
        "v_max3_u32 v35, v35, v84, v85\n"
        "v_mfma_f32_32x32x16_bf16 v[176:191], v[236:239], v[84:87], v[176:191]\n"
        "v_max3_u32 v35, v35, v86, v87\n"
        "s_waitcnt vmcnt(1)\n"
        "v_mfma_f32_32x32x16_bf16 v[160:175], v[240:243], v[88:91], v[160:175]\n"
        "v_max3_u32 v35, v35, v88, v89\n"
        "v_mfma_f32_32x32x16_bf16 v[176:191], v[244:247], v[88:91], v[176:191]\n"
        "v_max3_u32 v35, v35, v90, v91\n"
        "s_waitcnt vmcnt(0)\n"
        "v_mfma_f32_32x32x16_bf16 v[160:175], v[248:251], v[92:95], v[160:175]\n"
        "v_max3_u32 v35, v35, v92, v93\n"
        "v_mfma_f32_32x32x16_bf16 v[176:191], v[252:255], v[92:95], v[176:191]\n"
        "v_max3_u32 v35, v35, v94, v95\n"
        "v_cmp_eq_u32 vcc, -1, v35\n"
        "s_cbranch_vccnz .Llate_%=\n"
        ".Lwo2_%=:\n"
        "s_nop 6\n"
        "ds_write_b128 v28, v[160:163] offset:0\n"
        "ds_write_b128 v29, v[164:167] offset:0\n"
        "ds_write_b128 v30, v[168:171] offset:0\n"
        "ds_write_b128 v31, v[172:175] offset:0\n"
        "ds_write_b128 v28, v[176:179] offset:128\n"
        "ds_write_b128 v29, v[180:183] offset:128\n"
        "ds_write_b128 v30, v[184:187] offset:128\n"
        "ds_write_b128 v31, v[188:191] offset:128\n"
        "s_waitcnt lgkmcnt(0)\n"
        "s_barrier\n"
        "ds_read_b128 v[16:19], v32 offset:0\n"
        "ds_read_b128 v[20:23], v33 offset:0\n"
        "ds_read_b128 v[96:99], v32 offset:16384\n"
        "ds_read_b128 v[100:103], v33 offset:16384\n"
        "ds_read_b128 v[104:107], v32 offset:32768\n"
        "ds_read_b128 v[108:111], v33 offset:32768\n"
        "ds_read_b128 v[112:115], v32 offset:49152\n"
        "ds_read_b128 v[116:119], v33 offset:49152\n"
        "s_waitcnt lgkmcnt(5)\n"
        "v_pk_add_f32 v[16:17], v[16:17], v[96:97]\n"
        "v_pk_add_f32 v[18:19], v[18:19], v[98:99]\n"
        "s_waitcnt lgkmcnt(4)\n"
        "v_pk_add_f32 v[20:21], v[20:21], v[100:101]\n"
        "v_pk_add_f32 v[22:23], v[22:23], v[102:103]\n"
        "s_waitcnt lgkmcnt(3)\n"
        "v_pk_add_f32 v[16:17], v[16:17], v[104:105]\n"
        "v_pk_add_f32 v[18:19], v[18:19], v[106:107]\n"
        "s_waitcnt lgkmcnt(2)\n"
        "v_pk_add_f32 v[20:21], v[20:21], v[108:109]\n"
        "v_pk_add_f32 v[22:23], v[22:23], v[110:111]\n"
        "s_waitcnt lgkmcnt(1)\n"
        "v_pk_add_f32 v[16:17], v[16:17], v[112:113]\n"
        "v_pk_add_f32 v[18:19], v[18:19], v[114:115]\n"
        "s_waitcnt lgkmcnt(0)\n"
        "v_pk_add_f32 v[20:21], v[20:21], v[116:117]\n"
        "v_pk_add_f32 v[22:23], v[22:23], v[118:119]\n"
        "v_pk_fma_f32 v[16:17], v[16:17], v[40:41], v[48:49]\n"
        "v_pk_fma_f32 v[18:19], v[18:19], v[42:43], v[50:51]\n"
        "v_pk_fma_f32 v[20:21], v[20:21], v[44:45], v[52:53]\n"
        "v_pk_fma_f32 v[22:23], v[22:23], v[46:47], v[54:55]\n"
        "v_cvt_pk_bf16_f32 v16, v16, v17\n"
        "v_cvt_pk_bf16_f32 v17, v18, v19\n"
        "v_cvt_pk_bf16_f32 v18, v20, v21\n"
        "v_cvt_pk_bf16_f32 v19, v22, v23\n"
        "global_store_dwordx4 v59, v[16:19], s[82:83]\n"
        "s_or_b32 s39, s2, s20\n"
        "s_cmp_lg_u32 s39, 0\n"
        "s_cbranch_scc1 .Lfin_%=\n"
        "v_cmp_ne_u64 vcc, s[30:31], v[120:121]\n"
        "v_cmp_ne_u64 s[56:57], s[30:31], v[122:123]\n"
        "s_nop 0\n"
        "s_or_b64 vcc, vcc, s[56:57]\n"
        "v_cmp_ne_u64 s[56:57], s[30:31], v[124:125]\n"
        "v_cmp_ne_u64 s[58:59], s[30:31], v[126:127]\n"
        "s_or_b64 vcc, vcc, s[56:57]\n"
        "s_or_b64 vcc, vcc, s[58:59]\n"
        "s_cbranch_vccz .Lep_%=\n"
        "s_mov_b32 s81, 0x20000\n"
        ".Lpe0a_%=:\n"
        "global_load_dwordx2 v[56:57], v36, s[18:19] sc1\n"
        "s_waitcnt vmcnt(0)\n"
        "v_cmp_ne_u64 vcc, s[30:31], v[56:57]\n"
        "s_cbranch_vccz .Lqe0a_%=\n"
        "s_sleep 6\n"
        "s_sub_u32 s81, s81, 1\n"
        "s_cbranch_scc0 .Lpe0a_%=\n"
        ".Lqe0a_%=:\n"
        "s_mov_b32 s81, 0x20000\n"
        ".Lpe0b_%=:\n"
        "global_load_dwordx2 v[56:57], v36, s[18:19] offset:8 sc1\n"
        "s_waitcnt vmcnt(0)\n"
        "v_cmp_ne_u64 vcc, s[30:31], v[56:57]\n"
        "s_cbranch_vccz .Lqe0b_%=\n"
        "s_sleep 6\n"
        "s_sub_u32 s81, s81, 1\n"
        "s_cbranch_scc0 .Lpe0b_%=\n"
        ".Lqe0b_%=:\n"
        "s_mov_b32 s81, 0x20000\n"
        ".Lpe1a_%=:\n"
        "global_load_dwordx2 v[56:57], v36, s[18:19] offset:1024 sc1\n"
        "s_waitcnt vmcnt(0)\n"
        "v_cmp_ne_u64 vcc, s[30:31], v[56:57]\n"
        "s_cbranch_vccz .Lqe1a_%=\n"
        "s_sleep 6\n"
        "s_sub_u32 s81, s81, 1\n"
        "s_cbranch_scc0 .Lpe1a_%=\n"
        ".Lqe1a_%=:\n"
        "s_mov_b32 s81, 0x20000\n"
        ".Lpe1b_%=:\n"
        "global_load_dwordx2 v[56:57], v36, s[18:19] offset:1032 sc1\n"
        "s_waitcnt vmcnt(0)\n"
        "v_cmp_ne_u64 vcc, s[30:31], v[56:57]\n"
        "s_cbranch_vccz .Lqe1b_%=\n"
        "s_sleep 6\n"
        "s_sub_u32 s81, s81, 1\n"
        "s_cbranch_scc0 .Lpe1b_%=\n"
        ".Lqe1b_%=:\n"
        ".Lep_%=:\n"
        "s_mov_b64 exec, 1\n"
        "v_mov_b32 v56, s30\n"
        "v_mov_b32 v57, s31\n"
        "global_store_dwordx2 v15, v[56:57], s[18:19] sc1\n"
        "s_mov_b64 exec, -1\n"
        ".Lfin_%=:\n"
        "s_endpgm\n"
        ".Llate_%=:\n"
        "s_sub_u32 s81, s81, 1\n"
        "s_cbranch_scc1 .Lwo2_%=\n"
        "s_mov_b64 exec, 0xff\n"
        "buffer_load_dword v37, v34, s[32:35], s88 offen offset:1020 sc1\n"
        "s_waitcnt vmcnt(0)\n"
        "v_cmp_eq_u32 vcc, -1, v37\n"
        "s_mov_b64 exec, -1\n"
        "s_cbranch_vccz .Llate2_%=\n"
        "s_sleep 2\n"
        "s_branch .Llate_%=\n"
        ".Llate2_%=:\n"
        "buffer_load_dwordx4 v[64:67], v15, s[32:35], s84 offen offset:0 sc1\n"
        "buffer_load_dwordx4 v[68:71], v15, s[32:35], s84 offen offset:1024 sc1\n"
        "buffer_load_dwordx4 v[72:75], v15, s[32:35], s85 offen offset:0 sc1\n"
        "buffer_load_dwordx4 v[76:79], v15, s[32:35], s85 offen offset:1024 sc1\n"
        "buffer_load_dwordx4 v[80:83], v15, s[32:35], s86 offen offset:0 sc1\n"
        "buffer_load_dwordx4 v[84:87], v15, s[32:35], s86 offen offset:1024 sc1\n"
        "buffer_load_dwordx4 v[88:91], v15, s[32:35], s87 offen offset:0 sc1\n"
        "buffer_load_dwordx4 v[92:95], v15, s[32:35], s87 offen offset:1024 sc1\n"
        "s_branch .Lwo_%=\n"
        ".Lredo_%=:\n"
        "s_and_b32 s39, s20, 3\n"
        "s_lshl_b32 s56, 0x11, s39\n"
        "s_and_b32 s56, s56, vcc_lo\n"
        "s_cmp_eq_u32 s56, 0\n"
        "s_cbranch_scc1 .Lredo2_%=\n"
        "v_add_u32 v8, 32768, v15\n"
        "v_lshrrev_b32 v16, 9, v15\n"
        "v_and_b32 v9, 0xf0, v15\n"
        "v_lshl_or_b32 v9, v16, 8, v9\n"
        "v_add_u32 v9, 33792, v9\n"
        "v_mov_b32 v16, 0xff800000\n"
        "v_mov_b32 v17, v8\n"
        "s_mov_b32 s80, 64\n"
        ".Lm_%=:\n"
        "ds_read_b128 v[44:47], v17\n"
        "v_add_u32 v17, 0x800, v17\n"
        "s_waitcnt lgkmcnt(0)\n"
        "v_mfma_f32_32x32x16_bf16 v[64:79], v[44:47], v[0:3], 0\n"
        "s_nop 11\n"
        "v_max3_f32 v16, v16, v64, v65\n"
        "v_max3_f32 v16, v16, v66, v67\n"
        "v_max3_f32 v16, v16, v68, v69\n"
        "v_max3_f32 v16, v16, v70, v71\n"
        "v_max3_f32 v16, v16, v72, v73\n"
        "v_max3_f32 v16, v16, v74, v75\n"
        "v_max3_f32 v16, v16, v76, v77\n"
        "v_max3_f32 v16, v16, v78, v79\n"
        "s_sub_u32 s80, s80, 1\n"
        "s_cmp_lg_u32 s80, 0\n"
        "s_cbranch_scc1 .Lm_%=\n"
        "v_mov_b32 v17, v16\n"
        "s_nop 1\n"
        "v_permlane32_swap_b32 v16, v17\n"
        "v_max_f32 v16, v16, v17\n"
        "v_xor_b32 v16, 0x80000000, v16\n"
        "v_mov_b32 v128, v16\n"
        "v_mov_b32 v129, v16\n"
        "v_mov_b32 v130, v16\n"
        "v_mov_b32 v131, v16\n"
        "v_mov_b32 v132, v16\n"
        "v_mov_b32 v133, v16\n"
        "v_mov_b32 v134, v16\n"
        "v_mov_b32 v135, v16\n"
        "v_mov_b32 v136, v16\n"
        "v_mov_b32 v137, v16\n"
        "v_mov_b32 v138, v16\n"
        "v_mov_b32 v139, v16\n"
        "v_mov_b32 v140, v16\n"
        "v_mov_b32 v141, v16\n"
        "v_mov_b32 v142, v16\n"
        "v_mov_b32 v143, v16\n"
        "v_mov_b32 v96, 0\n"
        "v_mov_b32 v97, 0\n"
        "v_mov_b32 v98, 0\n"
        "v_mov_b32 v99, 0\n"
        "v_mov_b32 v100, 0\n"
        "v_mov_b32 v101, 0\n"
        "v_mov_b32 v102, 0\n"
        "v_mov_b32 v103, 0\n"
        "v_mov_b32 v104, 0\n"
        "v_mov_b32 v105, 0\n"
        "v_mov_b32 v106, 0\n"
        "v_mov_b32 v107, 0\n"
        "v_mov_b32 v108, 0\n"
        "v_mov_b32 v109, 0\n"
        "v_mov_b32 v110, 0\n"
        "v_mov_b32 v111, 0\n"
        "s_mov_b32 s80, 64\n"
        ".Lf_%=:\n"
        "ds_read_b128 v[44:47], v8\n"
        "s_mov_b64 exec, s[60:61]\n"
        "ds_read_b128 v[40:43], v9\n"
        "ds_read_b128 v[36:39], v9 offset:512\n"
        "s_mov_b64 exec, -1\n"
        "v_add_u32 v8, 0x800, v8\n"
        "v_add_u32 v9, 0x800, v9\n"
        "s_waitcnt lgkmcnt(0)\n"
        "v_mfma_f32_32x32x16_bf16 v[64:79], v[44:47], v[0:3], v[128:143]\n"
        "s_nop 11\n"
        "v_exp_f32 v64, v64\n"
        "v_exp_f32 v65, v65\n"
        "v_exp_f32 v66, v66\n"
        "v_exp_f32 v67, v67\n"
        "v_exp_f32 v68, v68\n"
        "v_exp_f32 v69, v69\n"
        "v_exp_f32 v70, v70\n"
        "v_exp_f32 v71, v71\n"
        "v_exp_f32 v72, v72\n"
        "v_exp_f32 v73, v73\n"
        "v_exp_f32 v74, v74\n"
        "v_exp_f32 v75, v75\n"
        "v_exp_f32 v76, v76\n"
        "v_exp_f32 v77, v77\n"
        "v_exp_f32 v78, v78\n"
        "v_exp_f32 v79, v79\n"
        "v_cvt_pk_bf16_f32 v48, v64, v65\n"
        "v_cvt_pk_bf16_f32 v49, v66, v67\n"
        "v_cvt_pk_bf16_f32 v50, v68, v69\n"
        "v_cvt_pk_bf16_f32 v51, v70, v71\n"
        "v_cvt_pk_bf16_f32 v52, v72, v73\n"
        "v_cvt_pk_bf16_f32 v53, v74, v75\n"
        "v_cvt_pk_bf16_f32 v54, v76, v77\n"
        "v_cvt_pk_bf16_f32 v55, v78, v79\n"
        "v_mfma_f32_32x32x16_bf16 v[96:111], v[40:43], v[48:51], v[96:111]\n"
        "s_nop 0\n"
        "v_mfma_f32_32x32x16_bf16 v[96:111], v[36:39], v[52:55], v[96:111]\n"
        "s_sub_u32 s80, s80, 1\n"
        "s_cmp_lg_u32 s80, 0\n"
        "s_cbranch_scc1 .Lf_%=\n"
        "s_nop 7\n"
        "s_nop 7\n"
        ".Lredo2_%=:\n"
        "s_barrier\n"
        "s_branch .Lback_%=\n"
        ".Lend_%=:\n"
        :
        : "{s[0:1]}"(__builtin_amdgcn_kernarg_segment_ptr()), "{s2}"(blockIdx.x), "{v0}"(threadIdx.x)
        : "memory", "v255", "s101", "vcc");
}

extern "C" void kernel_launch(void* const* d_in, const int* in_sizes, int n_in, void* d_out, int out_size, void* d_ws, size_t ws_size,
                              hipStream_t stream) {
    (void)hipFuncSetAttribute((const void*)fused_block, hipFuncAttributeMaxDynamicSharedMemorySize, 163840);
    fused_block<<<256, 512, 163840, stream>>>(d_in[0], d_in[1], d_in[2], d_in[4], d_in[5], d_in[6], d_out, d_ws);
}
```
